```python
import torch

def _softmax(x, axis=-1):
    return torch.softmax(x, dim=axis)
def _gelu(x):
    return torch.nn.functional.gelu(x, approximate="tanh")
def _silu(x):
    return torch.nn.functional.silu(x)
def _sigmoid(x):
    return torch.sigmoid(x)
def _layernorm(x, axis=-1, eps=1e-5):
    m = x.mean(dim=axis, keepdim=True)
    v = x.var(dim=axis, keepdim=True, unbiased=False)
    return (x - m) / torch.sqrt(v + eps)
def _scatter_add(tgt, idx, src, axis=0):
    return tgt.clone().index_add_(axis, idx, src)
def _segment_sum(src, seg, n):
    out = torch.zeros((n,) + src.shape[1:], dtype=src.dtype)
    return out.index_add_(0, seg, src)
def _pad(x, pw):
    flat = tuple(v for pair in reversed(pw) for v in pair)
    return torch.nn.functional.pad(x, flat)

kernel_name = 'sequential_preLN__spectral_fnet__dense_activation_squared_relu__prompt'

def setup_inputs():

    _w = (lambda a: (lambda t: t.to(torch.bfloat16) if t.is_floating_point() else t)(torch.as_tensor(a)))
    def _A_setup():
        B, S, D = 1, 2048, 640
        ns = torch.arange(S)[:, None] * torch.arange(S)[None, :]
        nd = torch.arange(D)[:, None] * torch.arange(D)[None, :]
        Ws_re = torch.cos(-2.0 * torch.pi * ns / S).to(float)
        Ws_im = torch.sin(-2.0 * torch.pi * ns / S).to(float)
        Wd_re = torch.cos(-2.0 * torch.pi * nd / D).to(float)
        Wd_im = torch.sin(-2.0 * torch.pi * nd / D).to(float)
        return {
            "x": _w((lambda *s: torch.randn(*s, dtype=torch.bfloat16) * 0.05)(B, S, D)),
            "dft_seq_re": _w(Ws_re),
            "dft_seq_im": _w(Ws_im),
            "dft_hid_re": _w(Wd_re),
            "dft_hid_im": _w(Wd_im),
        }
    _A_in = _A_setup()

    def _B_setup():
        B, S, D, F = 1, 2048, 640, 1728
        return {
            "x": _w((lambda *s: torch.randn(*s, dtype=torch.bfloat16) * 0.05)(B, S, D)),
            "w1": _w((lambda *s: torch.randn(*s, dtype=torch.bfloat16) * 0.2)(D, F)),
            "b1": _w((lambda *s: torch.randn(*s, dtype=torch.bfloat16) * 0.2)(F)),
            "w2": _w((lambda *s: torch.randn(*s, dtype=torch.bfloat16) * 0.2)(F, D)),
            "b2": _w((lambda *s: torch.randn(*s, dtype=torch.bfloat16) * 0.2)(D)),
        }
    _B_in = _B_setup()

    return {
        "A_x": _A_in["x"],
        "A_dft_seq_re": _A_in["dft_seq_re"],
        "A_dft_seq_im": _A_in["dft_seq_im"],
        "A_dft_hid_re": _A_in["dft_hid_re"],
        "A_dft_hid_im": _A_in["dft_hid_im"],
        "B_x": _B_in["x"],
        "B_w1": _B_in["w1"],
        "B_b1": _B_in["b1"],
        "B_w2": _B_in["w2"],
        "B_b2": _B_in["b2"],
    }

def reference(A_x, A_dft_seq_re, A_dft_seq_im, A_dft_hid_re, A_dft_hid_im, B_x, B_w1, B_b1, B_w2, B_b2):
    def _A_ref(x, dft_seq_re, dft_seq_im, dft_hid_re, dft_hid_im):
        a = torch.einsum("bsd,ed->bse", x, dft_hid_re)
        b = torch.einsum("bsd,ed->bse", x, dft_hid_im)
        y = torch.einsum("ts,bse->bte", dft_seq_re, a) - torch.einsum("ts,bse->bte", dft_seq_im, b)
        return y

    def _B_ref(x, w1, b1, w2, b2):
        h = torch.einsum("bsd,df->bsf", x, w1) + b1
        zero = torch.zeros_like(h)
        r = torch.where(h > zero, h, zero)
        h = r * r
        out = torch.einsum("bsf,fd->bsd", h, w2) + b2
        return out

    x = A_x
    h = x + _A_ref(_layernorm(x, axis=-1), A_dft_seq_re, A_dft_seq_im, A_dft_hid_re, A_dft_hid_im)
    h2 = h + _B_ref(_layernorm(h, axis=-1), B_w1, B_b1, B_w2, B_b2)
    return h2
```

```cpp
#include <hip/hip_runtime.h>
#include <hip/hip_bf16.h>
#include <stdint.h>

#define LDS __attribute__((address_space(3)))
typedef LDS char* ldsp;
typedef short v4s __attribute__((ext_vector_type(4)));
typedef short v8s __attribute__((ext_vector_type(8)));
typedef __bf16 v8b __attribute__((ext_vector_type(8)));
typedef __bf16 v2b __attribute__((ext_vector_type(2)));
typedef float v4f __attribute__((ext_vector_type(4)));
typedef float v2f __attribute__((ext_vector_type(2)));
typedef unsigned v4u __attribute__((ext_vector_type(4)));
typedef unsigned v2u __attribute__((ext_vector_type(2)));

constexpr int S_ = 2048, D_ = 640, F_ = 1728;
constexpr size_t OFF_LNF = 0;
constexpr size_t OFF_PQF = OFF_LNF + 40 * 64 * 1024;
constexpr size_t OFF_HF  = OFF_PQF + 128 * 40 * 1024;
constexpr size_t OFF_HH  = OFF_HF + 128 * 20 * 1024;
constexpr size_t OFF_W1F = OFF_HH + 128 * 54 * 1024;
constexpr size_t OFF_W2F = OFF_W1F + 108 * 20 * 1024;
constexpr size_t OFF_CS  = OFF_W2F + 40 * 54 * 1024;
constexpr size_t OFF_ST  = OFF_CS + 20 * 1728 * 4;

struct Params {
  const char* x; const char* cr; const char* ci; const char* dr; const char* di;
  const char* w1; const char* b1; const char* w2; const char* b2;
  char* out; char* ws;
};

#if defined(__HIP_DEVICE_COMPILE__)
typedef __amdgpu_buffer_rsrc_t rsrc_t;
__device__ __forceinline__ rsrc_t mkrsrc(const void* p, unsigned bytes) { return __builtin_amdgcn_make_buffer_rsrc((void*)p, 0, bytes, 0x00020000); }
template <int AUX> __device__ __forceinline__ void dma(rsrc_t r, ldsp dst, int voff, int soff) {
  __builtin_amdgcn_raw_ptr_buffer_load_lds(r, (LDS void*)dst, 16, voff, soff, 0, AUX);
}
template <int N> __device__ __forceinline__ void waitv() { asm volatile("s_waitcnt vmcnt(%0)" ::"n"(N) : "memory"); }
__device__ __forceinline__ void bar() { __builtin_amdgcn_s_barrier(); }
__device__ __forceinline__ void waitl0() { __builtin_amdgcn_sched_barrier(0); __builtin_amdgcn_s_waitcnt(0xC07F); __builtin_amdgcn_sched_barrier(0); }
__device__ __forceinline__ v4f mfma(v8s a, v8s b, v4f c) {
  return __builtin_amdgcn_mfma_f32_16x16x32_bf16(__builtin_bit_cast(v8b, a), __builtin_bit_cast(v8b, b), c, 0, 0, 0);
}
__device__ __forceinline__ unsigned pk(float a, float b) { v2b r = {(__bf16)a, (__bf16)b}; return __builtin_bit_cast(unsigned, r); }
__device__ __forceinline__ float lo(unsigned u) { return __builtin_bit_cast(float, u << 16); }
__device__ __forceinline__ float hi(unsigned u) { return __builtin_bit_cast(float, u & 0xffff0000u); }
template <int NM, int ND> __device__ __forceinline__ void ilv() {
#pragma unroll
  for (int i = 0; i < NM; i++) { __builtin_amdgcn_sched_group_barrier(0x8, 1, 0); if (i < ND) __builtin_amdgcn_sched_group_barrier(0x100, 1, 0); }
}
__device__ __forceinline__ int piece_col(int T, int p, bool split) { return split ? 16 * T + 4 * p : 32 * (T >> 1) + 8 * p + 4 * (T & 1); }
__device__ __forceinline__ bool split640(int T) { return (T >> 1) % 5 == 2; }
__device__ __forceinline__ bool split1728(int T) { return (T >> 1) % 7 == 3 && T < 84; }

template <int NJ, bool KSPLIT, bool ASWZ, bool BSWZ, int NSTEPS, int STG, int NST, int BOFF>
__device__ __forceinline__ void consume(ldsp smem, int w, int lane, v4f (&acc)[2][NJ]) {
  const int g = lane >> 4, r = lane & 15;
  const int kh0 = KSPLIT ? (w >> 1) : 0;
  const int rb = KSPLIT ? (w & 1) * 2 : w * 2;
  const int sw = ((kh0 * 4 + g) ^ (r >> 1)) * 16;
  const int a0 = ASWZ ? (rb * 16 + r) * 128 + sw : (rb * 2 + kh0) * 1024 + lane * 16;
  const int a1 = ASWZ ? a0 ^ 64 : a0 + 1024;
  const int b0 = BOFF + (BSWZ ? r * 128 + sw : kh0 * 1024 + lane * 16);
  const int b1 = BSWZ ? b0 ^ 64 : b0 + 1024;
  v8s pa[2], pb[NJ], qa[2], qb[NJ];
#pragma unroll
  for (int i = 0; i < 2; i++) { qa[i] = v8s{0, 0, 0, 0, 0, 0, 0, 0};
#pragma unroll
    for (int j = 0; j < NJ; j++) acc[i][j] = v4f{0, 0, 0, 0}; }
#pragma unroll
  for (int j = 0; j < NJ; j++) qb[j] = v8s{0, 0, 0, 0, 0, 0, 0, 0};
#define RD(A, B, ao, bo) { _Pragma("unroll") for (int i = 0; i < 2; i++) A[i] = *(LDS v8s*)(base + ao + i * 2048); \
                           _Pragma("unroll") for (int j = 0; j < NJ; j++) B[j] = *(LDS v8s*)(base + bo + j * 2048); }
#define MM(A, B) { _Pragma("unroll") for (int j = 0; j < NJ; j++) _Pragma("unroll") for (int i = 0; i < 2; i++) acc[i][j] = mfma(B[j], A[i], acc[i][j]); }
  bar();
  int st = 0;
  if (!KSPLIT) {
#pragma unroll 1
    for (int s = 0; s < NSTEPS; s++) {
      ldsp base = smem + st * STG;
      RD(pa, pb, a0, b0); MM(qa, qb); ilv<2 * NJ, NJ + 2>(); waitl0();
      RD(qa, qb, a1, b1); MM(pa, pb); ilv<2 * NJ, NJ + 2>(); waitl0();
      bar();
      st++; if (st == NST) st = 0;
    }
    MM(qa, qb);
  } else {
#pragma unroll 1
    for (int s = 0; s < NSTEPS / 2; s++) {
      ldsp base = smem + st * STG;
      RD(pa, pb, a0, b0); MM(qa, qb); ilv<2 * NJ, NJ + 2>(); waitl0();
      bar();
      st++; if (st == NST) st = 0;
      base = smem + st * STG;
      RD(qa, qb, a0, b0); MM(pa, pb); ilv<2 * NJ, NJ + 2>(); waitl0();
      bar();
      st++; if (st == NST) st = 0;
    }
    if (NSTEPS & 1) {
      ldsp base = smem + st * STG;
      RD(pa, pb, a0, b0); MM(qa, qb); ilv<2 * NJ, NJ + 2>(); waitl0();
      bar();
      MM(pa, pb);
    } else MM(qa, qb);
  }
#undef RD
#undef MM
}

template <int PER, int DEP, int NST, int NSTEPS, class FN>
__device__ __forceinline__ void produce(FN issue) {
#pragma unroll
  for (int s = 0; s < DEP; s++) issue(s, s);
  waitv<PER * (DEP - 1)>(); bar();
  int st = DEP % NST;
#pragma unroll 1
  for (int s = DEP; s < NSTEPS; s++) {
    issue(s, st); st++; if (st == NST) st = 0;
    waitv<PER * (DEP - 1)>(); bar();
  }
  if (DEP >= 4) { waitv<PER * 2>(); bar(); }
  if (DEP >= 3) { waitv<PER>(); bar(); }
  waitv<0>(); bar();
  bar();
}

__device__ __forceinline__ void pair_reduce(ldsp smem, int w, int lane, v4f (&acc)[2][5], v4f (&o)[5]) {
  const int keep = w >> 1;
  ldsp mine = smem + w * 5120 + lane * 16, other = smem + (w ^ 2) * 5120 + lane * 16;
#pragma unroll
  for (int j = 0; j < 5; j++) *(LDS v4f*)(mine + j * 1024) = keep ? acc[0][j] : acc[1][j];
  waitl0(); bar();
#pragma unroll
  for (int j = 0; j < 5; j++) o[j] = (keep ? acc[1][j] : acc[0][j]) + *(LDS v4f*)(other + j * 1024);
}
#endif

__global__ __launch_bounds__(512) void k_prep(Params p) {
#if defined(__HIP_DEVICE_COMPILE__)
  extern __shared__ __attribute__((aligned(16))) char smem_[];
  ldsp smem = (ldsp)smem_;
  const int lane = threadIdx.x & 63, w = __builtin_amdgcn_readfirstlane(threadIdx.x >> 6), b = blockIdx.x;
  constexpr int RS = 1312;
  const unsigned* xr = (const unsigned*)(p.x + (size_t)(b * 8 + w) * 1280);
  unsigned xv[5];
#pragma unroll
  for (int i = 0; i < 5; i++) xv[i] = xr[lane + 64 * i];
  const int u = b * 5 + w;
  const bool has = w < 5 && u < 1080, isw1 = u < 540;
  ldsp wl = smem + 16384 + w * 4096;
  int kb = 0, cu = 0;
  if (has) {
    const int uu = isw1 ? u : u - 540;
    kb = isw1 ? uu / 27 : uu / 10; cu = isw1 ? uu % 27 : uu % 10;
    const int ld = isw1 ? F_ * 2 : D_ * 2;
    rsrc_t r = mkrsrc(isw1 ? p.w1 : p.w2, D_ * F_ * 2);
#pragma unroll
    for (int i = 0; i < 4; i++) dma<0>(r, wl + i * 1024, (kb * 32 + i * 8 + (lane >> 3)) * ld + cu * 128 + (lane & 7) * 16, 0);
  }
  float s = 0;
#pragma unroll
  for (int i = 0; i < 5; i++) s += lo(xv[i]) + hi(xv[i]);
#pragma unroll
  for (int m = 1; m < 64; m <<= 1) s += __shfl_xor(s, m);
  const float mean = s * (1.f / 640.f);
  float q = 0;
#pragma unroll
  for (int i = 0; i < 5; i++) { const float a = lo(xv[i]) - mean, c = hi(xv[i]) - mean; q += a * a + c * c; }
#pragma unroll
  for (int m = 1; m < 64; m <<= 1) q += __shfl_xor(q, m);
  const float rs = 1.0f / sqrtf(q * (1.f / 640.f) + 1e-5f);
#pragma unroll
  for (int i = 0; i < 5; i++) *(LDS unsigned*)(smem + w * RS + (lane + 64 * i) * 4) = pk((lo(xv[i]) - mean) * rs, (hi(xv[i]) - mean) * rs);
  waitl0(); bar();
  const int g = lane >> 4, qq = (lane & 15) >> 2, pp = lane & 3;
  if (w < 5) {
#pragma unroll
    for (int i = 0; i < 2; i++) {
      const int T = (w * 2 + i) * 4 + g;
      ldsp a = smem + qq * RS + piece_col(T, pp, split640(T)) * 2;
      const v4s v0 = __builtin_amdgcn_ds_read_tr16_b64_v4i16((LDS v4s*)a), v1 = __builtin_amdgcn_ds_read_tr16_b64_v4i16((LDS v4s*)(a + 4 * RS));
      *(v8s*)(p.ws + OFF_LNF + (size_t)T * 65536 + (b >> 2) * 1024 + (b & 3) * 256 + (lane & 15) * 16) = __builtin_shufflevector(v0, v1, 0, 1, 2, 3, 4, 5, 6, 7);
    }
  }
  if (has) {
    waitv<0>();
#pragma unroll
    for (int t = 0; t < 4; t++) {
      const int T = cu * 4 + t;
      const bool sp = isw1 ? split1728(T) : split640(T);
      ldsp a = wl + (g * 8 + qq) * 128 + (piece_col(T, pp, sp) - cu * 64) * 2;
      const v4s v0 = __builtin_amdgcn_ds_read_tr16_b64_v4i16((LDS v4s*)a), v1 = __builtin_amdgcn_ds_read_tr16_b64_v4i16((LDS v4s*)(a + 512));
      const v8s v = __builtin_shufflevector(v0, v1, 0, 1, 2, 3, 4, 5, 6, 7);
      char* dst = p.ws + (isw1 ? OFF_W1F + (size_t)T * 20480 : OFF_W2F + (size_t)T * 55296) + kb * 1024 + lane * 16;
      *(v8s*)dst = v;
      if (isw1) {
        const v4u uv = __builtin_bit_cast(v4u, v);
        float c = (lo(uv[0]) + hi(uv[0])) + (lo(uv[1]) + hi(uv[1])) + (lo(uv[2]) + hi(uv[2])) + (lo(uv[3]) + hi(uv[3]));
        c += __shfl_xor(c, 16); c += __shfl_xor(c, 32);
        if (lane < 16) ((float*)(p.ws + OFF_CS))[kb * F_ + T * 16 + lane] = c;
      }
    }
  }
#endif
}

__global__ __launch_bounds__(384) void k_g1(Params p) {
#if defined(__HIP_DEVICE_COMPILE__)
  extern __shared__ __attribute__((aligned(16))) char smem_[];
  ldsp smem = (ldsp)smem_;
  const int lane = threadIdx.x & 63, w = __builtin_amdgcn_readfirstlane(threadIdx.x >> 6), b = blockIdx.x;
  const int tg = (b & 7) * 2 + (b >> 7), c = (b >> 3) & 15, pq = c >> 3, nt = c & 7, odd = nt & 1;
  constexpr int STG = 26 * 1024, NST = 4;
  if (w >= 4) {
    const int lw = w - 4;
    rsrc_t ra = mkrsrc((pq ? p.ci : p.cr) + (size_t)tg * 128 * 4096, 128 * 4096), rb = mkrsrc(p.ws + OFF_LNF, 40 * 65536);
    int va[8];
#pragma unroll
    for (int i = 0; i < 8; i++) { const int row = (lw * 8 + i) * 8 + (lane >> 3); va[i] = row * 4096 + ((lane & 7) ^ ((row >> 1) & 7)) * 16; }
    int vb[5];
#pragma unroll
    for (int i = 0; i < 5; i++) { const int f = lw * 5 + i, j = f >> 1, T = 5 * nt + (odd ? (j + 1) % 5 : j); vb[i] = T * 65536 + (f & 1) * 1024 + lane * 16; }
    produce<13, 3, NST, 32>([&](int s, int st) {
      ldsp base = smem + st * STG;
#pragma unroll
      for (int i = 0; i < 8; i++) dma<0>(ra, base + (lw * 8 + i) * 1024, va[i], s * 128);
#pragma unroll
      for (int i = 0; i < 5; i++) dma<0>(rb, base + 16384 + (lw * 5 + i) * 1024, vb[i], s * 2048);
    });
  } else {
    v4f acc[2][5];
    consume<5, false, true, false, 32, STG, NST, 16384>(smem, w, lane, acc);
    const float sg = pq ? -1.f : 1.f;
    const int g = lane >> 4, kb0 = pq * 20 + 5 * (nt >> 1);
#pragma unroll
    for (int mi = 0; mi < 2; mi++) {
      char* dst = p.ws + OFF_PQF + (size_t)(tg * 8 + w * 2 + mi) * 40960;
#pragma unroll
      for (int u = 0; u < 2; u++) {
        const v4f a = acc[mi][2 * u] * sg, c2 = acc[mi][2 * u + 1] * sg;
        *(v4u*)(dst + (kb0 + odd * 3 + u) * 1024 + lane * 16) = v4u{pk(a[0], a[1]), pk(a[2], a[3]), pk(c2[0], c2[1]), pk(c2[2], c2[3])};
      }
      const v4f a = acc[mi][4] * sg;
      *(v2u*)(dst + (kb0 + 2) * 1024 + ((odd * 2 + (g >> 1)) * 16 + (lane & 15)) * 16 + (g & 1) * 8) = v2u{pk(a[0], a[1]), pk(a[2], a[3])};
    }
  }
#endif
}

__global__ __launch_bounds__(384) void k_g2(Params p) {
#if defined(__HIP_DEVICE_COMPILE__)
  extern __shared__ __attribute__((aligned(16))) char smem_[];
  ldsp smem = (ldsp)smem_;
  const int lane = threadIdx.x & 63, w = __builtin_amdgcn_readfirstlane(threadIdx.x >> 6), b = blockIdx.x;
  const int tg = (b & 7) * 2 + (b >> 7), c = (b >> 3) & 15, mh = c >> 3, nt = c & 7, odd = nt & 1;
  constexpr int STG = 18 * 1024, NST = 5;
  if (w >= 4) {
    const int lw = w - 4;
    rsrc_t ra = mkrsrc(p.ws + OFF_PQF + (size_t)(tg * 8 + mh * 4) * 40960, 4 * 40960), rr = mkrsrc(p.dr, D_ * D_ * 2), ri = mkrsrc(p.di, D_ * D_ * 2);
    int va[4], vb[5];
#pragma unroll
    for (int i = 0; i < 4; i++) { const int f = lw * 4 + i; va[i] = (f >> 1) * 40960 + (f & 1) * 1024 + lane * 16; }
#pragma unroll
    for (int i = 0; i < 5; i++) {
      const int row = (lw * 5 + i) * 8 + (lane >> 3), j = row >> 4, T = 5 * nt + (odd ? (j + 1) % 5 : j), n = row & 15;
      vb[i] = (piece_col(T, n >> 2, j == 4) + (n & 3)) * 1280 + ((lane & 7) ^ ((row >> 1) & 7)) * 16;
    }
    produce<9, 4, NST, 20>([&](int s, int st) {
      ldsp base = smem + st * STG;
#pragma unroll
      for (int i = 0; i < 4; i++) dma<0>(ra, base + (lw * 4 + i) * 1024, va[i], s * 2048);
      if (s < 10) {
#pragma unroll
        for (int i = 0; i < 5; i++) dma<0>(rr, base + 8192 + (lw * 5 + i) * 1024, vb[i], s * 128);
      } else {
#pragma unroll
        for (int i = 0; i < 5; i++) dma<0>(ri, base + 8192 + (lw * 5 + i) * 1024, vb[i], (s - 10) * 128);
      }
    });
    bar();
  } else {
    const int g = lane >> 4, tb = tg * 8 + mh * 4 + (w & 1) * 2 + (w >> 1), t = tb * 16 + (lane & 15);
    const int kb0 = 5 * (nt >> 1), cp = (kb0 + odd * 3) * 32 + g * 8, cs = (kb0 + 2) * 32 + odd * 16 + g * 4;
    const v4u x0 = *(const v4u*)(p.x + (size_t)t * 1280 + cp * 2), x1 = *(const v4u*)(p.x + (size_t)t * 1280 + cp * 2 + 64);
    const v2u x2 = *(const v2u*)(p.x + (size_t)t * 1280 + cs * 2);
    v4f acc[2][5], o[5];
    consume<5, true, false, true, 20, STG, NST, 8192>(smem, w, lane, acc);
    pair_reduce(smem, w, lane, acc, o);
    const v4u h0 = {pk(o[0][0] + lo(x0[0]), o[0][1] + hi(x0[0])), pk(o[0][2] + lo(x0[1]), o[0][3] + hi(x0[1])),
                    pk(o[1][0] + lo(x0[2]), o[1][1] + hi(x0[2])), pk(o[1][2] + lo(x0[3]), o[1][3] + hi(x0[3]))};
    const v4u h1 = {pk(o[2][0] + lo(x1[0]), o[2][1] + hi(x1[0])), pk(o[2][2] + lo(x1[1]), o[2][3] + hi(x1[1])),
                    pk(o[3][0] + lo(x1[2]), o[3][1] + hi(x1[2])), pk(o[3][2] + lo(x1[3]), o[3][3] + hi(x1[3]))};
    const v2u h2 = {pk(o[4][0] + lo(x2[0]), o[4][1] + hi(x2[0])), pk(o[4][2] + lo(x2[1]), o[4][3] + hi(x2[1]))};
    char* dst = p.ws + OFF_HF + (size_t)tb * 20480;
    *(v4u*)(dst + (kb0 + odd * 3) * 1024 + lane * 16) = h0;
    *(v4u*)(dst + (kb0 + odd * 3 + 1) * 1024 + lane * 16) = h1;
    *(v2u*)(dst + (kb0 + 2) * 1024 + ((odd * 2 + (g >> 1)) * 16 + (lane & 15)) * 16 + (g & 1) * 8) = h2;
    float s = lo(h2[0]) + hi(h2[0]) + lo(h2[1]) + hi(h2[1]);
#pragma unroll
    for (int i = 0; i < 4; i++) s += lo(h0[i]) + hi(h0[i]) + lo(h1[i]) + hi(h1[i]);
    s += __shfl_xor(s, 16); s += __shfl_xor(s, 32);
    const float m = s * (1.f / 80.f);
    float q = 0;
#pragma unroll
    for (int i = 0; i < 4; i++) { const float a = lo(h0[i]) - m, c2 = hi(h0[i]) - m, d = lo(h1[i]) - m, e = hi(h1[i]) - m; q += a * a + c2 * c2 + d * d + e * e; }
#pragma unroll
    for (int i = 0; i < 2; i++) { const float a = lo(h2[i]) - m, c2 = hi(h2[i]) - m; q += a * a + c2 * c2; }
    q += __shfl_xor(q, 16); q += __shfl_xor(q, 32);
    if (lane < 16) *(v2f*)(p.ws + OFF_ST + (size_t)t * 64 + nt * 8) = v2f{m, q};
  }
#endif
}

__global__ __launch_bounds__(384) void k_g3(Params p) {
#if defined(__HIP_DEVICE_COMPILE__)
  extern __shared__ __attribute__((aligned(16))) char smem_[];
  ldsp smem = (ldsp)smem_;
  const int lane = threadIdx.x & 63, w = __builtin_amdgcn_readfirstlane(threadIdx.x >> 6), b = blockIdx.x;
  const int tg = (b & 7) * 2 + (b >> 7), c = (b >> 3) & 15;
  const bool seven = c < 12;
  const int odd = seven ? c & 1 : 0, T0 = seven ? 7 * c : 84 + 6 * (c - 12);
  const int Ts = odd ? T0 : T0 + 6;
  constexpr int STG = 30 * 1024, NST = 4, XOFF = STG * NST;
  if (w >= 4) {
    const int lw = w - 4;
    rsrc_t ra = mkrsrc(p.ws + OFF_HF + (size_t)tg * 8 * 20480, 8 * 20480), rb = mkrsrc(p.ws + OFF_W1F, 108 * 20480);
    const int i2 = lw * 64 + lane;
    {
      const int j = i2 >> 4, T = j < 6 ? T0 + odd + j : Ts;
      float cs = 0;
      if (i2 < (seven ? 112 : 96)) {
        const float* src = (const float*)(p.ws + OFF_CS) + T * 16 + (i2 & 15);
#pragma unroll
        for (int k = 0; k < 20; k++) cs += src[k * F_];
      }
      const v4f* st = (const v4f*)(p.ws + OFF_ST + (size_t)(tg * 128 + i2) * 64);
      const v4f s0 = st[0], s1 = st[1], s2 = st[2], s3 = st[3];
      const float m = (s0[0] + s0[2] + s1[0] + s1[2] + s2[0] + s2[2] + s3[0] + s3[2]) * 0.125f;
      float q = s0[1] + s0[3] + s1[1] + s1[3] + s2[1] + s2[3] + s3[1] + s3[3];
      const float d0 = s0[0] - m, d1 = s0[2] - m, d2 = s1[0] - m, d3 = s1[2] - m, d4 = s2[0] - m, d5 = s2[2] - m, d6 = s3[0] - m, d7 = s3[2] - m;
      q += 80.f * (d0 * d0 + d1 * d1 + d2 * d2 + d3 * d3 + d4 * d4 + d5 * d5 + d6 * d6 + d7 * d7);
      const float rs = 1.0f / sqrtf(q * (1.f / 640.f) + 1e-5f);
      *(LDS float*)(smem + XOFF + i2 * 4) = cs;
      *(LDS v2f*)(smem + XOFF + 512 + i2 * 8) = v2f{m * rs, rs};
      waitl0();
    }
    int va[8], vb[7];
#pragma unroll
    for (int i = 0; i < 8; i++) { const int f = lw * 8 + i; va[i] = (f >> 1) * 20480 + (f & 1) * 1024 + lane * 16; }
#pragma unroll
    for (int i = 0; i < 7; i++) { const int f = lw * 7 + i, j = f >> 1, T = j < 6 ? T0 + odd + j : Ts;
      vb[i] = (j < 6 || seven) ? T * 20480 + (f & 1) * 1024 + lane * 16 : 0x40000000; }
    produce<15, 3, NST, 10>([&](int s, int st) {
      ldsp base = smem + st * STG;
#pragma unroll
      for (int i = 0; i < 8; i++) dma<0>(ra, base + (lw * 8 + i) * 1024, va[i], s * 2048);
#pragma unroll
      for (int i = 0; i < 7; i++) dma<0>(rb, base + 16384 + (lw * 7 + i) * 1024, vb[i], s * 2048);
    });
  } else {
    const int g = lane >> 4, kb0 = (T0 + odd) >> 1;
    v4u bp[3]; v2u bs = {0, 0};
#pragma unroll
    for (int u = 0; u < 3; u++) bp[u] = *(const v4u*)(p.b1 + ((kb0 + u) * 32 + g * 8) * 2);
    if (seven) bs = *(const v2u*)(p.b1 + (Ts * 16 + g * 4) * 2);
    v4f acc[2][7];
    consume<7, false, false, false, 10, STG, NST, 16384>(smem, w, lane, acc);
#pragma unroll
    for (int mi = 0; mi < 2; mi++) {
      const v2f st = *(LDS v2f*)(smem + XOFF + 512 + (w * 32 + mi * 16 + (lane & 15)) * 8);
      char* dst = p.ws + OFF_HH + (size_t)(tg * 8 + w * 2 + mi) * 55296;
      unsigned r[14];
#pragma unroll
      for (int j = 0; j < 7; j++) {
        const v4f cs = *(LDS v4f*)(smem + XOFF + (j * 16 + g * 4) * 4);
        const unsigned bb0 = j < 6 ? bp[j >> 1][(j & 1) * 2] : bs[0], bb1 = j < 6 ? bp[j >> 1][(j & 1) * 2 + 1] : bs[1];
        const float bias[4] = {lo(bb0), hi(bb0), lo(bb1), hi(bb1)};
        float z[4];
#pragma unroll
        for (int e = 0; e < 4; e++) { const float v = fmaxf(acc[mi][j][e] * st[1] - st[0] * cs[e] + bias[e], 0.f); z[e] = v * v; }
        r[2 * j] = pk(z[0], z[1]); r[2 * j + 1] = pk(z[2], z[3]);
      }
#pragma unroll
      for (int u = 0; u < 3; u++) *(v4u*)(dst + (kb0 + u) * 1024 + lane * 16) = v4u{r[4 * u], r[4 * u + 1], r[4 * u + 2], r[4 * u + 3]};
      if (seven) *(v2u*)(dst + (Ts >> 1) * 1024 + ((odd * 2 + (g >> 1)) * 16 + (lane & 15)) * 16 + (g & 1) * 8) = v2u{r[12], r[13]};
    }
  }
#endif
}

__global__ __launch_bounds__(384) void k_g4(Params p) {
#if defined(__HIP_DEVICE_COMPILE__)
  extern __shared__ __attribute__((aligned(16))) char smem_[];
  ldsp smem = (ldsp)smem_;
  const int lane = threadIdx.x & 63, w = __builtin_amdgcn_readfirstlane(threadIdx.x >> 6), b = blockIdx.x;
  const int tg = (b & 7) * 2 + (b >> 7), c = (b >> 3) & 15, mh = c >> 3, nt = c & 7, odd = nt & 1;
  constexpr int STG = 18 * 1024, NST = 5;
  if (w >= 4) {
    const int lw = w - 4;
    rsrc_t ra = mkrsrc(p.ws + OFF_HH + (size_t)(tg * 8 + mh * 4) * 55296, 4 * 55296), rb = mkrsrc(p.ws + OFF_W2F, 40 * 55296);
    int va[4], vb[5];
#pragma unroll
    for (int i = 0; i < 4; i++) { const int f = lw * 4 + i; va[i] = (f >> 1) * 55296 + (f & 1) * 1024 + lane * 16; }
#pragma unroll
    for (int i = 0; i < 5; i++) { const int f = lw * 5 + i, j = f >> 1, T = 5 * nt + (odd ? (j + 1) % 5 : j); vb[i] = T * 55296 + (f & 1) * 1024 + lane * 16; }
    produce<9, 4, NST, 27>([&](int s, int st) {
      ldsp base = smem + st * STG;
#pragma unroll
      for (int i = 0; i < 4; i++) dma<0>(ra, base + (lw * 4 + i) * 1024, va[i], s * 2048);
#pragma unroll
      for (int i = 0; i < 5; i++) dma<0>(rb, base + 8192 + (lw * 5 + i) * 1024, vb[i], s * 2048);
    });
    bar();
  } else {
    const int g = lane >> 4, tb = tg * 8 + mh * 4 + (w & 1) * 2 + (w >> 1), t = tb * 16 + (lane & 15);
    const int kb0 = 5 * (nt >> 1), cp = (kb0 + odd * 3) * 32 + g * 8, cs = (kb0 + 2) * 32 + odd * 16 + g * 4;
    const char* hs = p.ws + OFF_HF + (size_t)tb * 20480;
    const v4u h0 = *(const v4u*)(hs + (kb0 + odd * 3) * 1024 + lane * 16), h1 = *(const v4u*)(hs + (kb0 + odd * 3 + 1) * 1024 + lane * 16);
    const v2u h2 = *(const v2u*)(hs + (kb0 + 2) * 1024 + ((odd * 2 + (g >> 1)) * 16 + (lane & 15)) * 16 + (g & 1) * 8);
    const v4u c0 = *(const v4u*)(p.b2 + cp * 2), c1 = *(const v4u*)(p.b2 + cp * 2 + 64);
    const v2u c2 = *(const v2u*)(p.b2 + cs * 2);
    v4f acc[2][5], o[5];
    consume<5, true, false, false, 27, STG, NST, 8192>(smem, w, lane, acc);
    pair_reduce(smem, w, lane, acc, o);
#define FIN(a, e, h, c) pk(a[e] + lo(h) + lo(c), a[e + 1] + hi(h) + hi(c))
    char* dst = p.out + (size_t)t * 1280;
    *(v4u*)(dst + cp * 2) = v4u{FIN(o[0], 0, h0[0], c0[0]), FIN(o[0], 2, h0[1], c0[1]), FIN(o[1], 0, h0[2], c0[2]), FIN(o[1], 2, h0[3], c0[3])};
    *(v4u*)(dst + cp * 2 + 64) = v4u{FIN(o[2], 0, h1[0], c1[0]), FIN(o[2], 2, h1[1], c1[1]), FIN(o[3], 0, h1[2], c1[2]), FIN(o[3], 2, h1[3], c1[3])};
    *(v2u*)(dst + cs * 2) = v2u{FIN(o[4], 0, h2[0], c2[0]), FIN(o[4], 2, h2[1], c2[1])};
#undef FIN
  }
#endif
}

extern "C" void kernel_launch(void* const* d_in, const int* in_sizes, int n_in, void* d_out, int out_size, void* d_ws, size_t ws_size,
                              hipStream_t stream) {
  Params p;
  p.x = (const char*)d_in[0]; p.cr = (const char*)d_in[1]; p.ci = (const char*)d_in[2]; p.dr = (const char*)d_in[3]; p.di = (const char*)d_in[4];
  p.w1 = (const char*)d_in[6]; p.b1 = (const char*)d_in[7]; p.w2 = (const char*)d_in[8]; p.b2 = (const char*)d_in[9];
  p.out = (char*)d_out; p.ws = (char*)d_ws;
  constexpr int L0 = 16384 + 5 * 4096, L1 = 26 * 1024 * 4, L2 = 18 * 1024 * 5, L3 = 30 * 1024 * 4 + 512 + 1024, L4 = 18 * 1024 * 5;
  (void)hipFuncSetAttribute((const void*)k_g1, hipFuncAttributeMaxDynamicSharedMemorySize, L1);
  (void)hipFuncSetAttribute((const void*)k_g2, hipFuncAttributeMaxDynamicSharedMemorySize, L2);
  (void)hipFuncSetAttribute((const void*)k_g3, hipFuncAttributeMaxDynamicSharedMemorySize, L3);
  (void)hipFuncSetAttribute((const void*)k_g4, hipFuncAttributeMaxDynamicSharedMemorySize, L4);
  k_prep<<<256, 512, L0, stream>>>(p);
  k_g1<<<256, 384, L1, stream>>>(p);
  k_g2<<<256, 384, L2, stream>>>(p);
  k_g3<<<256, 384, L3, stream>>>(p);
  k_g4<<<256, 384, L4, stream>>>(p);
}
```

```cpp
#include <hip/hip_runtime.h>
#include <hip/hip_bf16.h>
#include <stdint.h>

#define LDS __attribute__((address_space(3)))
typedef LDS char* ldsp;
typedef short v4s __attribute__((ext_vector_type(4)));
typedef short v8s __attribute__((ext_vector_type(8)));
typedef __bf16 v8b __attribute__((ext_vector_type(8)));
typedef __bf16 v2b __attribute__((ext_vector_type(2)));
typedef float v4f __attribute__((ext_vector_type(4)));
typedef float v2f __attribute__((ext_vector_type(2)));
typedef unsigned v4u __attribute__((ext_vector_type(4)));
typedef unsigned v2u __attribute__((ext_vector_type(2)));

constexpr int S_ = 2048, D_ = 640, F_ = 1728;
constexpr size_t OFF_LNF = 0;
constexpr size_t OFF_PQF = OFF_LNF + 40 * 64 * 1024;
constexpr size_t OFF_HF  = OFF_PQF + 128 * 40 * 1024;
constexpr size_t OFF_HH  = OFF_HF + 128 * 20 * 1024;
constexpr size_t OFF_W1F = OFF_HH + 128 * 54 * 1024;
constexpr size_t OFF_W2F = OFF_W1F + 108 * 20 * 1024;
constexpr size_t OFF_CS  = OFF_W2F + 40 * 54 * 1024;
constexpr size_t OFF_ST  = OFF_CS + 20 * 1728 * 4;

struct Params {
  const char* x; const char* cr; const char* ci; const char* dr; const char* di;
  const char* w1; const char* b1; const char* w2; const char* b2;
  char* out; char* ws;
};

#if defined(__HIP_DEVICE_COMPILE__)
typedef __amdgpu_buffer_rsrc_t rsrc_t;
__device__ __forceinline__ rsrc_t mkrsrc(const void* p, unsigned bytes) { return __builtin_amdgcn_make_buffer_rsrc((void*)p, 0, bytes, 0x00020000); }
template <int AUX> __device__ __forceinline__ void dma(rsrc_t r, ldsp dst, int voff, int soff) {
  __builtin_amdgcn_raw_ptr_buffer_load_lds(r, (LDS void*)dst, 16, voff, soff, 0, AUX);
}
template <int N> __device__ __forceinline__ void waitv() { asm volatile("s_waitcnt vmcnt(%0)" ::"n"(N) : "memory"); }
__device__ __forceinline__ void bar() { __builtin_amdgcn_s_barrier(); }
__device__ __forceinline__ void waitl0() { __builtin_amdgcn_sched_barrier(0); __builtin_amdgcn_s_waitcnt(0xC07F); __builtin_amdgcn_sched_barrier(0); }
__device__ __forceinline__ v4f mfma(v8s a, v8s b, v4f c) {
  return __builtin_amdgcn_mfma_f32_16x16x32_bf16(__builtin_bit_cast(v8b, a), __builtin_bit_cast(v8b, b), c, 0, 0, 0);
}
__device__ __forceinline__ unsigned pk(float a, float b) { v2b r = {(__bf16)a, (__bf16)b}; return __builtin_bit_cast(unsigned, r); }
__device__ __forceinline__ float lo(unsigned u) { return __builtin_bit_cast(float, u << 16); }
__device__ __forceinline__ float hi(unsigned u) { return __builtin_bit_cast(float, u & 0xffff0000u); }
template <int NM, int ND> __device__ __forceinline__ void ilv() {
#pragma unroll
  for (int i = 0; i < NM; i++) { __builtin_amdgcn_sched_group_barrier(0x8, 1, 0); if (i < ND) __builtin_amdgcn_sched_group_barrier(0x100, 1, 0); }
}
__device__ __forceinline__ int piece_col(int T, int p, bool split) { return split ? 16 * T + 4 * p : 32 * (T >> 1) + 8 * p + 4 * (T & 1); }
__device__ __forceinline__ bool split640(int T) { return (T >> 1) % 5 == 2; }
__device__ __forceinline__ bool split1728(int T) { return (T >> 1) % 7 == 3 && T < 84; }

template <int NJ, bool KSPLIT, bool ASWZ, bool BSWZ, int NSTEPS, int STG, int NST, int BOFF>
__device__ __forceinline__ void consume(ldsp smem, int w, int lane, v4f (&acc)[2][NJ]) {
  const int g = lane >> 4, r = lane & 15;
  const int kh0 = KSPLIT ? (w >> 1) : 0;
  const int rb = KSPLIT ? (w & 1) * 2 : w * 2;
  const int sw = ((kh0 * 4 + g) ^ (r >> 1)) * 16;
  const int a0 = ASWZ ? (rb * 16 + r) * 128 + sw : (rb * 2 + kh0) * 1024 + lane * 16;
  const int a1 = ASWZ ? a0 ^ 64 : a0 + 1024;
  const int b0 = BOFF + (BSWZ ? r * 128 + sw : kh0 * 1024 + lane * 16);
  const int b1 = BSWZ ? b0 ^ 64 : b0 + 1024;
  v8s pa[2], pb[NJ], qa[2], qb[NJ];
#pragma unroll
  for (int i = 0; i < 2; i++) { qa[i] = v8s{0, 0, 0, 0, 0, 0, 0, 0};
#pragma unroll
    for (int j = 0; j < NJ; j++) acc[i][j] = v4f{0, 0, 0, 0}; }
#pragma unroll
  for (int j = 0; j < NJ; j++) qb[j] = v8s{0, 0, 0, 0, 0, 0, 0, 0};
#define RD(A, B, ao, bo) { _Pragma("unroll") for (int i = 0; i < 2; i++) A[i] = *(LDS v8s*)(base + ao + i * 2048); \
                           _Pragma("unroll") for (int j = 0; j < NJ; j++) B[j] = *(LDS v8s*)(base + bo + j * 2048); }
#define MM(A, B) { _Pragma("unroll") for (int j = 0; j < NJ; j++) _Pragma("unroll") for (int i = 0; i < 2; i++) acc[i][j] = mfma(B[j], A[i], acc[i][j]); }
  bar();
  int st = 0;
  if (!KSPLIT) {
#pragma unroll 1
    for (int s = 0; s < NSTEPS; s++) {
      ldsp base = smem + st * STG;
      RD(pa, pb, a0, b0); MM(qa, qb); ilv<2 * NJ, NJ + 2>(); waitl0();
      RD(qa, qb, a1, b1); MM(pa, pb); ilv<2 * NJ, NJ + 2>(); waitl0();
      bar();
      st++; if (st == NST) st = 0;
    }
    MM(qa, qb);
  } else {
#pragma unroll 1
    for (int s = 0; s < NSTEPS / 2; s++) {
      ldsp base = smem + st * STG;
      RD(pa, pb, a0, b0); MM(qa, qb); ilv<2 * NJ, NJ + 2>(); waitl0();
      bar();
      st++; if (st == NST) st = 0;
      base = smem + st * STG;
      RD(qa, qb, a0, b0); MM(pa, pb); ilv<2 * NJ, NJ + 2>(); waitl0();
      bar();
      st++; if (st == NST) st = 0;
    }
    if (NSTEPS & 1) {
      ldsp base = smem + st * STG;
      RD(pa, pb, a0, b0); MM(qa, qb); ilv<2 * NJ, NJ + 2>(); waitl0();
      bar();
      MM(pa, pb);
    } else MM(qa, qb);
  }
#undef RD
#undef MM
}

template <int PER, int DEP, int NST, int NSTEPS, class FN>
__device__ __forceinline__ void produce(FN issue) {
#pragma unroll
  for (int s = 0; s < DEP; s++) issue(s, s);
  waitv<PER * (DEP - 1)>(); bar();
  int st = DEP % NST;
#pragma unroll 1
  for (int s = DEP; s < NSTEPS; s++) {
    issue(s, st); st++; if (st == NST) st = 0;
    waitv<PER * (DEP - 1)>(); bar();
  }
  if (DEP >= 4) { waitv<PER * 2>(); bar(); }
  if (DEP >= 3) { waitv<PER>(); bar(); }
  waitv<0>(); bar();
  bar();
}

__device__ __forceinline__ void pair_reduce(ldsp smem, int w, int lane, v4f (&acc)[2][5], v4f (&o)[5]) {
  const int keep = w >> 1;
  ldsp mine = smem + w * 5120 + lane * 16, other = smem + (w ^ 2) * 5120 + lane * 16;
#pragma unroll
  for (int j = 0; j < 5; j++) *(LDS v4f*)(mine + j * 1024) = keep ? acc[0][j] : acc[1][j];
  waitl0(); bar();
#pragma unroll
  for (int j = 0; j < 5; j++) o[j] = (keep ? acc[1][j] : acc[0][j]) + *(LDS v4f*)(other + j * 1024);
}
#endif

__global__ __launch_bounds__(512) void k_prep(Params p) {
#if defined(__HIP_DEVICE_COMPILE__)
  extern __shared__ __attribute__((aligned(16))) char smem_[];
  ldsp smem = (ldsp)smem_;
  const int lane = threadIdx.x & 63, w = __builtin_amdgcn_readfirstlane(threadIdx.x >> 6), b = blockIdx.x;
  constexpr int RS = 1312;
  const unsigned* xr = (const unsigned*)(p.x + (size_t)(b * 8 + w) * 1280);
  unsigned xv[5];
#pragma unroll
  for (int i = 0; i < 5; i++) xv[i] = xr[lane + 64 * i];
  const int u = b * 5 + w;
  const bool has = w < 5 && u < 1080, isw1 = u < 540;
  ldsp wl = smem + 16384 + w * 4096;
  int kb = 0, cu = 0;
  if (has) {
    const int uu = isw1 ? u : u - 540;
    kb = isw1 ? uu / 27 : uu / 10; cu = isw1 ? uu % 27 : uu % 10;
    const int ld = isw1 ? F_ * 2 : D_ * 2;
    rsrc_t r = mkrsrc(isw1 ? p.w1 : p.w2, D_ * F_ * 2);
#pragma unroll
    for (int i = 0; i < 4; i++) dma<0>(r, wl + i * 1024, (kb * 32 + i * 8 + (lane >> 3)) * ld + cu * 128 + (lane & 7) * 16, 0);
  }

  if (w >= 5) {
    const int j = b >> 3, xc = b & 7;
    rsrc_t r = mkrsrc((j >> 4 ? p.ci : p.cr) + (size_t)(xc * 256 + (j & 15) * 16) * 4096, 16 * 4096);
    ldsp dl = smem + 16384 + 5 * 4096;
    for (int i = w - 5; i < 64; i += 3) dma<0>(r, dl, ((i & 1) * 8 + (lane >> 3)) * 4096 + (i >> 1) * 128 + (lane & 7) * 16, 0);
  }
  float s = 0;
#pragma unroll
  for (int i = 0; i < 5; i++) s += lo(xv[i]) + hi(xv[i]);
#pragma unroll
  for (int m = 1; m < 64; m <<= 1) s += __shfl_xor(s, m);
  const float mean = s * (1.f / 640.f);
  float q = 0;
#pragma unroll
  for (int i = 0; i < 5; i++) { const float a = lo(xv[i]) - mean, c = hi(xv[i]) - mean; q += a * a + c * c; }
#pragma unroll
  for (int m = 1; m < 64; m <<= 1) q += __shfl_xor(q, m);
  const float rs = 1.0f / sqrtf(q * (1.f / 640.f) + 1e-5f);
#pragma unroll
  for (int i = 0; i < 5; i++) *(LDS unsigned*)(smem + w * RS + (lane + 64 * i) * 4) = pk((lo(xv[i]) - mean) * rs, (hi(xv[i]) - mean) * rs);
  waitl0(); bar();
  const int g = lane >> 4, qq = (lane & 15) >> 2, pp = lane & 3;
  if (w < 5) {
#pragma unroll
    for (int i = 0; i < 2; i++) {
      const int T = (w * 2 + i) * 4 + g;
      ldsp a = smem + qq * RS + piece_col(T, pp, split640(T)) * 2;
      const v4s v0 = __builtin_amdgcn_ds_read_tr16_b64_v4i16((LDS v4s*)a), v1 = __builtin_amdgcn_ds_read_tr16_b64_v4i16((LDS v4s*)(a + 4 * RS));
      *(v8s*)(p.ws + OFF_LNF + (size_t)T * 65536 + (b >> 2) * 1024 + (b & 3) * 256 + (lane & 15) * 16) = __builtin_shufflevector(v0, v1, 0, 1, 2, 3, 4, 5, 6, 7);
    }
  }
  if (has) {
    waitv<0>();
#pragma unroll
    for (int t = 0; t < 4; t++) {
      const int T = cu * 4 + t;
      const bool sp = isw1 ? split1728(T) : split640(T);
      ldsp a = wl + (g * 8 + qq) * 128 + (piece_col(T, pp, sp) - cu * 64) * 2;
      const v4s v0 = __builtin_amdgcn_ds_read_tr16_b64_v4i16((LDS v4s*)a), v1 = __builtin_amdgcn_ds_read_tr16_b64_v4i16((LDS v4s*)(a + 512));
      const v8s v = __builtin_shufflevector(v0, v1, 0, 1, 2, 3, 4, 5, 6, 7);
      char* dst = p.ws + (isw1 ? OFF_W1F + (size_t)T * 20480 : OFF_W2F + (size_t)T * 55296) + kb * 1024 + lane * 16;
      *(v8s*)dst = v;
      if (isw1) {
        const v4u uv = __builtin_bit_cast(v4u, v);
        float c = (lo(uv[0]) + hi(uv[0])) + (lo(uv[1]) + hi(uv[1])) + (lo(uv[2]) + hi(uv[2])) + (lo(uv[3]) + hi(uv[3]));
        c += __shfl_xor(c, 16); c += __shfl_xor(c, 32);
        if (lane < 16) ((float*)(p.ws + OFF_CS))[kb * F_ + T * 16 + lane] = c;
      }
    }
  }
#endif
}

__global__ __launch_bounds__(384) void k_g1(Params p) {
#if defined(__HIP_DEVICE_COMPILE__)
  extern __shared__ __attribute__((aligned(16))) char smem_[];
  ldsp smem = (ldsp)smem_;
  const int lane = threadIdx.x & 63, w = __builtin_amdgcn_readfirstlane(threadIdx.x >> 6), b = blockIdx.x;
  const int tg = (b & 7) * 2 + (b >> 7), c = (b >> 3) & 15, pq = c >> 3, nt = c & 7, odd = nt & 1;
  constexpr int STG = 26 * 1024, NST = 4;
  if (w >= 4) {
    const int lw = w - 4;
    rsrc_t ra = mkrsrc((pq ? p.ci : p.cr) + (size_t)tg * 128 * 4096, 128 * 4096), rb = mkrsrc(p.ws + OFF_LNF, 40 * 65536);
    int va[8];
#pragma unroll
    for (int i = 0; i < 8; i++) { const int row = (lw * 8 + i) * 8 + (lane >> 3); va[i] = row * 4096 + ((lane & 7) ^ ((row >> 1) & 7)) * 16; }
    int vb[5];
#pragma unroll
    for (int i = 0; i < 5; i++) { const int f = lw * 5 + i, j = f >> 1, T = 5 * nt + (odd ? (j + 1) % 5 : j); vb[i] = T * 65536 + (f & 1) * 1024 + lane * 16; }
    produce<13, 3, NST, 32>([&](int s, int st) {
      ldsp base = smem + st * STG;
#pragma unroll
      for (int i = 0; i < 8; i++) dma<0>(ra, base + (lw * 8 + i) * 1024, va[i], s * 128);
#pragma unroll
      for (int i = 0; i < 5; i++) dma<0>(rb, base + 16384 + (lw * 5 + i) * 1024, vb[i], s * 2048);
    });
  } else {
    v4f acc[2][5];
    consume<5, false, true, false, 32, STG, NST, 16384>(smem, w, lane, acc);
    const float sg = pq ? -1.f : 1.f;
    const int g = lane >> 4, kb0 = pq * 20 + 5 * (nt >> 1);
#pragma unroll
    for (int mi = 0; mi < 2; mi++) {
      char* dst = p.ws + OFF_PQF + (size_t)(tg * 8 + w * 2 + mi) * 40960;
#pragma unroll
      for (int u = 0; u < 2; u++) {
        const v4f a = acc[mi][2 * u] * sg, c2 = acc[mi][2 * u + 1] * sg;
        *(v4u*)(dst + (kb0 + odd * 3 + u) * 1024 + lane * 16) = v4u{pk(a[0], a[1]), pk(a[2], a[3]), pk(c2[0], c2[1]), pk(c2[2], c2[3])};
      }
      const v4f a = acc[mi][4] * sg;
      *(v2u*)(dst + (kb0 + 2) * 1024 + ((odd * 2 + (g >> 1)) * 16 + (lane & 15)) * 16 + (g & 1) * 8) = v2u{pk(a[0], a[1]), pk(a[2], a[3])};
    }
  }
#endif
}

__global__ __launch_bounds__(384) void k_g2(Params p) {
#if defined(__HIP_DEVICE_COMPILE__)
  extern __shared__ __attribute__((aligned(16))) char smem_[];
  ldsp smem = (ldsp)smem_;
  const int lane = threadIdx.x & 63, w = __builtin_amdgcn_readfirstlane(threadIdx.x >> 6), b = blockIdx.x;
  const int tg = (b & 7) * 2 + (b >> 7), c = (b >> 3) & 15, mh = c >> 3, nt = c & 7, odd = nt & 1;
  constexpr int STG = 18 * 1024, NST = 5;
  if (w >= 4) {
    const int lw = w - 4;
    rsrc_t ra = mkrsrc(p.ws + OFF_PQF + (size_t)(tg * 8 + mh * 4) * 40960, 4 * 40960), rr = mkrsrc(p.dr, D_ * D_ * 2), ri = mkrsrc(p.di, D_ * D_ * 2);
    int va[4], vb[5];
#pragma unroll
    for (int i = 0; i < 4; i++) { const int f = lw * 4 + i; va[i] = (f >> 1) * 40960 + (f & 1) * 1024 + lane * 16; }
#pragma unroll
    for (int i = 0; i < 5; i++) {
      const int row = (lw * 5 + i) * 8 + (lane >> 3), j = row >> 4, T = 5 * nt + (odd ? (j + 1) % 5 : j), n = row & 15;
      vb[i] = (piece_col(T, n >> 2, j == 4) + (n & 3)) * 1280 + ((lane & 7) ^ ((row >> 1) & 7)) * 16;
    }
    produce<9, 4, NST, 20>([&](int s, int st) {
      ldsp base = smem + st * STG;
#pragma unroll
      for (int i = 0; i < 4; i++) dma<0>(ra, base + (lw * 4 + i) * 1024, va[i], s * 2048);
      if (s < 10) {
#pragma unroll
        for (int i = 0; i < 5; i++) dma<0>(rr, base + 8192 + (lw * 5 + i) * 1024, vb[i], s * 128);
      } else {
#pragma unroll
        for (int i = 0; i < 5; i++) dma<0>(ri, base + 8192 + (lw * 5 + i) * 1024, vb[i], (s - 10) * 128);
      }
    });
    bar();
  } else {
    const int g = lane >> 4, tb = tg * 8 + mh * 4 + (w & 1) * 2 + (w >> 1), t = tb * 16 + (lane & 15);
    const int kb0 = 5 * (nt >> 1), cp = (kb0 + odd * 3) * 32 + g * 8, cs = (kb0 + 2) * 32 + odd * 16 + g * 4;
    const v4u x0 = *(const v4u*)(p.x + (size_t)t * 1280 + cp * 2), x1 = *(const v4u*)(p.x + (size_t)t * 1280 + cp * 2 + 64);
    const v2u x2 = *(const v2u*)(p.x + (size_t)t * 1280 + cs * 2);
    v4f acc[2][5], o[5];
    consume<5, true, false, true, 20, STG, NST, 8192>(smem, w, lane, acc);
    pair_reduce(smem, w, lane, acc, o);
    const v4u h0 = {pk(o[0][0] + lo(x0[0]), o[0][1] + hi(x0[0])), pk(o[0][2] + lo(x0[1]), o[0][3] + hi(x0[1])),
                    pk(o[1][0] + lo(x0[2]), o[1][1] + hi(x0[2])), pk(o[1][2] + lo(x0[3]), o[1][3] + hi(x0[3]))};
    const v4u h1 = {pk(o[2][0] + lo(x1[0]), o[2][1] + hi(x1[0])), pk(o[2][2] + lo(x1[1]), o[2][3] + hi(x1[1])),
                    pk(o[3][0] + lo(x1[2]), o[3][1] + hi(x1[2])), pk(o[3][2] + lo(x1[3]), o[3][3] + hi(x1[3]))};
    const v2u h2 = {pk(o[4][0] + lo(x2[0]), o[4][1] + hi(x2[0])), pk(o[4][2] + lo(x2[1]), o[4][3] + hi(x2[1]))};
    char* dst = p.ws + OFF_HF + (size_t)tb * 20480;
    *(v4u*)(dst + (kb0 + odd * 3) * 1024 + lane * 16) = h0;
    *(v4u*)(dst + (kb0 + odd * 3 + 1) * 1024 + lane * 16) = h1;
    *(v2u*)(dst + (kb0 + 2) * 1024 + ((odd * 2 + (g >> 1)) * 16 + (lane & 15)) * 16 + (g & 1) * 8) = h2;
    float s = lo(h2[0]) + hi(h2[0]) + lo(h2[1]) + hi(h2[1]);
#pragma unroll
    for (int i = 0; i < 4; i++) s += lo(h0[i]) + hi(h0[i]) + lo(h1[i]) + hi(h1[i]);
    s += __shfl_xor(s, 16); s += __shfl_xor(s, 32);
    const float m = s * (1.f / 80.f);
    float q = 0;
#pragma unroll
    for (int i = 0; i < 4; i++) { const float a = lo(h0[i]) - m, c2 = hi(h0[i]) - m, d = lo(h1[i]) - m, e = hi(h1[i]) - m; q += a * a + c2 * c2 + d * d + e * e; }
#pragma unroll
    for (int i = 0; i < 2; i++) { const float a = lo(h2[i]) - m, c2 = hi(h2[i]) - m; q += a * a + c2 * c2; }
    q += __shfl_xor(q, 16); q += __shfl_xor(q, 32);
    if (lane < 16) *(v2f*)(p.ws + OFF_ST + (size_t)t * 64 + nt * 8) = v2f{m, q};
  }
#endif
}

__global__ __launch_bounds__(384) void k_g3(Params p) {
#if defined(__HIP_DEVICE_COMPILE__)
  extern __shared__ __attribute__((aligned(16))) char smem_[];
  ldsp smem = (ldsp)smem_;
  const int lane = threadIdx.x & 63, w = __builtin_amdgcn_readfirstlane(threadIdx.x >> 6), b = blockIdx.x;
  const int tg = (b & 7) * 2 + (b >> 7), c = (b >> 3) & 15;
  const bool seven = c < 12;
  const int odd = seven ? c & 1 : 0, T0 = seven ? 7 * c : 84 + 6 * (c - 12);
  const int Ts = odd ? T0 : T0 + 6;
  constexpr int STG = 30 * 1024, NST = 4, XOFF = STG * NST;
  if (w >= 4) {
    const int lw = w - 4;
    rsrc_t ra = mkrsrc(p.ws + OFF_HF + (size_t)tg * 8 * 20480, 8 * 20480), rb = mkrsrc(p.ws + OFF_W1F, 108 * 20480);
    const int i2 = lw * 64 + lane;
    {
      const int j = i2 >> 4, T = j < 6 ? T0 + odd + j : Ts;
      float cs = 0;
      if (i2 < (seven ? 112 : 96)) {
        const float* src = (const float*)(p.ws + OFF_CS) + T * 16 + (i2 & 15);
#pragma unroll
        for (int k = 0; k < 20; k++) cs += src[k * F_];
      }
      const v4f* st = (const v4f*)(p.ws + OFF_ST + (size_t)(tg * 128 + i2) * 64);
      const v4f s0 = st[0], s1 = st[1], s2 = st[2], s3 = st[3];
      const float m = (s0[0] + s0[2] + s1[0] + s1[2] + s2[0] + s2[2] + s3[0] + s3[2]) * 0.125f;
      float q = s0[1] + s0[3] + s1[1] + s1[3] + s2[1] + s2[3] + s3[1] + s3[3];
      const float d0 = s0[0] - m, d1 = s0[2] - m, d2 = s1[0] - m, d3 = s1[2] - m, d4 = s2[0] - m, d5 = s2[2] - m, d6 = s3[0] - m, d7 = s3[2] - m;
      q += 80.f * (d0 * d0 + d1 * d1 + d2 * d2 + d3 * d3 + d4 * d4 + d5 * d5 + d6 * d6 + d7 * d7);
      const float rs = 1.0f / sqrtf(q * (1.f / 640.f) + 1e-5f);
      *(LDS float*)(smem + XOFF + i2 * 4) = cs;
      *(LDS v2f*)(smem + XOFF + 512 + i2 * 8) = v2f{m * rs, rs};
      waitl0();
    }
    int va[8], vb[7];
#pragma unroll
    for (int i = 0; i < 8; i++) { const int f = lw * 8 + i; va[i] = (f >> 1) * 20480 + (f & 1) * 1024 + lane * 16; }
#pragma unroll
    for (int i = 0; i < 7; i++) { const int f = lw * 7 + i, j = f >> 1, T = j < 6 ? T0 + odd + j : Ts;
      vb[i] = (j < 6 || seven) ? T * 20480 + (f & 1) * 1024 + lane * 16 : 0x40000000; }
    produce<15, 3, NST, 10>([&](int s, int st) {
      ldsp base = smem + st * STG;
#pragma unroll
      for (int i = 0; i < 8; i++) dma<0>(ra, base + (lw * 8 + i) * 1024, va[i], s * 2048);
#pragma unroll
      for (int i = 0; i < 7; i++) dma<0>(rb, base + 16384 + (lw * 7 + i) * 1024, vb[i], s * 2048);
    });
  } else {
    const int g = lane >> 4, kb0 = (T0 + odd) >> 1;
    v4u bp[3]; v2u bs = {0, 0};
#pragma unroll
    for (int u = 0; u < 3; u++) bp[u] = *(const v4u*)(p.b1 + ((kb0 + u) * 32 + g * 8) * 2);
    if (seven) bs = *(const v2u*)(p.b1 + (Ts * 16 + g * 4) * 2);
    v4f acc[2][7];
    consume<7, false, false, false, 10, STG, NST, 16384>(smem, w, lane, acc);
#pragma unroll
    for (int mi = 0; mi < 2; mi++) {
      const v2f st = *(LDS v2f*)(smem + XOFF + 512 + (w * 32 + mi * 16 + (lane & 15)) * 8);
      char* dst = p.ws + OFF_HH + (size_t)(tg * 8 + w * 2 + mi) * 55296;
      unsigned r[14];
#pragma unroll
      for (int j = 0; j < 7; j++) {
        const v4f cs = *(LDS v4f*)(smem + XOFF + (j * 16 + g * 4) * 4);
        const unsigned bb0 = j < 6 ? bp[j >> 1][(j & 1) * 2] : bs[0], bb1 = j < 6 ? bp[j >> 1][(j & 1) * 2 + 1] : bs[1];
        const float bias[4] = {lo(bb0), hi(bb0), lo(bb1), hi(bb1)};
        float z[4];
#pragma unroll
        for (int e = 0; e < 4; e++) { const float v = fmaxf(acc[mi][j][e] * st[1] - st[0] * cs[e] + bias[e], 0.f); z[e] = v * v; }
        r[2 * j] = pk(z[0], z[1]); r[2 * j + 1] = pk(z[2], z[3]);
      }
#pragma unroll
      for (int u = 0; u < 3; u++) *(v4u*)(dst + (kb0 + u) * 1024 + lane * 16) = v4u{r[4 * u], r[4 * u + 1], r[4 * u + 2], r[4 * u + 3]};
      if (seven) *(v2u*)(dst + (Ts >> 1) * 1024 + ((odd * 2 + (g >> 1)) * 16 + (lane & 15)) * 16 + (g & 1) * 8) = v2u{r[12], r[13]};
    }
  }
#endif
}

__global__ __launch_bounds__(384) void k_g4(Params p) {
#if defined(__HIP_DEVICE_COMPILE__)
  extern __shared__ __attribute__((aligned(16))) char smem_[];
  ldsp smem = (ldsp)smem_;
  const int lane = threadIdx.x & 63, w = __builtin_amdgcn_readfirstlane(threadIdx.x >> 6), b = blockIdx.x;
  const int tg = (b & 7) * 2 + (b >> 7), c = (b >> 3) & 15, mh = c >> 3, nt = c & 7, odd = nt & 1;
  constexpr int STG = 18 * 1024, NST = 5;
  if (w >= 4) {
    const int lw = w - 4;
    rsrc_t ra = mkrsrc(p.ws + OFF_HH + (size_t)(tg * 8 + mh * 4) * 55296, 4 * 55296), rb = mkrsrc(p.ws + OFF_W2F, 40 * 55296);
    int va[4], vb[5];
#pragma unroll
    for (int i = 0; i < 4; i++) { const int f = lw * 4 + i; va[i] = (f >> 1) * 55296 + (f & 1) * 1024 + lane * 16; }
#pragma unroll
    for (int i = 0; i < 5; i++) { const int f = lw * 5 + i, j = f >> 1, T = 5 * nt + (odd ? (j + 1) % 5 : j); vb[i] = T * 55296 + (f & 1) * 1024 + lane * 16; }
    produce<9, 4, NST, 27>([&](int s, int st) {
      ldsp base = smem + st * STG;
#pragma unroll
      for (int i = 0; i < 4; i++) dma<0>(ra, base + (lw * 4 + i) * 1024, va[i], s * 2048);
#pragma unroll
      for (int i = 0; i < 5; i++) dma<0>(rb, base + 8192 + (lw * 5 + i) * 1024, vb[i], s * 2048);
    });
    bar();
  } else {
    const int g = lane >> 4, tb = tg * 8 + mh * 4 + (w & 1) * 2 + (w >> 1), t = tb * 16 + (lane & 15);
    const int kb0 = 5 * (nt >> 1), cp = (kb0 + odd * 3) * 32 + g * 8, cs = (kb0 + 2) * 32 + odd * 16 + g * 4;
    const char* hs = p.ws + OFF_HF + (size_t)tb * 20480;
    const v4u h0 = *(const v4u*)(hs + (kb0 + odd * 3) * 1024 + lane * 16), h1 = *(const v4u*)(hs + (kb0 + odd * 3 + 1) * 1024 + lane * 16);
    const v2u h2 = *(const v2u*)(hs + (kb0 + 2) * 1024 + ((odd * 2 + (g >> 1)) * 16 + (lane & 15)) * 16 + (g & 1) * 8);
    const v4u c0 = *(const v4u*)(p.b2 + cp * 2), c1 = *(const v4u*)(p.b2 + cp * 2 + 64);
    const v2u c2 = *(const v2u*)(p.b2 + cs * 2);
    v4f acc[2][5], o[5];
    consume<5, true, false, false, 27, STG, NST, 8192>(smem, w, lane, acc);
    pair_reduce(smem, w, lane, acc, o);
#define FIN(a, e, h, c) pk(a[e] + lo(h) + lo(c), a[e + 1] + hi(h) + hi(c))
    char* dst = p.out + (size_t)t * 1280;
    *(v4u*)(dst + cp * 2) = v4u{FIN(o[0], 0, h0[0], c0[0]), FIN(o[0], 2, h0[1], c0[1]), FIN(o[1], 0, h0[2], c0[2]), FIN(o[1], 2, h0[3], c0[3])};
    *(v4u*)(dst + cp * 2 + 64) = v4u{FIN(o[2], 0, h1[0], c1[0]), FIN(o[2], 2, h1[1], c1[1]), FIN(o[3], 0, h1[2], c1[2]), FIN(o[3], 2, h1[3], c1[3])};
    *(v2u*)(dst + cs * 2) = v2u{FIN(o[4], 0, h2[0], c2[0]), FIN(o[4], 2, h2[1], c2[1])};
#undef FIN
  }
#endif
}

extern "C" void kernel_launch(void* const* d_in, const int* in_sizes, int n_in, void* d_out, int out_size, void* d_ws, size_t ws_size,
                              hipStream_t stream) {
  Params p;
  p.x = (const char*)d_in[0]; p.cr = (const char*)d_in[1]; p.ci = (const char*)d_in[2]; p.dr = (const char*)d_in[3]; p.di = (const char*)d_in[4];
  p.w1 = (const char*)d_in[6]; p.b1 = (const char*)d_in[7]; p.w2 = (const char*)d_in[8]; p.b2 = (const char*)d_in[9];
  p.out = (char*)d_out; p.ws = (char*)d_ws;
  constexpr int L0 = 16384 + 5 * 4096 + 1024, L1 = 26 * 1024 * 4, L2 = 18 * 1024 * 5, L3 = 30 * 1024 * 4 + 512 + 1024, L4 = 18 * 1024 * 5;
  (void)hipFuncSetAttribute((const void*)k_g1, hipFuncAttributeMaxDynamicSharedMemorySize, L1);
  (void)hipFuncSetAttribute((const void*)k_g2, hipFuncAttributeMaxDynamicSharedMemorySize, L2);
  (void)hipFuncSetAttribute((const void*)k_g3, hipFuncAttributeMaxDynamicSharedMemorySize, L3);
  (void)hipFuncSetAttribute((const void*)k_g4, hipFuncAttributeMaxDynamicSharedMemorySize, L4);
  k_prep<<<256, 512, L0, stream>>>(p);
  k_g1<<<256, 384, L1, stream>>>(p);
  k_g2<<<256, 384, L2, stream>>>(p);
  k_g3<<<256, 384, L3, stream>>>(p);
  k_g4<<<256, 384, L4, stream>>>(p);
}
```

```cpp
#include <hip/hip_runtime.h>
#include <hip/hip_bf16.h>
#include <stdint.h>

#define LDS __attribute__((address_space(3)))
typedef LDS char* ldsp;
typedef short v4s __attribute__((ext_vector_type(4)));
typedef short v8s __attribute__((ext_vector_type(8)));
typedef __bf16 v8b __attribute__((ext_vector_type(8)));
typedef __bf16 v2b __attribute__((ext_vector_type(2)));
typedef float v4f __attribute__((ext_vector_type(4)));
typedef float v2f __attribute__((ext_vector_type(2)));
typedef unsigned v4u __attribute__((ext_vector_type(4)));
typedef unsigned v2u __attribute__((ext_vector_type(2)));

constexpr int S_ = 2048, D_ = 640, F_ = 1728;
constexpr size_t OFF_LNF = 0;
constexpr size_t OFF_PQF = OFF_LNF + 40 * 64 * 1024;
constexpr size_t OFF_HF  = OFF_PQF + 128 * 40 * 1024;
constexpr size_t OFF_HH  = OFF_HF + 128 * 20 * 1024;
constexpr size_t OFF_W1F = OFF_HH + 128 * 54 * 1024;
constexpr size_t OFF_W2F = OFF_W1F + 108 * 20 * 1024;
constexpr size_t OFF_CS  = OFF_W2F + 40 * 54 * 1024;
constexpr size_t OFF_ST  = OFF_CS + 20 * 1728 * 4;

constexpr size_t OFF_DBG = 64u << 20;
#define TS(K, I) do { if (lane == 0) ((unsigned long long*)(p.ws + OFF_DBG))[((K) * 256 + b) * 8 + (I)] = __builtin_amdgcn_s_memrealtime(); } while (0)
struct Params {
  const char* x; const char* cr; const char* ci; const char* dr; const char* di;
  const char* w1; const char* b1; const char* w2; const char* b2;
  char* out; char* ws;
};

#if defined(__HIP_DEVICE_COMPILE__)
typedef __amdgpu_buffer_rsrc_t rsrc_t;
__device__ __forceinline__ rsrc_t mkrsrc(const void* p, unsigned bytes) { return __builtin_amdgcn_make_buffer_rsrc((void*)p, 0, bytes, 0x00020000); }
template <int AUX> __device__ __forceinline__ void dma(rsrc_t r, ldsp dst, int voff, int soff) {
  __builtin_amdgcn_raw_ptr_buffer_load_lds(r, (LDS void*)dst, 16, voff, soff, 0, AUX);
}
template <int N> __device__ __forceinline__ void waitv() { asm volatile("s_waitcnt vmcnt(%0)" ::"n"(N) : "memory"); }
__device__ __forceinline__ void bar() { __builtin_amdgcn_s_barrier(); }
__device__ __forceinline__ void waitl0() { __builtin_amdgcn_sched_barrier(0); __builtin_amdgcn_s_waitcnt(0xC07F); __builtin_amdgcn_sched_barrier(0); }
__device__ __forceinline__ v4f mfma(v8s a, v8s b, v4f c) {
  return __builtin_amdgcn_mfma_f32_16x16x32_bf16(__builtin_bit_cast(v8b, a), __builtin_bit_cast(v8b, b), c, 0, 0, 0);
}
__device__ __forceinline__ unsigned pk(float a, float b) { v2b r = {(__bf16)a, (__bf16)b}; return __builtin_bit_cast(unsigned, r); }
__device__ __forceinline__ float lo(unsigned u) { return __builtin_bit_cast(float, u << 16); }
__device__ __forceinline__ float hi(unsigned u) { return __builtin_bit_cast(float, u & 0xffff0000u); }
template <int NM, int ND> __device__ __forceinline__ void ilv() {
#pragma unroll
  for (int i = 0; i < NM; i++) { __builtin_amdgcn_sched_group_barrier(0x8, 1, 0); if (i < ND) __builtin_amdgcn_sched_group_barrier(0x100, 1, 0); }
}
__device__ __forceinline__ int piece_col(int T, int p, bool split) { return split ? 16 * T + 4 * p : 32 * (T >> 1) + 8 * p + 4 * (T & 1); }
__device__ __forceinline__ bool split640(int T) { return (T >> 1) % 5 == 2; }
__device__ __forceinline__ bool split1728(int T) { return (T >> 1) % 7 == 3 && T < 84; }

template <int NJ, bool KSPLIT, bool ASWZ, bool BSWZ, int NSTEPS, int STG, int NST, int BOFF>
__device__ __forceinline__ void consume(ldsp smem, int w, int lane, v4f (&acc)[2][NJ], Params& p, int b, int K) {
  const int g = lane >> 4, r = lane & 15;
  const int kh0 = KSPLIT ? (w >> 1) : 0;
  const int rb = KSPLIT ? (w & 1) * 2 : w * 2;
  const int sw = ((kh0 * 4 + g) ^ (r >> 1)) * 16;
  const int a0 = ASWZ ? (rb * 16 + r) * 128 + sw : (rb * 2 + kh0) * 1024 + lane * 16;
  const int a1 = ASWZ ? a0 ^ 64 : a0 + 1024;
  const int b0 = BOFF + (BSWZ ? r * 128 + sw : kh0 * 1024 + lane * 16);
  const int b1 = BSWZ ? b0 ^ 64 : b0 + 1024;
  v8s pa[2], pb[NJ], qa[2], qb[NJ];
#pragma unroll
  for (int i = 0; i < 2; i++) { qa[i] = v8s{0, 0, 0, 0, 0, 0, 0, 0};
#pragma unroll
    for (int j = 0; j < NJ; j++) acc[i][j] = v4f{0, 0, 0, 0}; }
#pragma unroll
  for (int j = 0; j < NJ; j++) qb[j] = v8s{0, 0, 0, 0, 0, 0, 0, 0};
#define RD(A, B, ao, bo) { _Pragma("unroll") for (int i = 0; i < 2; i++) A[i] = *(LDS v8s*)(base + ao + i * 2048); \
                           _Pragma("unroll") for (int j = 0; j < NJ; j++) B[j] = *(LDS v8s*)(base + bo + j * 2048); }
#define MM(A, B) { _Pragma("unroll") for (int j = 0; j < NJ; j++) _Pragma("unroll") for (int i = 0; i < 2; i++) acc[i][j] = mfma(B[j], A[i], acc[i][j]); }
  bar();
  if (w == 0) TS(K, 1);
  int st = 0;
  if (!KSPLIT) {
#pragma unroll 1
    for (int s = 0; s < NSTEPS; s++) {
      ldsp base = smem + st * STG;
      RD(pa, pb, a0, b0); MM(qa, qb); ilv<2 * NJ, NJ + 2>(); waitl0();
      RD(qa, qb, a1, b1); MM(pa, pb); ilv<2 * NJ, NJ + 2>(); waitl0();
      bar();
      st++; if (st == NST) st = 0;
    }
    MM(qa, qb);
  } else {
#pragma unroll 1
    for (int s = 0; s < NSTEPS / 2; s++) {
      ldsp base = smem + st * STG;
      RD(pa, pb, a0, b0); MM(qa, qb); ilv<2 * NJ, NJ + 2>(); waitl0();
      bar();
      st++; if (st == NST) st = 0;
      base = smem + st * STG;
      RD(qa, qb, a0, b0); MM(pa, pb); ilv<2 * NJ, NJ + 2>(); waitl0();
      bar();
      st++; if (st == NST) st = 0;
    }
    if (NSTEPS & 1) {
      ldsp base = smem + st * STG;
      RD(pa, pb, a0, b0); MM(qa, qb); ilv<2 * NJ, NJ + 2>(); waitl0();
      bar();
      MM(pa, pb);
    } else MM(qa, qb);
  }
  if (w == 0) TS(K, 2);
#undef RD
#undef MM
}

template <int PER, int DEP, int NST, int NSTEPS, class FN>
__device__ __forceinline__ void produce(FN issue) {
#pragma unroll
  for (int s = 0; s < DEP; s++) issue(s, s);
  waitv<PER * (DEP - 1)>(); bar();
  int st = DEP % NST;
#pragma unroll 1
  for (int s = DEP; s < NSTEPS; s++) {
    issue(s, st); st++; if (st == NST) st = 0;
    waitv<PER * (DEP - 1)>(); bar();
  }
  if (DEP >= 4) { waitv<PER * 2>(); bar(); }
  if (DEP >= 3) { waitv<PER>(); bar(); }
  waitv<0>(); bar();
  bar();
}

__device__ __forceinline__ void pair_reduce(ldsp smem, int w, int lane, v4f (&acc)[2][5], v4f (&o)[5]) {
  const int keep = w >> 1;
  ldsp mine = smem + w * 5120 + lane * 16, other = smem + (w ^ 2) * 5120 + lane * 16;
#pragma unroll
  for (int j = 0; j < 5; j++) *(LDS v4f*)(mine + j * 1024) = keep ? acc[0][j] : acc[1][j];
  waitl0(); bar();
#pragma unroll
  for (int j = 0; j < 5; j++) o[j] = (keep ? acc[1][j] : acc[0][j]) + *(LDS v4f*)(other + j * 1024);
}
#endif

__global__ __launch_bounds__(512) void k_prep(Params p) {
#if defined(__HIP_DEVICE_COMPILE__)
  extern __shared__ __attribute__((aligned(16))) char smem_[];
  ldsp smem = (ldsp)smem_;
  const int lane = threadIdx.x & 63, w = __builtin_amdgcn_readfirstlane(threadIdx.x >> 6), b = blockIdx.x;
  if (w == 0) TS(0, 0);
  constexpr int RS = 1312;
  const unsigned* xr = (const unsigned*)(p.x + (size_t)(b * 8 + w) * 1280);
  unsigned xv[5];
#pragma unroll
  for (int i = 0; i < 5; i++) xv[i] = xr[lane + 64 * i];
  const int u = b * 5 + w;
  const bool has = w < 5 && u < 1080, isw1 = u < 540;
  ldsp wl = smem + 16384 + w * 4096;
  int kb = 0, cu = 0;
  if (has) {
    const int uu = isw1 ? u : u - 540;
    kb = isw1 ? uu / 27 : uu / 10; cu = isw1 ? uu % 27 : uu % 10;
    const int ld = isw1 ? F_ * 2 : D_ * 2;
    rsrc_t r = mkrsrc(isw1 ? p.w1 : p.w2, D_ * F_ * 2);
#pragma unroll
    for (int i = 0; i < 4; i++) dma<0>(r, wl + i * 1024, (kb * 32 + i * 8 + (lane >> 3)) * ld + cu * 128 + (lane & 7) * 16, 0);
  }
  float s = 0;
#pragma unroll
  for (int i = 0; i < 5; i++) s += lo(xv[i]) + hi(xv[i]);
#pragma unroll
  for (int m = 1; m < 64; m <<= 1) s += __shfl_xor(s, m);
  const float mean = s * (1.f / 640.f);
  float q = 0;
#pragma unroll
  for (int i = 0; i < 5; i++) { const float a = lo(xv[i]) - mean, c = hi(xv[i]) - mean; q += a * a + c * c; }
#pragma unroll
  for (int m = 1; m < 64; m <<= 1) q += __shfl_xor(q, m);
  const float rs = 1.0f / sqrtf(q * (1.f / 640.f) + 1e-5f);
#pragma unroll
  for (int i = 0; i < 5; i++) *(LDS unsigned*)(smem + w * RS + (lane + 64 * i) * 4) = pk((lo(xv[i]) - mean) * rs, (hi(xv[i]) - mean) * rs);
  waitl0(); bar();
  if (w == 0) TS(0, 1);
  const int g = lane >> 4, qq = (lane & 15) >> 2, pp = lane & 3;
  if (w < 5) {
#pragma unroll
    for (int i = 0; i < 2; i++) {
      const int T = (w * 2 + i) * 4 + g;
      ldsp a = smem + qq * RS + piece_col(T, pp, split640(T)) * 2;
      const v4s v0 = __builtin_amdgcn_ds_read_tr16_b64_v4i16((LDS v4s*)a), v1 = __builtin_amdgcn_ds_read_tr16_b64_v4i16((LDS v4s*)(a + 4 * RS));
      *(v8s*)(p.ws + OFF_LNF + (size_t)T * 65536 + (b >> 2) * 1024 + (b & 3) * 256 + (lane & 15) * 16) = __builtin_shufflevector(v0, v1, 0, 1, 2, 3, 4, 5, 6, 7);
    }
  }
  if (has) {
    waitv<0>();
#pragma unroll
    for (int t = 0; t < 4; t++) {
      const int T = cu * 4 + t;
      const bool sp = isw1 ? split1728(T) : split640(T);
      ldsp a = wl + (g * 8 + qq) * 128 + (piece_col(T, pp, sp) - cu * 64) * 2;
      const v4s v0 = __builtin_amdgcn_ds_read_tr16_b64_v4i16((LDS v4s*)a), v1 = __builtin_amdgcn_ds_read_tr16_b64_v4i16((LDS v4s*)(a + 512));
      const v8s v = __builtin_shufflevector(v0, v1, 0, 1, 2, 3, 4, 5, 6, 7);
      char* dst = p.ws + (isw1 ? OFF_W1F + (size_t)T * 20480 : OFF_W2F + (size_t)T * 55296) + kb * 1024 + lane * 16;
      *(v8s*)dst = v;
      if (isw1) {
        const v4u uv = __builtin_bit_cast(v4u, v);
        float c = (lo(uv[0]) + hi(uv[0])) + (lo(uv[1]) + hi(uv[1])) + (lo(uv[2]) + hi(uv[2])) + (lo(uv[3]) + hi(uv[3]));
        c += __shfl_xor(c, 16); c += __shfl_xor(c, 32);
        if (lane < 16) ((float*)(p.ws + OFF_CS))[kb * F_ + T * 16 + lane] = c;
      }
    }
  }
  if (w == 0) { TS(0, 2); waitv<0>(); TS(0, 3); }
#endif
}

__global__ __launch_bounds__(384) void k_g1(Params p) {
#if defined(__HIP_DEVICE_COMPILE__)
  extern __shared__ __attribute__((aligned(16))) char smem_[];
  ldsp smem = (ldsp)smem_;
  const int lane = threadIdx.x & 63, w = __builtin_amdgcn_readfirstlane(threadIdx.x >> 6), b = blockIdx.x;
  const int tg = (b & 7) * 2 + (b >> 7), c = (b >> 3) & 15, pq = c >> 3, nt = c & 7, odd = nt & 1;
  constexpr int STG = 26 * 1024, NST = 4;
  if (w == 0) TS(1, 0);
  if (w >= 4) {
    const int lw = w - 4;
    rsrc_t ra = mkrsrc((pq ? p.ci : p.cr) + (size_t)tg * 128 * 4096, 128 * 4096), rb = mkrsrc(p.ws + OFF_LNF, 40 * 65536);
    int va[8];
#pragma unroll
    for (int i = 0; i < 8; i++) { const int row = (lw * 8 + i) * 8 + (lane >> 3); va[i] = row * 4096 + ((lane & 7) ^ ((row >> 1) & 7)) * 16; }
    int vb[5];
#pragma unroll
    for (int i = 0; i < 5; i++) { const int f = lw * 5 + i, j = f >> 1, T = 5 * nt + (odd ? (j + 1) % 5 : j); vb[i] = T * 65536 + (f & 1) * 1024 + lane * 16; }
    produce<13, 3, NST, 32>([&](int s, int st) {
      ldsp base = smem + st * STG;
#pragma unroll
      for (int i = 0; i < 8; i++) dma<0>(ra, base + (lw * 8 + i) * 1024, va[i], s * 128);
#pragma unroll
      for (int i = 0; i < 5; i++) dma<0>(rb, base + 16384 + (lw * 5 + i) * 1024, vb[i], s * 2048);
    });
  } else {
    v4f acc[2][5];
    consume<5, false, true, false, 32, STG, NST, 16384>(smem, w, lane, acc, p, b, 1);
    const float sg = pq ? -1.f : 1.f;
    const int g = lane >> 4, kb0 = pq * 20 + 5 * (nt >> 1);
#pragma unroll
    for (int mi = 0; mi < 2; mi++) {
      char* dst = p.ws + OFF_PQF + (size_t)(tg * 8 + w * 2 + mi) * 40960;
#pragma unroll
      for (int u = 0; u < 2; u++) {
        const v4f a = acc[mi][2 * u] * sg, c2 = acc[mi][2 * u + 1] * sg;
        *(v4u*)(dst + (kb0 + odd * 3 + u) * 1024 + lane * 16) = v4u{pk(a[0], a[1]), pk(a[2], a[3]), pk(c2[0], c2[1]), pk(c2[2], c2[3])};
      }
      const v4f a = acc[mi][4] * sg;
      *(v2u*)(dst + (kb0 + 2) * 1024 + ((odd * 2 + (g >> 1)) * 16 + (lane & 15)) * 16 + (g & 1) * 8) = v2u{pk(a[0], a[1]), pk(a[2], a[3])};
    }
    if (w == 0) { TS(1, 3); waitv<0>(); TS(1, 4); }
  }
#endif
}

__global__ __launch_bounds__(384) void k_g2(Params p) {
#if defined(__HIP_DEVICE_COMPILE__)
  extern __shared__ __attribute__((aligned(16))) char smem_[];
  ldsp smem = (ldsp)smem_;
  const int lane = threadIdx.x & 63, w = __builtin_amdgcn_readfirstlane(threadIdx.x >> 6), b = blockIdx.x;
  const int tg = (b & 7) * 2 + (b >> 7), c = (b >> 3) & 15, mh = c >> 3, nt = c & 7, odd = nt & 1;
  constexpr int STG = 18 * 1024, NST = 5;
  if (w == 0) TS(2, 0);
  if (w >= 4) {
    const int lw = w - 4;
    rsrc_t ra = mkrsrc(p.ws + OFF_PQF + (size_t)(tg * 8 + mh * 4) * 40960, 4 * 40960), rr = mkrsrc(p.dr, D_ * D_ * 2), ri = mkrsrc(p.di, D_ * D_ * 2);
    int va[4], vb[5];
#pragma unroll
    for (int i = 0; i < 4; i++) { const int f = lw * 4 + i; va[i] = (f >> 1) * 40960 + (f & 1) * 1024 + lane * 16; }
#pragma unroll
    for (int i = 0; i < 5; i++) {
      const int row = (lw * 5 + i) * 8 + (lane >> 3), j = row >> 4, T = 5 * nt + (odd ? (j + 1) % 5 : j), n = row & 15;
      vb[i] = (piece_col(T, n >> 2, j == 4) + (n & 3)) * 1280 + ((lane & 7) ^ ((row >> 1) & 7)) * 16;
    }
    produce<9, 4, NST, 20>([&](int s, int st) {
      ldsp base = smem + st * STG;
#pragma unroll
      for (int i = 0; i < 4; i++) dma<0>(ra, base + (lw * 4 + i) * 1024, va[i], s * 2048);
      if (s < 10) {
#pragma unroll
        for (int i = 0; i < 5; i++) dma<0>(rr, base + 8192 + (lw * 5 + i) * 1024, vb[i], s * 128);
      } else {
#pragma unroll
        for (int i = 0; i < 5; i++) dma<0>(ri, base + 8192 + (lw * 5 + i) * 1024, vb[i], (s - 10) * 128);
      }
    });
    bar();
  } else {
    const int g = lane >> 4, tb = tg * 8 + mh * 4 + (w & 1) * 2 + (w >> 1), t = tb * 16 + (lane & 15);
    const int kb0 = 5 * (nt >> 1), cp = (kb0 + odd * 3) * 32 + g * 8, cs = (kb0 + 2) * 32 + odd * 16 + g * 4;
    const v4u x0 = *(const v4u*)(p.x + (size_t)t * 1280 + cp * 2), x1 = *(const v4u*)(p.x + (size_t)t * 1280 + cp * 2 + 64);
    const v2u x2 = *(const v2u*)(p.x + (size_t)t * 1280 + cs * 2);
    v4f acc[2][5], o[5];
    consume<5, true, false, true, 20, STG, NST, 8192>(smem, w, lane, acc, p, b, 2);
    pair_reduce(smem, w, lane, acc, o);
    const v4u h0 = {pk(o[0][0] + lo(x0[0]), o[0][1] + hi(x0[0])), pk(o[0][2] + lo(x0[1]), o[0][3] + hi(x0[1])),
                    pk(o[1][0] + lo(x0[2]), o[1][1] + hi(x0[2])), pk(o[1][2] + lo(x0[3]), o[1][3] + hi(x0[3]))};
    const v4u h1 = {pk(o[2][0] + lo(x1[0]), o[2][1] + hi(x1[0])), pk(o[2][2] + lo(x1[1]), o[2][3] + hi(x1[1])),
                    pk(o[3][0] + lo(x1[2]), o[3][1] + hi(x1[2])), pk(o[3][2] + lo(x1[3]), o[3][3] + hi(x1[3]))};
    const v2u h2 = {pk(o[4][0] + lo(x2[0]), o[4][1] + hi(x2[0])), pk(o[4][2] + lo(x2[1]), o[4][3] + hi(x2[1]))};
    char* dst = p.ws + OFF_HF + (size_t)tb * 20480;
    *(v4u*)(dst + (kb0 + odd * 3) * 1024 + lane * 16) = h0;
    *(v4u*)(dst + (kb0 + odd * 3 + 1) * 1024 + lane * 16) = h1;
    *(v2u*)(dst + (kb0 + 2) * 1024 + ((odd * 2 + (g >> 1)) * 16 + (lane & 15)) * 16 + (g & 1) * 8) = h2;
    float s = lo(h2[0]) + hi(h2[0]) + lo(h2[1]) + hi(h2[1]);
#pragma unroll
    for (int i = 0; i < 4; i++) s += lo(h0[i]) + hi(h0[i]) + lo(h1[i]) + hi(h1[i]);
    s += __shfl_xor(s, 16); s += __shfl_xor(s, 32);
    const float m = s * (1.f / 80.f);
    float q = 0;
#pragma unroll
    for (int i = 0; i < 4; i++) { const float a = lo(h0[i]) - m, c2 = hi(h0[i]) - m, d = lo(h1[i]) - m, e = hi(h1[i]) - m; q += a * a + c2 * c2 + d * d + e * e; }
#pragma unroll
    for (int i = 0; i < 2; i++) { const float a = lo(h2[i]) - m, c2 = hi(h2[i]) - m; q += a * a + c2 * c2; }
    q += __shfl_xor(q, 16); q += __shfl_xor(q, 32);
    if (lane < 16) *(v2f*)(p.ws + OFF_ST + (size_t)t * 64 + nt * 8) = v2f{m, q};
    if (w == 0) { TS(2, 3); waitv<0>(); TS(2, 4); }
  }
#endif
}

__global__ __launch_bounds__(384) void k_g3(Params p) {
#if defined(__HIP_DEVICE_COMPILE__)
  extern __shared__ __attribute__((aligned(16))) char smem_[];
  ldsp smem = (ldsp)smem_;
  const int lane = threadIdx.x & 63, w = __builtin_amdgcn_readfirstlane(threadIdx.x >> 6), b = blockIdx.x;
  const int tg = (b & 7) * 2 + (b >> 7), c = (b >> 3) & 15;
  const bool seven = c < 12;
  const int odd = seven ? c & 1 : 0, T0 = seven ? 7 * c : 84 + 6 * (c - 12);
  const int Ts = odd ? T0 : T0 + 6;
  constexpr int STG = 30 * 1024, NST = 4, XOFF = STG * NST;
  if (w == 0) TS(3, 0);
  if (w >= 4) {
    const int lw = w - 4;
    rsrc_t ra = mkrsrc(p.ws + OFF_HF + (size_t)tg * 8 * 20480, 8 * 20480), rb = mkrsrc(p.ws + OFF_W1F, 108 * 20480);
    const int i2 = lw * 64 + lane;
    {
      const int j = i2 >> 4, T = j < 6 ? T0 + odd + j : Ts;
      float cs = 0;
      if (i2 < (seven ? 112 : 96)) {
        const float* src = (const float*)(p.ws + OFF_CS) + T * 16 + (i2 & 15);
#pragma unroll
        for (int k = 0; k < 20; k++) cs += src[k * F_];
      }
      const v4f* st = (const v4f*)(p.ws + OFF_ST + (size_t)(tg * 128 + i2) * 64);
      const v4f s0 = st[0], s1 = st[1], s2 = st[2], s3 = st[3];
      const float m = (s0[0] + s0[2] + s1[0] + s1[2] + s2[0] + s2[2] + s3[0] + s3[2]) * 0.125f;
      float q = s0[1] + s0[3] + s1[1] + s1[3] + s2[1] + s2[3] + s3[1] + s3[3];
      const float d0 = s0[0] - m, d1 = s0[2] - m, d2 = s1[0] - m, d3 = s1[2] - m, d4 = s2[0] - m, d5 = s2[2] - m, d6 = s3[0] - m, d7 = s3[2] - m;
      q += 80.f * (d0 * d0 + d1 * d1 + d2 * d2 + d3 * d3 + d4 * d4 + d5 * d5 + d6 * d6 + d7 * d7);
      const float rs = 1.0f / sqrtf(q * (1.f / 640.f) + 1e-5f);
      *(LDS float*)(smem + XOFF + i2 * 4) = cs;
      *(LDS v2f*)(smem + XOFF + 512 + i2 * 8) = v2f{m * rs, rs};
      waitl0();
    }
    int va[8], vb[7];
#pragma unroll
    for (int i = 0; i < 8; i++) { const int f = lw * 8 + i; va[i] = (f >> 1) * 20480 + (f & 1) * 1024 + lane * 16; }
#pragma unroll
    for (int i = 0; i < 7; i++) { const int f = lw * 7 + i, j = f >> 1, T = j < 6 ? T0 + odd + j : Ts;
      vb[i] = (j < 6 || seven) ? T * 20480 + (f & 1) * 1024 + lane * 16 : 0x40000000; }
    produce<15, 3, NST, 10>([&](int s, int st) {
      ldsp base = smem + st * STG;
#pragma unroll
      for (int i = 0; i < 8; i++) dma<0>(ra, base + (lw * 8 + i) * 1024, va[i], s * 2048);
#pragma unroll
      for (int i = 0; i < 7; i++) dma<0>(rb, base + 16384 + (lw * 7 + i) * 1024, vb[i], s * 2048);
    });
  } else {
    const int g = lane >> 4, kb0 = (T0 + odd) >> 1;
    v4u bp[3]; v2u bs = {0, 0};
#pragma unroll
    for (int u = 0; u < 3; u++) bp[u] = *(const v4u*)(p.b1 + ((kb0 + u) * 32 + g * 8) * 2);
    if (seven) bs = *(const v2u*)(p.b1 + (Ts * 16 + g * 4) * 2);
    v4f acc[2][7];
    consume<7, false, false, false, 10, STG, NST, 16384>(smem, w, lane, acc, p, b, 3);
#pragma unroll
    for (int mi = 0; mi < 2; mi++) {
      const v2f st = *(LDS v2f*)(smem + XOFF + 512 + (w * 32 + mi * 16 + (lane & 15)) * 8);
      char* dst = p.ws + OFF_HH + (size_t)(tg * 8 + w * 2 + mi) * 55296;
      unsigned r[14];
#pragma unroll
      for (int j = 0; j < 7; j++) {
        const v4f cs = *(LDS v4f*)(smem + XOFF + (j * 16 + g * 4) * 4);
        const unsigned bb0 = j < 6 ? bp[j >> 1][(j & 1) * 2] : bs[0], bb1 = j < 6 ? bp[j >> 1][(j & 1) * 2 + 1] : bs[1];
        const float bias[4] = {lo(bb0), hi(bb0), lo(bb1), hi(bb1)};
        float z[4];
#pragma unroll
        for (int e = 0; e < 4; e++) { const float v = fmaxf(acc[mi][j][e] * st[1] - st[0] * cs[e] + bias[e], 0.f); z[e] = v * v; }
        r[2 * j] = pk(z[0], z[1]); r[2 * j + 1] = pk(z[2], z[3]);
      }
#pragma unroll
      for (int u = 0; u < 3; u++) *(v4u*)(dst + (kb0 + u) * 1024 + lane * 16) = v4u{r[4 * u], r[4 * u + 1], r[4 * u + 2], r[4 * u + 3]};
      if (seven) *(v2u*)(dst + (Ts >> 1) * 1024 + ((odd * 2 + (g >> 1)) * 16 + (lane & 15)) * 16 + (g & 1) * 8) = v2u{r[12], r[13]};
    }
    if (w == 0) { TS(3, 3); waitv<0>(); TS(3, 4); }
  }
#endif
}

__global__ __launch_bounds__(384) void k_g4(Params p) {
#if defined(__HIP_DEVICE_COMPILE__)
  extern __shared__ __attribute__((aligned(16))) char smem_[];
  ldsp smem = (ldsp)smem_;
  const int lane = threadIdx.x & 63, w = __builtin_amdgcn_readfirstlane(threadIdx.x >> 6), b = blockIdx.x;
  const int tg = (b & 7) * 2 + (b >> 7), c = (b >> 3) & 15, mh = c >> 3, nt = c & 7, odd = nt & 1;
  constexpr int STG = 18 * 1024, NST = 5;
  if (w == 0) TS(4, 0);
  if (w >= 4) {
    const int lw = w - 4;
    rsrc_t ra = mkrsrc(p.ws + OFF_HH + (size_t)(tg * 8 + mh * 4) * 55296, 4 * 55296), rb = mkrsrc(p.ws + OFF_W2F, 40 * 55296);
    int va[4], vb[5];
#pragma unroll
    for (int i = 0; i < 4; i++) { const int f = lw * 4 + i; va[i] = (f >> 1) * 55296 + (f & 1) * 1024 + lane * 16; }
#pragma unroll
    for (int i = 0; i < 5; i++) { const int f = lw * 5 + i, j = f >> 1, T = 5 * nt + (odd ? (j + 1) % 5 : j); vb[i] = T * 55296 + (f & 1) * 1024 + lane * 16; }
    produce<9, 4, NST, 27>([&](int s, int st) {
      ldsp base = smem + st * STG;
#pragma unroll
      for (int i = 0; i < 4; i++) dma<0>(ra, base + (lw * 4 + i) * 1024, va[i], s * 2048);
#pragma unroll
      for (int i = 0; i < 5; i++) dma<0>(rb, base + 8192 + (lw * 5 + i) * 1024, vb[i], s * 2048);
    });
    bar();
  } else {
    const int g = lane >> 4, tb = tg * 8 + mh * 4 + (w & 1) * 2 + (w >> 1), t = tb * 16 + (lane & 15);
    const int kb0 = 5 * (nt >> 1), cp = (kb0 + odd * 3) * 32 + g * 8, cs = (kb0 + 2) * 32 + odd * 16 + g * 4;
    const char* hs = p.ws + OFF_HF + (size_t)tb * 20480;
    const v4u h0 = *(const v4u*)(hs + (kb0 + odd * 3) * 1024 + lane * 16), h1 = *(const v4u*)(hs + (kb0 + odd * 3 + 1) * 1024 + lane * 16);
    const v2u h2 = *(const v2u*)(hs + (kb0 + 2) * 1024 + ((odd * 2 + (g >> 1)) * 16 + (lane & 15)) * 16 + (g & 1) * 8);
    const v4u c0 = *(const v4u*)(p.b2 + cp * 2), c1 = *(const v4u*)(p.b2 + cp * 2 + 64);
    const v2u c2 = *(const v2u*)(p.b2 + cs * 2);
    v4f acc[2][5], o[5];
    consume<5, true, false, false, 27, STG, NST, 8192>(smem, w, lane, acc, p, b, 4);
    pair_reduce(smem, w, lane, acc, o);
#define FIN(a, e, h, c) pk(a[e] + lo(h) + lo(c), a[e + 1] + hi(h) + hi(c))
    char* dst = p.out + (size_t)t * 1280;
    *(v4u*)(dst + cp * 2) = v4u{FIN(o[0], 0, h0[0], c0[0]), FIN(o[0], 2, h0[1], c0[1]), FIN(o[1], 0, h0[2], c0[2]), FIN(o[1], 2, h0[3], c0[3])};
    *(v4u*)(dst + cp * 2 + 64) = v4u{FIN(o[2], 0, h1[0], c1[0]), FIN(o[2], 2, h1[1], c1[1]), FIN(o[3], 0, h1[2], c1[2]), FIN(o[3], 2, h1[3], c1[3])};
    *(v2u*)(dst + cs * 2) = v2u{FIN(o[4], 0, h2[0], c2[0]), FIN(o[4], 2, h2[1], c2[1])};
#undef FIN
    if (w == 0) { TS(4, 3); waitv<0>(); TS(4, 4); }
  }
#endif
}

template <int MODE, int K0, int I0, int K1, int I1, int AMP>
__global__ void k_amp(Params p) {
#if defined(__HIP_DEVICE_COMPILE__)
  const unsigned long long* t = (const unsigned long long*)(p.ws + OFF_DBG);
  const int l = threadIdx.x;
  unsigned long long t0 = __builtin_amdgcn_s_memrealtime();
  long long s = 0, mn = 0x7fffffffffffffffll, mx = 0;
  for (int i = 0; i < 4; i++) { const int b = l * 4 + i; long long a = t[(K0 * 256 + b) * 8 + I0], c = t[(K1 * 256 + b) * 8 + I1]; s += c - a; mn = a < mn ? a : mn; mx = c > mx ? c : mx; }
  for (int m = 1; m < 64; m <<= 1) { s += __shfl_xor(s, m); long long o = __shfl_xor(mn, m); mn = o < mn ? o : mn; o = __shfl_xor(mx, m); mx = o > mx ? o : mx; }
  long long v = MODE == 0 ? s * AMP / 256 : (mx - mn) * AMP;
  if (v > 100000 || v < 0) v = 0;
  while ((long long)(__builtin_amdgcn_s_memrealtime() - t0) < v) __builtin_amdgcn_s_sleep(2);
#endif
}
extern "C" void kernel_launch(void* const* d_in, const int* in_sizes, int n_in, void* d_out, int out_size, void* d_ws, size_t ws_size,
                              hipStream_t stream) {
  Params p;
  p.x = (const char*)d_in[0]; p.cr = (const char*)d_in[1]; p.ci = (const char*)d_in[2]; p.dr = (const char*)d_in[3]; p.di = (const char*)d_in[4];
  p.w1 = (const char*)d_in[6]; p.b1 = (const char*)d_in[7]; p.w2 = (const char*)d_in[8]; p.b2 = (const char*)d_in[9];
  p.out = (char*)d_out; p.ws = (char*)d_ws;
  constexpr int L0 = 16384 + 5 * 4096, L1 = 26 * 1024 * 4, L2 = 18 * 1024 * 5, L3 = 30 * 1024 * 4 + 512 + 1024, L4 = 18 * 1024 * 5;
  (void)hipFuncSetAttribute((const void*)k_g1, hipFuncAttributeMaxDynamicSharedMemorySize, L1);
  (void)hipFuncSetAttribute((const void*)k_g2, hipFuncAttributeMaxDynamicSharedMemorySize, L2);
  (void)hipFuncSetAttribute((const void*)k_g3, hipFuncAttributeMaxDynamicSharedMemorySize, L3);
  (void)hipFuncSetAttribute((const void*)k_g4, hipFuncAttributeMaxDynamicSharedMemorySize, L4);
  k_prep<<<256, 512, L0, stream>>>(p);
  k_g1<<<256, 384, L1, stream>>>(p);
  k_g2<<<256, 384, L2, stream>>>(p);
  k_g3<<<256, 384, L3, stream>>>(p);
  k_g4<<<256, 384, L4, stream>>>(p);
  k_amp<0,0,0,0,0,0><<<1, 64, 0, stream>>>(p);
}
```

```cpp
#include <hip/hip_runtime.h>
#include <hip/hip_bf16.h>
#include <stdint.h>

#define CTH 10
#define USE_INV 0

#define LDS __attribute__((address_space(3)))
typedef LDS char* ldsp;
typedef short v4s __attribute__((ext_vector_type(4)));
typedef short v8s __attribute__((ext_vector_type(8)));
typedef __bf16 v8b __attribute__((ext_vector_type(8)));
typedef __bf16 v2b __attribute__((ext_vector_type(2)));
typedef float v4f __attribute__((ext_vector_type(4)));
typedef float v2f __attribute__((ext_vector_type(2)));
typedef unsigned v4u __attribute__((ext_vector_type(4)));
typedef unsigned v2u __attribute__((ext_vector_type(2)));

constexpr int S_ = 2048, D_ = 640, F_ = 1728;
constexpr int OFF_E   = 0;
constexpr int FSTR    = 128;
constexpr int OFF_F0  = 4096;
constexpr int OFF_FW  = OFF_F0 + 256 * FSTR;
constexpr int OFF_FG  = OFF_FW + 256 * FSTR;
constexpr int OFF_LNF = 256 * 1024;
constexpr int OFF_PQF = OFF_LNF + 40 * 64 * 1024;
constexpr int OFF_HF  = OFF_PQF + 128 * 40 * 1024;
constexpr int OFF_HH  = OFF_HF + 128 * 20 * 1024;
constexpr int OFF_W1F = OFF_HH + 128 * 54 * 1024;
constexpr int OFF_W2F = OFF_W1F + 108 * 20 * 1024;
constexpr int OFF_CS  = OFF_W2F + 40 * 54 * 1024;
constexpr int OFF_ST  = OFF_CS + 20 * 1728 * 4;
constexpr int WS_BYTES = OFF_ST + 8 * 2048 * 8;
constexpr int OOB = 0x40000000;
constexpr int LDS_BYTES = 160 * 1024;

struct Params {
  const char* x; const char* cr; const char* ci; const char* dr; const char* di;
  const char* w1; const char* b1; const char* w2; const char* b2;
  char* out; char* ws;
};

#define TS(i)
#define TSOUT(a, n)

#if defined(__HIP_DEVICE_COMPILE__)
typedef __amdgpu_buffer_rsrc_t rsrc_t;
constexpr int SC1 = 16;
__device__ __forceinline__ rsrc_t mkrsrc(const void* p, unsigned bytes) { return __builtin_amdgcn_make_buffer_rsrc((void*)p, 0, bytes, 0x00020000); }
template <int AUX> __device__ __forceinline__ void dma(rsrc_t r, ldsp dst, int voff, int soff) {
  __builtin_amdgcn_raw_ptr_buffer_load_lds(r, (LDS void*)dst, 16, voff, soff, 0, AUX);
}
__device__ __forceinline__ void st16(rsrc_t r, int off, v4u v) { __builtin_amdgcn_raw_buffer_store_b128(v, r, off, 0, SC1); }
__device__ __forceinline__ void st8(rsrc_t r, int off, v2u v) { __builtin_amdgcn_raw_buffer_store_b64(v, r, off, 0, SC1); }
__device__ __forceinline__ void st4(rsrc_t r, int off, unsigned v) { __builtin_amdgcn_raw_buffer_store_b32(v, r, off, 0, SC1); }
__device__ __forceinline__ v4u ld16(rsrc_t r, int off) { return __builtin_amdgcn_raw_buffer_load_b128(r, off, 0, SC1); }
__device__ __forceinline__ v2u ld8(rsrc_t r, int off, int soff) { return __builtin_amdgcn_raw_buffer_load_b64(r, off, soff, SC1); }
__device__ __forceinline__ unsigned ld4(rsrc_t r, int off, int soff) { return __builtin_amdgcn_raw_buffer_load_b32(r, off, soff, SC1); }
template <int N> __device__ __forceinline__ void waitv() { asm volatile("s_waitcnt vmcnt(%0)" ::"n"(N) : "memory"); }
__device__ __forceinline__ void bar() { asm volatile("" ::: "memory"); __builtin_amdgcn_s_barrier(); asm volatile("" ::: "memory"); }
__device__ __forceinline__ void waitl0() { __builtin_amdgcn_sched_barrier(0); __builtin_amdgcn_s_waitcnt(0xC07F); __builtin_amdgcn_sched_barrier(0); }
__device__ __forceinline__ v4f mfma(v8s a, v8s b, v4f c) {
  return __builtin_amdgcn_mfma_f32_16x16x32_bf16(__builtin_bit_cast(v8b, a), __builtin_bit_cast(v8b, b), c, 0, 0, 0);
}
__device__ __forceinline__ unsigned pk(float a, float b) { v2b r = {(__bf16)a, (__bf16)b}; return __builtin_bit_cast(unsigned, r); }
__device__ __forceinline__ float asf(unsigned u) { return __builtin_bit_cast(float, u); }
__device__ __forceinline__ unsigned asu(float f) { return __builtin_bit_cast(unsigned, f); }
__device__ __forceinline__ float lo(unsigned u) { return asf(u << 16); }
__device__ __forceinline__ float hi(unsigned u) { return asf(u & 0xffff0000u); }
template <int CTRL> __device__ __forceinline__ float dppadd(float v) { return v + asf(__builtin_amdgcn_update_dpp(0, asu(v), CTRL, 0xf, 0xf, true)); }
__device__ __forceinline__ float rowsum(float v) { return dppadd<0x121>(dppadd<0x122>(dppadd<0x124>(dppadd<0x128>(v)))); }
__device__ __forceinline__ float xsum16(float v) { const auto r = __builtin_amdgcn_permlane16_swap(asu(v), asu(v), false, false); return asf(r[0]) + asf(r[1]); }
__device__ __forceinline__ float xsum32(float v) { const auto r = __builtin_amdgcn_permlane32_swap(asu(v), asu(v), false, false); return asf(r[0]) + asf(r[1]); }
template <int NM, int ND> __device__ __forceinline__ void ilv() {
#pragma unroll
  for (int i = 0; i < NM; i++) { __builtin_amdgcn_sched_group_barrier(0x8, 1, 0); if (i < ND) __builtin_amdgcn_sched_group_barrier(0x100, 1, 0); }
}
__device__ __forceinline__ int piece_col(int T, int p, bool split) { return 16 * T + 4 * p + ((4 * p - 12 * (T & 1)) & ((int)split - 1)); }
__device__ __forceinline__ bool split640(int T) { return (T >> 1) % 5 == 2; }
__device__ __forceinline__ bool split1728(int T) { return (T >> 1) % 7 == 3 && T < 84; }
__device__ __forceinline__ unsigned lds_add(ldsp a) { return __hip_atomic_fetch_add((LDS unsigned*)a, 1u, __ATOMIC_RELAXED, __HIP_MEMORY_SCOPE_WORKGROUP); }
__device__ __forceinline__ void lds_wait(ldsp a, unsigned v) { while (*(volatile LDS unsigned*)a != v) __builtin_amdgcn_s_sleep(1); asm volatile("" ::: "memory"); }
constexpr int SPIN_CAP = 1 << 17;
__device__ __forceinline__ bool all_eq(unsigned a, unsigned b2, unsigned tag) { return __builtin_amdgcn_ballot_w64(a != tag || b2 != tag) == 0; }
__device__ __forceinline__ void acquire() {
  asm volatile("" ::: "memory");
}
__device__ __forceinline__ void poll128(rsrc_t r, int off, unsigned tag) {
#pragma unroll 1
  for (int it = 0; it < SPIN_CAP; it++) { asm volatile("" ::: "memory"); if (all_eq(ld4(r, off, 0), ld4(r, off, 64 * FSTR), tag)) break; __builtin_amdgcn_s_sleep(1); }
}
__device__ __forceinline__ void poll16(rsrc_t r, int off, unsigned tag) {
#pragma unroll 1
  for (int it = 0; it < SPIN_CAP; it++) { asm volatile("" ::: "memory"); if ((__builtin_amdgcn_ballot_w64(ld4(r, off, 0) != tag) & 0xffff) == 0) break; __builtin_amdgcn_s_sleep(1); }
}

template <int NJ, bool KSPLIT, bool ASWZ, bool BSWZ, int NSTEPS, int NST, int AOFF, int ASTG, int BOFF, int BSTG>
__device__ __forceinline__ void consume(ldsp smem, int w, int lane, v4f (&acc)[2][NJ]) {
  const int g = lane >> 4, r = lane & 15;
  const int kh0 = KSPLIT ? (w >> 1) : 0;
  const int rb = KSPLIT ? (w & 1) * 2 : w * 2;
  const int sw = ((kh0 * 4 + g) ^ (r >> 1)) * 16;
  const int a0 = AOFF + (ASWZ ? (rb * 16 + r) * 128 + sw : (rb * 2 + kh0) * 1024 + lane * 16);
  const int a1 = ASWZ ? a0 ^ 64 : a0 + 1024;
  const int b0 = BOFF + (BSWZ ? r * 128 + sw : kh0 * 1024 + lane * 16);
  const int b1 = BSWZ ? b0 ^ 64 : b0 + 1024;
  v8s pa[2], pb[NJ], qa[2], qb[NJ];
#pragma unroll
  for (int i = 0; i < 2; i++) { qa[i] = v8s{0, 0, 0, 0, 0, 0, 0, 0};
#pragma unroll
    for (int j = 0; j < NJ; j++) acc[i][j] = v4f{0, 0, 0, 0}; }
#pragma unroll
  for (int j = 0; j < NJ; j++) qb[j] = v8s{0, 0, 0, 0, 0, 0, 0, 0};
#define RD(A, B, ao, bo) { _Pragma("unroll") for (int i = 0; i < 2; i++) A[i] = *(LDS v8s*)(smem + st * ASTG + ao + i * 2048); \
                           _Pragma("unroll") for (int j = 0; j < NJ; j++) B[j] = *(LDS v8s*)(smem + st * BSTG + bo + j * 2048); }
#define MM(A, B) { _Pragma("unroll") for (int j = 0; j < NJ; j++) _Pragma("unroll") for (int i = 0; i < 2; i++) acc[i][j] = mfma(B[j], A[i], acc[i][j]); }
  int st = 0;
  if (!KSPLIT) {
#pragma unroll 1
    for (int s = 0; s < NSTEPS; s++) {
      RD(pa, pb, a0, b0); MM(qa, qb); ilv<2 * NJ, NJ + 2>(); waitl0();
      RD(qa, qb, a1, b1); MM(pa, pb); ilv<2 * NJ, NJ + 2>(); waitl0();
      bar();
      st++; if (st == NST) st = 0;
    }
    MM(qa, qb);
  } else {
#pragma unroll 1
    for (int s = 0; s < NSTEPS / 2; s++) {
      RD(pa, pb, a0, b0); MM(qa, qb); ilv<2 * NJ, NJ + 2>(); waitl0();
      bar();
      st++; if (st == NST) st = 0;
      RD(qa, qb, a0, b0); MM(pa, pb); ilv<2 * NJ, NJ + 2>(); waitl0();
      bar();
      st++; if (st == NST) st = 0;
    }
    if (NSTEPS & 1) {
      RD(pa, pb, a0, b0); MM(qa, qb); ilv<2 * NJ, NJ + 2>(); waitl0();
      bar();
      MM(pa, pb);
    } else MM(qa, qb);
  }
#undef RD
#undef MM
}

template <int LATE, int PER, int DEP, int NST, int NSTEPS, int XTRA, int NNEXT, class FL, class FE, class FX, class FM, class FN>
__device__ __forceinline__ void produce(FL late, FE early, FX extra, FM mid, FN next) {
#pragma unroll
  for (int s = 0; s < DEP; s++) late(s, s);
  extra();
  waitv<LATE * (DEP - 1) + XTRA>(); bar();
  early(DEP, DEP % NST); late(DEP, DEP % NST);
  waitv<LATE * (DEP - 2) + XTRA + PER>(); bar();
  if (DEP == 4) { early(DEP + 1, (DEP + 1) % NST); late(DEP + 1, (DEP + 1) % NST); waitv<LATE * (DEP - 3) + XTRA + 2 * PER>(); bar(); }
  early(2 * DEP - 2, (2 * DEP - 2) % NST); late(2 * DEP - 2, (2 * DEP - 2) % NST);
  waitv<PER * (DEP - 1)>(); bar();
  mid();
  int st = (2 * DEP - 1) % NST;
#pragma unroll 1
  for (int s = 2 * DEP - 1; s < NSTEPS; s++) {
    early(s, st); late(s, st); st++; if (st == NST) st = 0;
    waitv<PER * (DEP - 1)>(); bar();
  }
  next();
  if (DEP == 4) { waitv<PER * 2 + NNEXT>(); bar(); }
  waitv<PER + NNEXT>(); bar();
  waitv<NNEXT>(); bar();
  bar();
}

__device__ __forceinline__ void pair_reduce(ldsp buf, int w, int lane, v4f (&acc)[2][5], v4f (&o)[5]) {
  const int keep = w >> 1;
  ldsp mine = buf + w * 5120 + lane * 16, other = buf + (w ^ 2) * 5120 + lane * 16;
#pragma unroll
  for (int j = 0; j < 5; j++) *(LDS v4f*)(mine + j * 1024) = keep ? acc[0][j] : acc[1][j];
  waitl0(); bar();
#pragma unroll
  for (int j = 0; j < 5; j++) o[j] = (keep ? acc[1][j] : acc[0][j]) + *(LDS v4f*)(other + j * 1024);
}
#endif

constexpr int L1A = 0, L1B = 64 * 1024, L2A = 0, L2B = 104 * 1024, L3B = 40 * 1024, L3A = 96 * 1024, L3X = 38 * 1024, L4B = 0, L4A = 50 * 1024;
constexpr int RS = 1312;
constexpr int L0R = 104 * 1024, L0C = L0R + 8 * RS, L0W = 115 * 1024, L0D = 136 * 1024;

__global__ __launch_bounds__(512) void k_fnet(Params p) {
#if defined(__HIP_DEVICE_COMPILE__)
  extern __shared__ __attribute__((aligned(16))) char smem_[];
  ldsp smem = (ldsp)smem_;
  const int lane = threadIdx.x & 63, w = __builtin_amdgcn_readfirstlane(threadIdx.x >> 6), b = blockIdx.x;
  const int tg = (b & 7) * 2 + (b >> 7), c = (b >> 3) & 15, hf = c >> 3, nt = c & 7, odd = nt & 1;
  const bool seven = c < 12;
  const int odd3 = seven ? c & 1 : 0, T0 = seven ? 7 * c : 84 + 6 * (c - 12), Ts = odd3 ? T0 : T0 + 6;
  const int g = lane >> 4;
  const rsrc_t rw = mkrsrc(p.ws, WS_BYTES);
  if (w < 4) {
    TS(0);
    unsigned ep = ld4(rw, OFF_E, 0);
    const v2u* xr = (const v2u*)(p.x + (size_t)(b * 8 + w * 2 + (lane >> 5)) * 1280) + (lane & 31);
    v2u xv[5];
#pragma unroll
    for (int i = 0; i < 5; i++) xv[i] = xr[32 * i];
    if (w == 0 && lane < 4) *(LDS unsigned*)(smem + L0C + lane * 4) = 0;
    waitl0(); bar();
    asm volatile("" : "+v"(ep), "+v"(xv[0]), "+v"(xv[1]), "+v"(xv[2]), "+v"(xv[3]), "+v"(xv[4]));
    const unsigned tag = __builtin_amdgcn_readfirstlane(ep) + 1;
    {
      float v[20], s = 0;
#pragma unroll
      for (int i = 0; i < 5; i++) { v[4 * i] = lo(xv[i][0]); v[4 * i + 1] = hi(xv[i][0]); v[4 * i + 2] = lo(xv[i][1]); v[4 * i + 3] = hi(xv[i][1]); }
#pragma unroll
      for (int i = 0; i < 20; i++) s += v[i];
      const float mean = xsum16(rowsum(s)) * (1.f / 640.f);
      float q = 0;
#pragma unroll
      for (int i = 0; i < 20; i++) { v[i] -= mean; q += v[i] * v[i]; }
      const float rs = 1.0f / sqrtf(xsum16(rowsum(q)) * (1.f / 640.f) + 1e-5f);
#pragma unroll
      for (int i = 0; i < 5; i++)
        *(LDS v2u*)(smem + L0R + (w * 2 + (lane >> 5)) * RS + ((lane & 31) + 32 * i) * 8) = v2u{pk(v[4 * i] * rs, v[4 * i + 1] * rs), pk(v[4 * i + 2] * rs, v[4 * i + 3] * rs)};
      if (w == 0 && lane == 0) *(LDS unsigned*)(smem + L0C + 16) = tag;
      waitl0();
      if (lane == 0) lds_add(smem + L0C);
      lds_wait(smem + L0C, 4);
      TS(1);
      const int qq = (lane & 15) >> 2, pp = lane & 3;
#pragma unroll
      for (int i = 0; i < 3; i++) {
        const int T = 10 * w + 4 * i + (i < 2 ? g : g & 1);
        ldsp a = smem + L0R + qq * RS + piece_col(T, pp, split640(T)) * 2;
        const v4s v0 = __builtin_amdgcn_ds_read_tr16_b64_v4i16((LDS v4s*)a), v1 = __builtin_amdgcn_ds_read_tr16_b64_v4i16((LDS v4s*)(a + 4 * RS));
        st16(rw, i == 2 && g >= 2 ? OOB : OFF_LNF + T * 65536 + (b >> 2) * 1024 + (b & 3) * 256 + (lane & 15) * 16,
             __builtin_bit_cast(v4u, __builtin_shufflevector(v0, v1, 0, 1, 2, 3, 4, 5, 6, 7)));
      }
      waitv<0>();
      unsigned old = 0;
      if (lane == 0) old = lds_add(smem + L0C + 4);
      if (__builtin_amdgcn_readfirstlane(old) == 3 && lane == 0) st4(rw, OFF_F0 + b * FSTR, tag);
      TS(2);
    }
    const int fl = OFF_FG + (tg * 16 + c) * FSTR;
    const int kb0 = 5 * (nt >> 1), sp = ((odd * 2 + (g >> 1)) * 16 + (lane & 15)) * 16 + (g & 1) * 8;
    {
      v4f acc[2][5];
      bar(); TS(3);
      consume<5, false, true, false, 32, 4, L1A, 16384, L1B, 10240>(smem, w, lane, acc);
      TS(4);
      const float sg = hf ? -1.f : 1.f;
#pragma unroll
      for (int mi = 0; mi < 2; mi++) {
        const int dst = OFF_PQF + (tg * 8 + w * 2 + mi) * 40960 + (hf * 20 + kb0) * 1024;
#pragma unroll
        for (int u = 0; u < 2; u++) {
          const v4f a = acc[mi][2 * u] * sg, c2 = acc[mi][2 * u + 1] * sg;
          st16(rw, dst + (odd * 3 + u) * 1024 + lane * 16, v4u{pk(a[0], a[1]), pk(a[2], a[3]), pk(c2[0], c2[1]), pk(c2[2], c2[3])});
        }
        const v4f a = acc[mi][4] * sg;
        st8(rw, dst + 2048 + sp, v2u{pk(a[0], a[1]), pk(a[2], a[3])});
      }
      waitv<0>(); bar();
      if (w == 0 && lane == 0) st4(rw, fl, tag);
      TS(5);
    }
    const int tb = tg * 8 + hf * 4 + (w & 1) * 2 + (w >> 1), t = tb * 16 + (lane & 15);
    const int cp = (kb0 + odd * 3) * 32 + g * 8, cs = (kb0 + 2) * 32 + odd * 16 + g * 4;
    v4u h0, h1; v2u h2;
    {
      const v4u x0 = *(const v4u*)(p.x + (size_t)t * 1280 + cp * 2), x1 = *(const v4u*)(p.x + (size_t)t * 1280 + cp * 2 + 64);
      const v2u x2 = *(const v2u*)(p.x + (size_t)t * 1280 + cs * 2);
      v4f acc[2][5], o[5];
      bar(); TS(6);
      consume<5, true, false, true, 20, 5, L2A, 8192, L2B, 10240>(smem, w, lane, acc);
      TS(7);
      pair_reduce(smem + L2A, w, lane, acc, o);
      o[0] += v4f{lo(x0[0]), hi(x0[0]), lo(x0[1]), hi(x0[1])}; o[1] += v4f{lo(x0[2]), hi(x0[2]), lo(x0[3]), hi(x0[3])};
      o[2] += v4f{lo(x1[0]), hi(x1[0]), lo(x1[1]), hi(x1[1])}; o[3] += v4f{lo(x1[2]), hi(x1[2]), lo(x1[3]), hi(x1[3])};
      o[4] += v4f{lo(x2[0]), hi(x2[0]), lo(x2[1]), hi(x2[1])};
      h0 = v4u{pk(o[0][0], o[0][1]), pk(o[0][2], o[0][3]), pk(o[1][0], o[1][1]), pk(o[1][2], o[1][3])};
      h1 = v4u{pk(o[2][0], o[2][1]), pk(o[2][2], o[2][3]), pk(o[3][0], o[3][1]), pk(o[3][2], o[3][3])};
      h2 = v2u{pk(o[4][0], o[4][1]), pk(o[4][2], o[4][3])};
      const int dst = OFF_HF + tb * 20480 + kb0 * 1024;
      st16(rw, dst + odd * 3072 + lane * 16, h0);
      st16(rw, dst + odd * 3072 + 1024 + lane * 16, h1);
      st8(rw, dst + 2048 + sp, h2);
      const v4f s4 = (o[0] + o[1]) + (o[2] + o[3]) + o[4];
      const float m = xsum32(xsum16((s4[0] + s4[1]) + (s4[2] + s4[3]))) * (1.f / 80.f);
      v4f q4 = {0, 0, 0, 0};
#pragma unroll
      for (int j = 0; j < 5; j++) { const v4f d = o[j] - m; q4 += d * d; }
      const float q = xsum32(xsum16((q4[0] + q4[1]) + (q4[2] + q4[3])));
      st8(rw, lane < 16 ? OFF_ST + nt * 16384 + t * 8 : OOB, v2u{asu(m), asu(q)});
      waitv<0>(); bar();
      if (w == 0 && lane == 0) st4(rw, fl + 256 * FSTR, tag);
      TS(8);
    }
    {
      const int k3 = (T0 + odd3) >> 1;
      v4u bp[3]; v2u bs = {0, 0};
#pragma unroll
      for (int u = 0; u < 3; u++) bp[u] = *(const v4u*)(p.b1 + ((k3 + u) * 32 + g * 8) * 2);
      if (seven) bs = *(const v2u*)(p.b1 + (Ts * 16 + g * 4) * 2);
      v4f acc[2][7];
      bar(); TS(9);
      consume<7, false, false, false, 10, 4, L3A, 16384, L3B, 14336>(smem, w, lane, acc);
      TS(10);
#pragma unroll
      for (int mi = 0; mi < 2; mi++) {
        const v2f st = *(LDS v2f*)(smem + L3X + 512 + (w * 32 + mi * 16 + (lane & 15)) * 8);
        const int dst = OFF_HH + (tg * 8 + w * 2 + mi) * 55296;
        unsigned r[14];
#pragma unroll
        for (int j = 0; j < 7; j++) {
          const v4f cs4 = *(LDS v4f*)(smem + L3X + (j * 16 + g * 4) * 4);
          const unsigned bb0 = j < 6 ? bp[j >> 1][(j & 1) * 2] : bs[0], bb1 = j < 6 ? bp[j >> 1][(j & 1) * 2 + 1] : bs[1];
          const v4f bias = {lo(bb0), hi(bb0), lo(bb1), hi(bb1)};
          v4f z = acc[mi][j] * st[1] + (bias - cs4 * st[0]);
          z = __builtin_elementwise_max(z, v4f{0, 0, 0, 0}); z *= z;
          r[2 * j] = pk(z[0], z[1]); r[2 * j + 1] = pk(z[2], z[3]);
          if (j & 1) st16(rw, dst + (k3 + (j >> 1)) * 1024 + lane * 16, v4u{r[2 * j - 2], r[2 * j - 1], r[2 * j], r[2 * j + 1]});
        }
        st8(rw, seven ? dst + (Ts >> 1) * 1024 + ((odd3 * 2 + (g >> 1)) * 16 + (lane & 15)) * 16 + (g & 1) * 8 : OOB, v2u{r[12], r[13]});
      }
      waitv<0>(); bar();
      if (w == 0 && lane == 0) st4(rw, fl + 512 * FSTR, tag);
      TS(11);
    }
    {
      const v4u c0 = *(const v4u*)(p.b2 + cp * 2), c1 = *(const v4u*)(p.b2 + cp * 2 + 64);
      const v2u c2 = *(const v2u*)(p.b2 + cs * 2);
      v4f acc[2][5], o[5];
      bar(); TS(12);
      consume<5, true, false, false, 27, 5, L4A, 8192, L4B, 10240>(smem, w, lane, acc);
      TS(13);
      pair_reduce(smem + L4A, w, lane, acc, o);
#define FIN(a, e, h, c) pk(a[e] + lo(h) + lo(c), a[e + 1] + hi(h) + hi(c))
      char* dst = p.out + (size_t)t * 1280;
      *(v4u*)(dst + cp * 2) = v4u{FIN(o[0], 0, h0[0], c0[0]), FIN(o[0], 2, h0[1], c0[1]), FIN(o[1], 0, h0[2], c0[2]), FIN(o[1], 2, h0[3], c0[3])};
      *(v4u*)(dst + cp * 2 + 64) = v4u{FIN(o[2], 0, h1[0], c1[0]), FIN(o[2], 2, h1[1], c1[1]), FIN(o[3], 0, h1[2], c1[2]), FIN(o[3], 2, h1[3], c1[3])};
      *(v2u*)(dst + cs * 2) = v2u{FIN(o[4], 0, h2[0], c2[0]), FIN(o[4], 2, h2[1], c2[1])};
#undef FIN
      TS(14);
    }
  } else if (w < 6) {
    const int lw = w - 4;
    bar();
    {
      const int j = b >> 3;
      const rsrc_t r = mkrsrc((j >> 4 ? p.ci : p.cr) + (size_t)((b & 7) * 256 + (j & 15) * 16) * 4096, 16 * 4096);
      const int vo = (lw * 8 + (lane >> 3)) * 4096 + (lane & 7) * 16;
#pragma unroll
      for (int i = 0; i < 32; i++) { dma<0>(r, smem + L0D, vo, i * 128); if (i >= CTH - 1) waitv<CTH - 1>(); }
    }
    int vt[5];
#pragma unroll
    for (int i = 0; i < 5; i++) { const int j = (lw * 5 + i) >> 1; vt[i] = 5 * nt + (odd ? (j + 1) % 5 : j); }
    const int fh = lw & 1;
    float csum = 0;
    unsigned cpart[20];
    unsigned tag;
    const int fl = lane < 16 ? OFF_FG + (tg * 16 + lane) * FSTR : OOB, fa = (lw * 128 + lane) * FSTR;
    int vd[5];
#pragma unroll
    for (int i = 0; i < 5; i++) {
      const int row = (lw * 5 + i) * 8 + (lane >> 3), j = row >> 4, T = 5 * nt + (odd ? (j + 1) % 5 : j), n = row & 15;
      vd[i] = (piece_col(T, n >> 2, j == 4) + (n & 3)) * 1280 + ((lane & 7) ^ ((row >> 1) & 7)) * 16;
    }
    auto e2 = [&](int s, int st) {
      const rsrc_t r = mkrsrc(s < 10 ? p.dr : p.di - 1280, D_ * D_ * 2 + (s < 10 ? 0 : 1280));
#pragma unroll
      for (int i = 0; i < 5; i++) dma<0>(r, smem + L2B + st * 10240 + (lw * 5 + i) * 1024, vd[i], s * 128);
    };
    {
      const rsrc_t ra = mkrsrc((hf ? p.ci : p.cr) + (size_t)tg * 128 * 4096, 128 * 4096);
      int va[8], vb[5];
#pragma unroll
      for (int i = 0; i < 8; i++) { const int row = (lw * 8 + i) * 8 + (lane >> 3); va[i] = row * 4096 + ((lane & 7) ^ ((row >> 1) & 7)) * 16; }
#pragma unroll
      for (int i = 0; i < 5; i++) vb[i] = OFF_LNF + vt[i] * 65536 + ((fh + i) & 1) * 1024 + lane * 16;
      auto e1 = [&](int s, int st) {
#pragma unroll
        for (int i = 0; i < 8; i++) dma<0>(ra, smem + L1A + st * 16384 + (lw * 8 + i) * 1024, va[i], s * 128);
      };
      auto l1 = [&](int s, int st) {
#pragma unroll
        for (int i = 0; i < 5; i++) dma<SC1>(rw, smem + L1B + st * 10240 + (lw * 5 + i) * 1024, vb[i], s * 2048);
      };
#pragma unroll
      for (int s = 0; s < 3; s++) { e1(s, s); waitv<(CTH > 8 ? CTH - 8 : 0)>(); }
      lds_wait(smem + L0C, 4);
      tag = __builtin_amdgcn_readfirstlane(*(volatile LDS unsigned*)(smem + L0C + 16));
      TS(16);
      poll128(rw, OFF_F0 + fa, tag);
      if (lane == 0) lds_add(smem + L0C + 12);
      lds_wait(smem + L0C + 12, 2); acquire();
      TS(17);
      if (b == 0 && w == 4 && lane == 0) st4(rw, OFF_E, tag);
      unsigned fw0, fw1;
      produce<5, 13, 3, 4, 32, 0, 22>(l1, e1, [] {}, [] {}, [&] {
#pragma unroll
        for (int s = 0; s < 4; s++) e2(s, s);
        fw0 = ld4(rw, OFF_FW + fa, 0); fw1 = ld4(rw, OFF_FW + fa, 64 * FSTR);
      });
      asm volatile("" : "+v"(fw0), "+v"(fw1));
      if (!all_eq(fw0, fw1, tag)) poll128(rw, OFF_FW + fa, tag);
      bar(); acquire();
      const int i2 = lw * 64 + lane, j = i2 >> 4, T = j < 6 ? T0 + odd3 + j : Ts;
      const int vc = i2 < (seven ? 112 : 96) ? OFF_CS + (T * 16 + (i2 & 15)) * 4 : OOB;
#pragma unroll
      for (int k = 0; k < 20; k++) cpart[k] = ld4(rw, vc, k * F_ * 4);
    }
    int vb3[7];
#pragma unroll
    for (int i = 0; i < 7; i++) { const int f = lw * 7 + i, j = f >> 1, T = j < 6 ? T0 + odd3 + j : Ts;
      vb3[i] = (j < 6 || seven) ? OFF_W1F + T * 20480 + (f & 1) * 1024 + lane * 16 : OOB; }
    auto e3 = [&](int s, int st) {
#pragma unroll
      for (int i = 0; i < 7; i++) dma<SC1>(rw, smem + L3B + st * 14336 + (lw * 7 + i) * 1024, vb3[i], s * 2048);
    };
    {
      const int va = OFF_PQF + (tg * 8 + hf * 4 + lw * 2) * 40960 + lane * 16;
      auto l2 = [&](int s, int st) {
#pragma unroll
        for (int i = 0; i < 4; i++) dma<SC1>(rw, smem + L2A + st * 8192 + (lw * 4 + i) * 1024, va + (i >> 1) * 40960 + (i & 1) * 1024, s * 2048);
      };
      poll16(rw, fl, tag); acquire();
      TS(18);
      produce<4, 9, 4, 5, 20, 0, 21>(l2, e2, [&] {
#pragma unroll
        for (int k = 0; k < 20; k++) csum += asf(cpart[k]);
      }, [] {}, [&] {
#pragma unroll
        for (int s = 0; s < 3; s++) e3(s, s);
      });
      bar(); bar();
    }
    int vb4[5];
#pragma unroll
    for (int i = 0; i < 5; i++) vb4[i] = OFF_W2F + vt[i] * 55296 + ((fh + i) & 1) * 1024 + lane * 16;
    auto e4 = [&](int s, int st) {
#pragma unroll
      for (int i = 0; i < 5; i++) dma<SC1>(rw, smem + L4B + st * 10240 + (lw * 5 + i) * 1024, vb4[i], s * 2048);
    };
    {
      const int va = OFF_HF + (tg * 8 + lw * 4) * 20480 + lane * 16;
      auto l3 = [&](int s, int st) {
#pragma unroll
        for (int i = 0; i < 8; i++) dma<SC1>(rw, smem + L3A + st * 16384 + (lw * 8 + i) * 1024, va + (i >> 1) * 20480 + (i & 1) * 1024, s * 2048);
      };
      const int i2 = lw * 64 + lane;
      v2u sv[8];
      poll16(rw, fl + 256 * FSTR, tag); acquire();
      TS(19);
      produce<8, 15, 3, 4, 10, 8, 15>(l3, e3, [&] {
#pragma unroll
        for (int k = 0; k < 8; k++) sv[k] = ld8(rw, OFF_ST + (tg * 128 + i2) * 8, k * 16384);
      }, [&] {
        float m = 0, q = 0;
#pragma unroll
        for (int k = 0; k < 8; k++) { asm volatile("" : "+v"(sv[k])); m += asf(sv[k][0]); q += asf(sv[k][1]); }
        m *= 0.125f;
#pragma unroll
        for (int k = 0; k < 8; k++) { const float d = asf(sv[k][0]) - m; q += 80.f * d * d; }
        const float rs = 1.0f / sqrtf(q * (1.f / 640.f) + 1e-5f);
        const unsigned a0 = (unsigned)(size_t)(smem + L3X + i2 * 4), a1 = (unsigned)(size_t)(smem + L3X + 512 + i2 * 8);
        asm volatile("ds_write_b32 %0, %1\n ds_write_b64 %2, %3\n s_waitcnt lgkmcnt(0)" ::"v"(a0), "v"(csum), "v"(a1), "v"(v2f{m * rs, rs}) : "memory");
      }, [&] {
#pragma unroll
        for (int s = 0; s < 3; s++) e4(s, s);
      });
      bar();
      e4(3, 3);
    }
    {
      const int va = OFF_HH + (tg * 8 + hf * 4 + lw * 2) * 55296 + lane * 16;
      auto l4 = [&](int s, int st) {
#pragma unroll
        for (int i = 0; i < 4; i++) dma<SC1>(rw, smem + L4A + st * 8192 + (lw * 4 + i) * 1024, va + (i >> 1) * 55296 + (i & 1) * 1024, s * 2048);
      };
      poll16(rw, fl + 512 * FSTR, tag); acquire();
      TS(20);
      produce<4, 9, 4, 5, 27, 0, 0>(l4, e4, [] {}, [] {}, [] {});
      bar();
    }
    if (w == 4) TSOUT(16, 5);
  } else {
    const int hw = w - 6, nu = hw == 0 && b < 56 ? 3 : 2;
    bar();
#pragma unroll 1
    for (int n = 0; n < nu; n++) {
      const int i = n < 2 ? hw * 2 + n : 4, u = b + 256 * i;
      const bool isw1 = u < 540;
      const int uu = isw1 ? u : u - 540, kb = isw1 ? uu / 27 : uu / 10, cu = isw1 ? uu % 27 : uu % 10, ld = isw1 ? F_ * 2 : D_ * 2;
      const rsrc_t r = mkrsrc(isw1 ? p.w1 : p.w2, D_ * F_ * 2);
#pragma unroll
      for (int k = 0; k < 4; k++) dma<0>(r, smem + L0W + i * 4096 + k * 1024, (kb * 32 + k * 8 + (lane >> 3)) * ld + cu * 128 + (lane & 7) * 16, 0);
    }
#pragma unroll
    for (int k = 0; k < 4; k++) {
      const int q = b + 256 * (hw * 3 + k);
      const rsrc_t r = mkrsrc(q < 800 ? p.dr + q * 1024 : p.di + (q - 800) * 1024, q < 1600 && (hw || k < 3) ? 1024 : 0);
      dma<0>(r, smem + L0D, lane * 16, 0);
    }
    waitv<4>();
    const int qq = (lane & 15) >> 2, pp = lane & 3;
#pragma unroll 1
    for (int n = 0; n < nu; n++) {
      const int i = n < 2 ? hw * 2 + n : 4, u = b + 256 * i;
      const bool isw1 = u < 540;
      const int uu = isw1 ? u : u - 540, kb = isw1 ? uu / 27 : uu / 10, cu = isw1 ? uu % 27 : uu % 10;
#pragma unroll
      for (int t = 0; t < 4; t++) {
        const int T = cu * 4 + t;
        const unsigned a = (unsigned)(size_t)(smem + L0W + i * 4096 + (g * 8 + qq) * 128 + (piece_col(T, pp, isw1 ? split1728(T) : split640(T)) - cu * 64) * 2);
        v2u v0, v1;
        asm volatile("ds_read_b64_tr_b16 %0, %2\n ds_read_b64_tr_b16 %1, %2 offset:512\n s_waitcnt lgkmcnt(0)" : "=&v"(v0), "=&v"(v1) : "v"(a) : "memory");
        st16(rw, (isw1 ? OFF_W1F + T * 20480 : OFF_W2F + T * 55296) + kb * 1024 + lane * 16, v4u{v0[0], v0[1], v1[0], v1[1]});
        if (isw1) {
          const float cs = xsum32(xsum16((lo(v0[0]) + hi(v0[0])) + (lo(v0[1]) + hi(v0[1])) + (lo(v1[0]) + hi(v1[0])) + (lo(v1[1]) + hi(v1[1]))));
          st4(rw, lane < 16 ? OFF_CS + (kb * F_ + T * 16 + lane) * 4 : OOB, asu(cs));
        }
      }
    }
    waitv<0>();
    lds_wait(smem + L0C, 4);
    const unsigned tag = *(volatile LDS unsigned*)(smem + L0C + 16);
    unsigned old = 0;
    if (lane == 0) old = lds_add(smem + L0C + 8);
    if (__builtin_amdgcn_readfirstlane(old) == 1 && lane == 0) st4(rw, OFF_FW + b * FSTR, tag);
    TS(21);
    if (w == 6) TSOUT(21, 1);
  }
#endif
}

extern "C" void kernel_launch(void* const* d_in, const int* in_sizes, int n_in, void* d_out, int out_size, void* d_ws, size_t ws_size,
                              hipStream_t stream) {
  Params p;
  p.x = (const char*)d_in[0]; p.cr = (const char*)d_in[1]; p.ci = (const char*)d_in[2]; p.dr = (const char*)d_in[3]; p.di = (const char*)d_in[4];
  p.w1 = (const char*)d_in[6]; p.b1 = (const char*)d_in[7]; p.w2 = (const char*)d_in[8]; p.b2 = (const char*)d_in[9];
  p.out = (char*)d_out; p.ws = (char*)d_ws;
  (void)hipFuncSetAttribute((const void*)k_fnet, hipFuncAttributeMaxDynamicSharedMemorySize, LDS_BYTES);
  k_fnet<<<256, 512, LDS_BYTES, stream>>>(p);
}
```

```cpp
#include <hip/hip_runtime.h>
#include <hip/hip_bf16.h>
#include <stdint.h>

#define CTH 8
#define MT1 2
#define NPRE 3
#define TD 8
#define TW 4
#define TX1 4
#define TX2 9
#define UD 6
#define REMAP3 1
#define CODELEN 24000

#define LDS __attribute__((address_space(3)))
typedef LDS char* ldsp;
typedef short v4s __attribute__((ext_vector_type(4)));
typedef short v8s __attribute__((ext_vector_type(8)));
typedef __bf16 v8b __attribute__((ext_vector_type(8)));
typedef __bf16 v2b __attribute__((ext_vector_type(2)));
typedef float v4f __attribute__((ext_vector_type(4)));
typedef float v2f __attribute__((ext_vector_type(2)));
typedef unsigned v4u __attribute__((ext_vector_type(4)));
typedef unsigned v2u __attribute__((ext_vector_type(2)));

constexpr int S_ = 2048, D_ = 640, F_ = 1728;
constexpr int OFF_E   = 0;
constexpr int FSTR    = 128;
constexpr int OFF_F0  = 4096;
constexpr int OFF_F0P = 1024;
constexpr int OFF_FWP = 2048;
constexpr int OFF_FG  = OFF_F0 + 256 * FSTR;
constexpr int OFF_LNF = 256 * 1024;
constexpr int OFF_PQF = OFF_LNF + 40 * 64 * 1024;
constexpr int OFF_HF  = OFF_PQF + 128 * 40 * 1024;
constexpr int OFF_HH  = OFF_HF + 128 * 20 * 1024;
constexpr int OFF_W1F = OFF_HH + 128 * 54 * 1024;
constexpr int OFF_W2F = OFF_W1F + 108 * 20 * 1024;
constexpr int OFF_CS  = OFF_W2F + 40 * 54 * 1024;
constexpr int OFF_ST  = OFF_CS + 20 * 1728 * 4;
constexpr int WS_BYTES = OFF_ST + 8 * 2048 * 8;
constexpr int OOB = 0x40000000;
constexpr int LDS_BYTES = 160 * 1024;

struct Params {
  const char* x; const char* cr; const char* ci; const char* dr; const char* di;
  const char* w1; const char* b1; const char* w2; const char* b2;
  char* out; char* ws;
};

#define TS(i) do {} while (0)
#define TSOUT(a, n)

#if defined(__HIP_DEVICE_COMPILE__)
typedef __amdgpu_buffer_rsrc_t rsrc_t;
constexpr int SC1 = 16;
__device__ __forceinline__ rsrc_t mkrsrc(const void* p, unsigned bytes) { return __builtin_amdgcn_make_buffer_rsrc((void*)p, 0, bytes, 0x00020000); }
template <int AUX> __device__ __forceinline__ void dma(rsrc_t r, ldsp dst, int voff, int soff) {
  __builtin_amdgcn_raw_ptr_buffer_load_lds(r, (LDS void*)dst, 16, voff, soff, 0, AUX);
}
__device__ __forceinline__ void st16(rsrc_t r, int off, v4u v) { __builtin_amdgcn_raw_buffer_store_b128(v, r, off, 0, SC1); }
__device__ __forceinline__ void st8(rsrc_t r, int off, v2u v) { __builtin_amdgcn_raw_buffer_store_b64(v, r, off, 0, SC1); }
__device__ __forceinline__ void st4(rsrc_t r, int off, unsigned v) { __builtin_amdgcn_raw_buffer_store_b32(v, r, off, 0, SC1); }
__device__ __forceinline__ v4u ld16(rsrc_t r, int off) { return __builtin_amdgcn_raw_buffer_load_b128(r, off, 0, SC1); }
__device__ __forceinline__ v2u ld8(rsrc_t r, int off, int soff) { return __builtin_amdgcn_raw_buffer_load_b64(r, off, soff, SC1); }
__device__ __forceinline__ unsigned ld4(rsrc_t r, int off, int soff) { return __builtin_amdgcn_raw_buffer_load_b32(r, off, soff, SC1); }
template <int N> __device__ __forceinline__ void waitv() { asm volatile("s_waitcnt vmcnt(%0)" ::"n"(N) : "memory"); }
__device__ __forceinline__ void bar() { asm volatile("" ::: "memory"); __builtin_amdgcn_s_barrier(); asm volatile("" ::: "memory"); }
__device__ __forceinline__ void waitl0() { __builtin_amdgcn_sched_barrier(0); __builtin_amdgcn_s_waitcnt(0xC07F); __builtin_amdgcn_sched_barrier(0); }
__device__ __forceinline__ v4f mfma(v8s a, v8s b, v4f c) {
  return __builtin_amdgcn_mfma_f32_16x16x32_bf16(__builtin_bit_cast(v8b, a), __builtin_bit_cast(v8b, b), c, 0, 0, 0);
}
__device__ __forceinline__ unsigned pk(float a, float b) { v2b r = {(__bf16)a, (__bf16)b}; return __builtin_bit_cast(unsigned, r); }
__device__ __forceinline__ float asf(unsigned u) { return __builtin_bit_cast(float, u); }
__device__ __forceinline__ unsigned asu(float f) { return __builtin_bit_cast(unsigned, f); }
__device__ __forceinline__ float lo(unsigned u) { return asf(u << 16); }
__device__ __forceinline__ float hi(unsigned u) { return asf(u & 0xffff0000u); }
template <int CTRL> __device__ __forceinline__ float dppadd(float v) { return v + asf(__builtin_amdgcn_update_dpp(0, asu(v), CTRL, 0xf, 0xf, true)); }
__device__ __forceinline__ float rowsum(float v) { return dppadd<0x121>(dppadd<0x122>(dppadd<0x124>(dppadd<0x128>(v)))); }
__device__ __forceinline__ float xsum16(float v) { const auto r = __builtin_amdgcn_permlane16_swap(asu(v), asu(v), false, false); return asf(r[0]) + asf(r[1]); }
__device__ __forceinline__ float xsum32(float v) { const auto r = __builtin_amdgcn_permlane32_swap(asu(v), asu(v), false, false); return asf(r[0]) + asf(r[1]); }
template <int NM, int ND> __device__ __forceinline__ void ilv() {
#pragma unroll
  for (int i = 0; i < NM; i++) { __builtin_amdgcn_sched_group_barrier(0x8, 1, 0); if (i < ND) __builtin_amdgcn_sched_group_barrier(0x100, 1, 0); }
}
__device__ __forceinline__ int piece_col(int T, int p, bool split) { return 16 * T + 4 * p + ((4 * p - 12 * (T & 1)) & ((int)split - 1)); }
__device__ __forceinline__ bool split640(int T) { return (T >> 1) % 5 == 2; }
__device__ __forceinline__ bool split1728(int T) { return (T >> 1) % 7 == 3 && T < 84; }
__device__ __forceinline__ unsigned lds_add(ldsp a) { return __hip_atomic_fetch_add((LDS unsigned*)a, 1u, __ATOMIC_RELAXED, __HIP_MEMORY_SCOPE_WORKGROUP); }
__device__ __forceinline__ void lds_wait(ldsp a, unsigned v) { while (*(volatile LDS unsigned*)a != v) __builtin_amdgcn_s_sleep(1); asm volatile("" ::: "memory"); }
constexpr int SPIN_CAP = 1 << 17;
__device__ __forceinline__ v4u ld16_async(const char* base, unsigned off) { v4u v; asm volatile("global_load_dwordx4 %0, %1, %2 sc1" : "=v"(v) : "v"(off), "s"(base) : "memory"); return v; }
__device__ __forceinline__ void sbar() { __builtin_amdgcn_sched_barrier(0); }
__device__ __forceinline__ bool eq4(v4u v, unsigned tag) { return __builtin_amdgcn_ballot_w64(v[0] != tag || v[1] != tag || v[2] != tag || v[3] != tag) == 0; }
__device__ __forceinline__ void poll256(rsrc_t r, int off, unsigned tag) {
#pragma unroll 1
  for (int it = 0; it < SPIN_CAP; it++) { asm volatile("" ::: "memory"); if (eq4(ld16(r, off), tag)) break; __builtin_amdgcn_s_sleep(2); }
  asm volatile("" ::: "memory");
}
template <unsigned MASK> __device__ __forceinline__ void polln(rsrc_t r, int off, unsigned tag) {
#pragma unroll 1
  for (int it = 0; it < SPIN_CAP; it++) { asm volatile("" ::: "memory"); if (((unsigned)__builtin_amdgcn_ballot_w64(ld4(r, off, 0) != tag) & MASK) == 0) break; __builtin_amdgcn_s_sleep(1); }
  asm volatile("" ::: "memory");
}

constexpr int K1 = 1024, L1S = 26 * K1, L1R = 90 * K1, L3X = 150 * K1;
constexpr int RS = 1312;
constexpr int L0R = 52 * K1, L0W = 130 * K1, L0C = 150 * K1, L0D = 152 * K1;

template <int MT>
__device__ __forceinline__ void consume1(ldsp smem, int w, int lane, v4f (&acc)[MT][5]) {
  const int g = lane >> 4, r = lane & 15;
  const int kh = MT == 4 ? w >> 1 : 0, rb = MT == 4 ? (w & 1) * 4 : w * 2;
  const int a0 = (rb * 16 + r) * 128 + (((kh * 4 + g) ^ (r >> 1)) * 16), a1 = MT == 4 ? a0 : a0 ^ 64;
  const int b0 = 16 * K1 + kh * K1 + lane * 16, b1 = MT == 4 ? b0 : b0 + K1;
  v8s pa[MT], pb[5], qa[MT], qb[5];
#pragma unroll
  for (int i = 0; i < MT; i++) { qa[i] = v8s{0, 0, 0, 0, 0, 0, 0, 0};
#pragma unroll
    for (int j = 0; j < 5; j++) acc[i][j] = v4f{0, 0, 0, 0}; }
#pragma unroll
  for (int j = 0; j < 5; j++) qb[j] = v8s{0, 0, 0, 0, 0, 0, 0, 0};
#define RD(A, B, ao, bo) { B[0] = *(LDS v8s*)(smem + so + bo); _Pragma("unroll") for (int i = 0; i < MT; i++) A[i] = *(LDS v8s*)(smem + so + ao + i * 2048); \
                           _Pragma("unroll") for (int j = 1; j < 5; j++) B[j] = *(LDS v8s*)(smem + so + bo + j * 2048); }
#define MM(A, B) { _Pragma("unroll") for (int j = 0; j < 5; j++) _Pragma("unroll") for (int i = 0; i < MT; i++) acc[i][j] = mfma(B[j], A[i], acc[i][j]); }
#define NEXT { bar(); so += L1S; if (so == 5 * L1S) so = 0; }
  int so = 3 * L1S;
#pragma unroll 1
  for (int s = 0; s < (MT == 4 ? 15 : 31); s++) {
    RD(pa, pb, a0, b0); MM(qa, qb); ilv<5 * MT, 5 + MT>(); sbar();
    if (MT == 4) NEXT
    RD(qa, qb, a1, b1); MM(pa, pb); ilv<5 * MT, 5 + MT>(); sbar();
    NEXT
  }
  RD(pa, pb, a0, b0); MM(qa, qb); ilv<5 * MT, 5 + MT>(); sbar();
  if (MT == 4) NEXT
  RD(qa, qb, a1, b1); MM(pa, pb); ilv<5 * MT, 5 + MT>();
  if (MT == 4) { waitl0(); bar(); } else sbar();
  MM(qa, qb);
#undef RD
#undef MM
#undef NEXT
}

template <int NJ, int NU, int ASTR, int AUS, class FB, class FS, class FX, class FT>
__device__ __forceinline__ void consume(ldsp bl0, rsrc_t rw, int va0, v4f (&acc)[2][NJ], FB boff, FS sync, FX extra, FT stamp) {
  v8s a[UD][2], bq[2][NJ];
  int va[2] = {va0, va0 + ASTR};
  ldsp bl[3] = {bl0, bl0 + 65536, bl0 + 131072};
  asm volatile("" : "+v"(va[0]), "+v"(va[1]), "+v"(bl[0]), "+v"(bl[1]), "+v"(bl[2]));
  bar();
  stamp(-1);
#define LDA(u) _Pragma("unroll") for (int i = 0; i < 2; i++) a[(u) % UD][i] = __builtin_bit_cast(v8s, __builtin_amdgcn_raw_buffer_load_b128(rw, va[i], (u) * AUS, SC1));
#define LDB(u) _Pragma("unroll") for (int j = 0; j < NJ; j++) bq[(u) & 1][j] = *(LDS v8s*)(bl[(boff(u) + j * 2048) >> 16] + ((boff(u) + j * 2048) & 0xffff));
  LDA(0) sbar(); LDB(0) sbar();
#pragma unroll
  for (int u = 1; u < UD - 1; u++) { LDA(u) sbar(); }
  extra();
  sbar();
#pragma unroll
  for (int u = 1; u <= NU; u++) {
    if (u < NU) { if (sync(u)) bar(); LDB(u) }
    const bool ld = u - 2 + UD < NU;
    if (ld) { LDA(u - 2 + UD) }
#pragma unroll
    for (int j = 0; j < NJ; j++)
#pragma unroll
      for (int i = 0; i < 2; i++) acc[i][j] = mfma(bq[(u - 1) & 1][j], a[(u - 1) % UD][i], u == 1 ? v4f{0, 0, 0, 0} : acc[i][j]);
#pragma unroll
    for (int k = 0; k < 2 * NJ; k++) {
      __builtin_amdgcn_sched_group_barrier(0x8, 1, 0);
      if (u < NU && k < NJ) __builtin_amdgcn_sched_group_barrier(0x100, 1, 0);
      if (ld && k >= NJ && k < NJ + 2) __builtin_amdgcn_sched_group_barrier(0x20, 1, 0);
    }
    sbar();
    stamp(u);
  }
#undef LDA
#undef LDB
}

template <int N>
__device__ __forceinline__ void pair_reduce(ldsp mine, ldsp other, int w, v4f (&acc)[2 * N][5], v4f (&o)[N][5]) {
  const int keep = w >> 1;
#pragma unroll
  for (int i = 0; i < N; i++)
#pragma unroll
    for (int j = 0; j < 5; j++) *(LDS v4f*)(mine + (i * 5 + j) * 1024) = keep ? acc[i][j] : acc[N + i][j];
  waitl0(); bar();
#pragma unroll
  for (int i = 0; i < N; i++)
#pragma unroll
    for (int j = 0; j < 5; j++) o[i][j] = (keep ? acc[N + i][j] : acc[i][j]) + *(LDS v4f*)(other + (i * 5 + j) * 1024);
}
#endif

__global__ __launch_bounds__(512) void k_fnet(Params p) {
#if defined(__HIP_DEVICE_COMPILE__)
  extern __shared__ __attribute__((aligned(16))) char smem_[];
  ldsp smem = (ldsp)smem_;
  const int lane = threadIdx.x & 63, w = __builtin_amdgcn_readfirstlane(threadIdx.x >> 6), b = blockIdx.x;
  const int tg = (b & 7) * 2 + (b >> 7), c = (b >> 3) & 15, hf = c >> 3, nt = c & 7, odd = nt & 1;
  const int tg3 = REMAP3 ? (b & 6) * 2 + (b >> 6) : tg, c3 = REMAP3 ? (b & 1) * 8 + nt : c;
  const bool seven = c3 < 12;
  const int odd3 = seven ? c3 & 1 : 0, T0 = seven ? 7 * c3 : 84 + 6 * (c3 - 12), Ts = odd3 ? T0 : T0 + 6;
  const int g = lane >> 4;
  const rsrc_t rw = mkrsrc(p.ws, WS_BYTES);
  if (w < 4) {
    TS(0);
    unsigned ep = ld4(rw, OFF_E, 0);
    const v2u* xr = (const v2u*)(p.x + (size_t)(b * 8 + w * 2 + (lane >> 5)) * 1280) + (lane & 31);
    v2u xv[5];
#pragma unroll
    for (int i = 0; i < 5; i++) xv[i] = xr[32 * i];
    if (w == 0 && lane < 4) *(LDS unsigned*)(smem + L0C + lane * 4) = 0;
    waitl0(); bar();
    asm volatile("" : "+v"(ep), "+v"(xv[0]), "+v"(xv[1]), "+v"(xv[2]), "+v"(xv[3]), "+v"(xv[4]));
    const unsigned tag = __builtin_amdgcn_readfirstlane(ep) + 1;
    {
      float v[20], s = 0;
#pragma unroll
      for (int i = 0; i < 5; i++) { v[4 * i] = lo(xv[i][0]); v[4 * i + 1] = hi(xv[i][0]); v[4 * i + 2] = lo(xv[i][1]); v[4 * i + 3] = hi(xv[i][1]); }
#pragma unroll
      for (int i = 0; i < 20; i++) s += v[i];
      const float mean = xsum16(rowsum(s)) * (1.f / 640.f);
      float q = 0;
#pragma unroll
      for (int i = 0; i < 20; i++) { v[i] -= mean; q += v[i] * v[i]; }
      const float rs = 1.0f / sqrtf(xsum16(rowsum(q)) * (1.f / 640.f) + 1e-5f);
#pragma unroll
      for (int i = 0; i < 5; i++)
        *(LDS v2u*)(smem + L0R + (w * 2 + (lane >> 5)) * RS + ((lane & 31) + 32 * i) * 8) = v2u{pk(v[4 * i] * rs, v[4 * i + 1] * rs), pk(v[4 * i + 2] * rs, v[4 * i + 3] * rs)};
      if (w == 0 && lane == 0) *(LDS unsigned*)(smem + L0C + 16) = tag;
      waitl0();
      if (lane == 0) lds_add(smem + L0C);
      lds_wait(smem + L0C, 4);
      TS(1);
      const int qq = (lane & 15) >> 2, pp = lane & 3;
#pragma unroll
      for (int i = 0; i < 3; i++) {
        const int T = 10 * w + 4 * i + (i < 2 ? g : g & 1);
        ldsp a = smem + L0R + qq * RS + piece_col(T, pp, split640(T)) * 2;
        const v4s v0 = __builtin_amdgcn_ds_read_tr16_b64_v4i16((LDS v4s*)a), v1 = __builtin_amdgcn_ds_read_tr16_b64_v4i16((LDS v4s*)(a + 4 * RS));
        st16(rw, i == 2 && g >= 2 ? OOB : OFF_LNF + T * 65536 + (b >> 2) * 1024 + (b & 3) * 256 + (lane & 15) * 16,
             __builtin_bit_cast(v4u, __builtin_shufflevector(v0, v1, 0, 1, 2, 3, 4, 5, 6, 7)));
      }
      waitv<0>();
      unsigned old = 0;
      if (lane == 0) old = lds_add(smem + L0C + 4);
      if (__builtin_amdgcn_readfirstlane(old) == 3 && lane < 2) st4(rw, lane ? OFF_F0P + b * 4 : OFF_F0 + b * FSTR, tag);
      TS(2);
    }
    const int kb0 = 5 * (nt >> 1), sp = ((odd * 2 + (g >> 1)) * 16 + (lane & 15)) * 16 + (g & 1) * 8;
    const int kh = w >> 1, tb0 = tg * 8 + hf * 4 + (w & 1) * 2, tb = tb0 + kh, t = tb * 16 + (lane & 15);
    const int cp = (kb0 + odd * 3) * 32 + g * 8, cs = (kb0 + 2) * 32 + odd * 16 + g * 4;
    sbar();
    const v4u x0 = *(const v4u*)(p.x + (size_t)t * 1280 + cp * 2), x1 = *(const v4u*)(p.x + (size_t)t * 1280 + cp * 2 + 64);
    const v2u x2 = *(const v2u*)(p.x + (size_t)t * 1280 + cs * 2);
    sbar();
    {
      v4f acc[MT1][5];
      bar(); TS(3);
      consume1<MT1>(smem, w, lane, acc);
      TS(4);
      v4f (&o)[2][5] = acc;
      const int tb1 = tg * 8 + w * 2;
      const float sg = hf ? -1.f : 1.f;
#pragma unroll
      for (int mi = 0; mi < 2; mi++) {
        const int dst = OFF_PQF + (tb1 + mi) * 40960 + (hf * 20 + kb0) * 1024;
#pragma unroll
        for (int u = 0; u < 2; u++) {
          const v4f a = o[mi][2 * u] * sg, c2 = o[mi][2 * u + 1] * sg;
          st16(rw, dst + (odd * 3 + u) * 1024 + lane * 16, v4u{pk(a[0], a[1]), pk(a[2], a[3]), pk(c2[0], c2[1]), pk(c2[2], c2[3])});
        }
        const v4f a = o[mi][4] * sg;
        st8(rw, dst + 2048 + sp, v2u{pk(a[0], a[1]), pk(a[2], a[3])});
      }
      TS(50); bar(); TS(51); waitv<0>(); TS(52); bar();
      if (w == 0 && lane == 0) st4(rw, OFF_FG + (tg * 16 + c) * FSTR, tag);
      TS(5);
    }
    v4u h0, h1; v2u h2;
    {
      v4f acc[2][5], o[1][5];
      const int r = lane & 15;
      consume<5, 20, 40960, 2048>(smem + r * 128 + ((kh * 4 + g) ^ (r >> 1)) * 16, rw, OFF_PQF + tb0 * 40960 + kh * 1024 + lane * 16, acc,
        [](int u) { return (130 + 10 * u) % 160 * K1; }, [](int u) { return u == 5 || u == 16; }, [] {}, [&](int u) { if (u < 0) TS(6); if (u == 1) TS(32); if (u == 4) TS(33); if (u == 9) TS(34); if (u == 12) TS(35); if (u == 15) TS(36); if (u == 18) TS(37); });
      TS(7);
      pair_reduce<1>(smem + (100 + 5 * w) * K1 + lane * 16, smem + (100 + 5 * (w ^ 2)) * K1 + lane * 16, w, acc, o);
      v4f (&z)[5] = o[0];
      z[0] += v4f{lo(x0[0]), hi(x0[0]), lo(x0[1]), hi(x0[1])}; z[1] += v4f{lo(x0[2]), hi(x0[2]), lo(x0[3]), hi(x0[3])};
      z[2] += v4f{lo(x1[0]), hi(x1[0]), lo(x1[1]), hi(x1[1])}; z[3] += v4f{lo(x1[2]), hi(x1[2]), lo(x1[3]), hi(x1[3])};
      z[4] += v4f{lo(x2[0]), hi(x2[0]), lo(x2[1]), hi(x2[1])};
      h0 = v4u{pk(z[0][0], z[0][1]), pk(z[0][2], z[0][3]), pk(z[1][0], z[1][1]), pk(z[1][2], z[1][3])};
      h1 = v4u{pk(z[2][0], z[2][1]), pk(z[2][2], z[2][3]), pk(z[3][0], z[3][1]), pk(z[3][2], z[3][3])};
      h2 = v2u{pk(z[4][0], z[4][1]), pk(z[4][2], z[4][3])};
      const int dst = OFF_HF + tb * 20480 + kb0 * 1024;
      st16(rw, dst + odd * 3072 + lane * 16, h0);
      st16(rw, dst + odd * 3072 + 1024 + lane * 16, h1);
      st8(rw, dst + 2048 + sp, h2);
      const v4f s4 = (z[0] + z[1]) + (z[2] + z[3]) + z[4];
      const float m = xsum32(xsum16((s4[0] + s4[1]) + (s4[2] + s4[3]))) * (1.f / 80.f);
      v4f q4 = {0, 0, 0, 0};
#pragma unroll
      for (int j = 0; j < 5; j++) { const v4f d = z[j] - m; q4 += d * d; }
      const float q = xsum32(xsum16((q4[0] + q4[1]) + (q4[2] + q4[3])));
      st8(rw, lane < 16 ? OFF_ST + nt * 16384 + t * 8 : OOB, v2u{asu(m), asu(q)});
      TS(53); bar(); TS(54); waitv<0>(); TS(55); bar();
      if (w == 0 && lane == 0) st4(rw, OFF_FG + (256 + tg * 16 + c) * FSTR, tag);
      TS(8);
    }
    {
      const int k3 = (T0 + odd3) >> 1;
      v4f acc[2][7];
      consume<7, 20, 20480, 1024>(smem + lane * 16, rw, OFF_HF + (tg3 * 8 + w * 2) * 20480 + lane * 16, acc,
        [](int u) { return (10 + 14 * (u >> 1) + (u & 1)) * K1; }, [](int u) { return u == 7 || u == 13; }, [] {}, [&](int u) { if (u < 0) TS(9); if (u == 1) TS(38); if (u == 4) TS(39); if (u == 8) TS(40); if (u == 12) TS(41); if (u == 15) TS(42); if (u == 18) TS(43); });
      TS(10);
#pragma unroll
      for (int mi = 0; mi < 2; mi++) {
        const v2f st = *(LDS v2f*)(smem + L3X + 1024 + (w * 32 + mi * 16 + (lane & 15)) * 8);
        const int dst = OFF_HH + (tg3 * 8 + w * 2 + mi) * 55296;
        unsigned r[14];
#pragma unroll
        for (int j = 0; j < 7; j++) {
          const v4f cs4 = *(LDS v4f*)(smem + L3X + (j * 16 + g * 4) * 4), bias = *(LDS v4f*)(smem + L3X + 512 + (j * 16 + g * 4) * 4);
          v4f z = acc[mi][j] * st[1] + (bias - cs4 * st[0]);
          z = __builtin_elementwise_max(z, v4f{0, 0, 0, 0}); z *= z;
          r[2 * j] = pk(z[0], z[1]); r[2 * j + 1] = pk(z[2], z[3]);
          if (j & 1) st16(rw, dst + (k3 + (j >> 1)) * 1024 + lane * 16, v4u{r[2 * j - 2], r[2 * j - 1], r[2 * j], r[2 * j + 1]});
        }
        st8(rw, seven ? dst + (Ts >> 1) * 1024 + ((odd3 * 2 + (g >> 1)) * 16 + (lane & 15)) * 16 + (g & 1) * 8 : OOB, v2u{r[12], r[13]});
      }
      TS(56); bar(); TS(57); waitv<0>(); TS(58); bar();
      if (w == 0 && lane == 0) st4(rw, OFF_FG + (512 + tg3 * 16 + c3) * FSTR, tag);
      TS(11);
    }
    {
      v4u c0, c1; v2u c2;
      v4f acc[2][5], o[1][5];
      consume<5, 27, 55296, 2048>(smem + kh * 1024 + lane * 16, rw, OFF_HH + tb0 * 55296 + kh * 1024 + lane * 16, acc,
        [](int u) { return u % 15 * 10 * K1; }, [](int u) { return u == 5 || u == 9 || u == 15 || u == 19 || u == 23; }, [&] {
          c0 = *(const v4u*)(p.b2 + cp * 2); c1 = *(const v4u*)(p.b2 + cp * 2 + 64); c2 = *(const v2u*)(p.b2 + cs * 2); }, [&](int u) { if (u < 0) TS(12); if (u == 1) TS(44); if (u == 6) TS(45); if (u == 11) TS(46); if (u == 16) TS(47); if (u == 21) TS(48); if (u == 25) TS(49); });
      TS(13);
      pair_reduce<1>(smem + w * 5120 + lane * 16, smem + (w ^ 2) * 5120 + lane * 16, w, acc, o);
      v4f (&z)[5] = o[0];
#define FIN(a, e, h, c) pk(a[e] + lo(h) + lo(c), a[e + 1] + hi(h) + hi(c))
      char* dst = p.out + (size_t)t * 1280;
      *(v4u*)(dst + cp * 2) = v4u{FIN(z[0], 0, h0[0], c0[0]), FIN(z[0], 2, h0[1], c0[1]), FIN(z[1], 0, h0[2], c0[2]), FIN(z[1], 2, h0[3], c0[3])};
      *(v4u*)(dst + cp * 2 + 64) = v4u{FIN(z[2], 0, h1[0], c1[0]), FIN(z[2], 2, h1[1], c1[1]), FIN(z[3], 0, h1[2], c1[2]), FIN(z[3], 2, h1[3], c1[3])};
      *(v2u*)(dst + cs * 2) = v2u{FIN(z[4], 0, h2[0], c2[0]), FIN(z[4], 2, h2[1], c2[1])};
#undef FIN
      TS(14);
    }
  } else if (w < 6) {
    const int lw = w - 4;
    int vt[5];
#pragma unroll
    for (int i = 0; i < 5; i++) { const int j = (lw * 5 + i) >> 1; vt[i] = 5 * nt + (odd ? (j + 1) % 5 : j); }
    unsigned tag;
    int vd[5];
#pragma unroll
    for (int i = 0; i < 5; i++) {
      const int row = (lw * 5 + i) * 8 + (lane >> 3), j = row >> 4, T = 5 * nt + (odd ? (j + 1) % 5 : j), n = row & 15;
      vd[i] = (piece_col(T, n >> 2, j == 4) + (n & 3)) * 1280 + ((lane & 7) ^ ((row >> 1) & 7)) * 16;
    }
    auto e2 = [&](int s0, int s1) {
#pragma unroll 1
      for (int s = s0; s < s1; s++) {
        const rsrc_t r = mkrsrc(s < 10 ? p.dr : p.di - 1280, D_ * D_ * 2 + (s < 10 ? 0 : 1280));
        ldsp d = smem + ((s + 13) & 15) * 10240 + lw * 5120;
#pragma unroll
        for (int i = 0; i < 5; i++) dma<0>(r, d + i * 1024, vd[i], s * 128);
      }
    };
    {
      const rsrc_t ra = mkrsrc((hf ? p.ci : p.cr) + (size_t)tg * 128 * 4096, 128 * 4096);
      int va[8], vb[5];
#pragma unroll
      for (int i = 0; i < 8; i++) { const int row = (lw * 8 + i) * 8 + (lane >> 3); va[i] = row * 4096 + ((lane & 7) ^ ((row >> 1) & 7)) * 16; }
#pragma unroll
      for (int i = 0; i < 5; i++) vb[i] = OFF_LNF + vt[i] * 65536 + ((lw + i) & 1) * 1024 + lane * 16;
      auto l1 = [&](int s, int so) {
#pragma unroll
        for (int i = 0; i < 5; i++) dma<SC1>(rw, smem + so + (16 + lw * 5 + i) * 1024, vb[i],
          ((s + 4 * nt) & 31) * 2048
          );
      };
      auto e1 = [&](int s, int so, bool wa) {
        if (wa) {
#pragma unroll
          for (int i = 0; i < 8; i++) dma<0>(ra, smem + so + (lw * 8 + i) * 1024, va[i], ((s + 4 * nt) & 31) * 128);
        }
        l1(s, so);
      };
      bar();
#pragma unroll
      for (int s = 0; s < NPRE; s++)
#pragma unroll
        for (int i = 0; i < 8; i++) { dma<0>(ra, smem + (s + 3) % 5 * L1S + (lw * 8 + i) * 1024, va[i], (s + 4 * nt) * 128); if (s * 8 + i >= CTH - 1) waitv<CTH - 1>(); }
      lds_wait(smem + L0C, 4);
      tag = __builtin_amdgcn_readfirstlane(*(volatile LDS unsigned*)(smem + L0C + 16));
      TS(16);
      polln<0xffffffffu>(rw, lane < 32 ? OFF_F0 + (32 * nt + lane) * FSTR : OOB, tag);
      TS(17);
      constexpr int N2 = NPRE > 2 ? 5 : 13, N3 = NPRE > 3 ? 5 : 13;
      l1(0, 3 * L1S);
      v4u fp = ld16_async(p.ws, OFF_F0P + lane * 16);
      l1(1, 4 * L1S); e1(2, 0, NPRE < 3);
      waitv<6 + N2>(); bar();
      e1(3, L1S, NPRE < 4);
      waitv<N2 + N3>();
      asm volatile("" : "+v"(fp));
      if (lw == 0) {
        if (!eq4(fp, tag)) poll256(rw, OFF_F0P + lane * 16, tag);
        if (b == 0 && lane == 0) st4(rw, OFF_E, tag);
      }
      bar();
      e1(4, 2 * L1S, true); waitv<N3 + 13>(); bar();
      int so = 3 * L1S;
#pragma unroll 1
      for (int s = 5; s < 32; s++) {
        e1(s, so, true); so += L1S; if (so == 5 * L1S) so = 0;
        waitv<26>(); bar();
      }
      e2(0, 5); waitv<38>(); bar();
      e2(5, TD < 8 ? TD : 8); waitv<5 * (TD < 8 ? TD : 8)>(); bar();
      e2(8, TD);
      if (MT1 == 4) { bar(); bar(); }
      bar();
      e2(TD, 16);
      TS(59); bar();
    }
    int vb3[7];
#pragma unroll
    for (int i = 0; i < 7; i++) { const int f = lw * 7 + i, j = f >> 1, T = j < 6 ? T0 + odd3 + j : Ts;
      vb3[i] = (j < 6 || seven) ? OFF_W1F + T * 20480 + (f & 1) * 1024 + lane * 16 : OOB; }
    auto e3 = [&](int s0, int s1) {
#pragma unroll 1
      for (int s = s0; s < s1; s++) {
        ldsp d = smem + (10 + 14 * s + lw * 7) * K1;
#pragma unroll
        for (int i = 0; i < 7; i++) dma<SC1>(rw, d + i * 1024, vb3[i], s * 2048);
      }
    };
    const int fl = lane < 16 ? OFF_FG + lane * FSTR : OOB, i2 = lw * 64 + lane;
    unsigned cpart[20];
    unsigned short bb;
    {
      if (lw == 0) polln<0xffffu>(rw, fl + tg * 16 * FSTR, tag); else waitv<0>();
      TS(18);
      bar();
      v4u fp = ld16_async(p.ws, OFF_FWP + lane * 16);
      bar();
      e2(16, 20);
      waitv<0>();
      asm volatile("" : "+v"(fp));
      if (lw == 0 && !eq4(fp, tag)) poll256(rw, OFF_FWP + lane * 16, tag);
      bar();
      e3(0, TW);
      bar(); bar();
      const int j = i2 >> 4, T = j < 6 ? T0 + odd3 + j : Ts;
      const bool on = i2 < (seven ? 112 : 96);
      const int vc = on ? OFF_CS + (T * 16 + (i2 & 15)) * 4 : OOB;
      e3(TW, 10);
      bb = __builtin_amdgcn_raw_buffer_load_b16(mkrsrc(p.b1, F_ * 2), on ? (piece_col(T, (i2 & 15) >> 2, split1728(T)) + (i2 & 3)) * 2 : OOB, 0, 0);
#pragma unroll
      for (int k = 0; k < 20; k++) cpart[k] = ld4(rw, vc, k * F_ * 4);
      TS(60); bar();
    }
    int vb4[5];
#pragma unroll
    for (int i = 0; i < 5; i++) vb4[i] = OFF_W2F + vt[i] * 55296 + ((lw + i) & 1) * 1024 + lane * 16;
    auto e4 = [&](int s0, int s1) {
#pragma unroll 1
      for (int s = s0; s < s1; s++) {
        ldsp d = smem + (s >= 15 ? s - 15 : s) * 10240 + lw * 5120;
#pragma unroll
        for (int i = 0; i < 5; i++) dma<SC1>(rw, d + i * 1024, vb4[i], s * 2048);
      }
    };
    {
      if (lw == 0) polln<0xffffu>(rw, fl + (256 + tg3 * 16) * FSTR, tag); else waitv<0>();
      TS(19);
      bar();
      v2u sv[8];
#pragma unroll
      for (int k = 0; k < 8; k++) sv[k] = ld8(rw, OFF_ST + (tg3 * 128 + i2) * 8, k * 16384);
      e4(0, 1);
      float csum = 0, m = 0, q = 0;
#pragma unroll
      for (int k = 0; k < 20; k++) { asm volatile("" : "+v"(cpart[k])); csum += asf(cpart[k]); }
      const float bias = lo(bb);
#pragma unroll
      for (int k = 0; k < 8; k++) { asm volatile("" : "+v"(sv[k])); m += asf(sv[k][0]); q += asf(sv[k][1]); }
      m *= 0.125f;
#pragma unroll
      for (int k = 0; k < 8; k++) { const float d = asf(sv[k][0]) - m; q += 80.f * d * d; }
      const float rs = 1.0f / sqrtf(q * (1.f / 640.f) + 1e-5f);
      const unsigned a0 = (unsigned)(size_t)(smem + L3X + i2 * 4), a1 = (unsigned)(size_t)(smem + L3X + 1024 + i2 * 8);
      asm volatile("ds_write_b32 %0, %1\n ds_write_b32 %0, %2 offset:512\n ds_write_b64 %3, %4\n s_waitcnt lgkmcnt(0)" ::"v"(a0), "v"(csum), "v"(bias), "v"(a1), "v"(v2f{m * rs, rs}) : "memory");
      bar();
      e4(1, TX1);
      bar();
      e4(TX1, TX2);
      bar();
      e4(TX2, 15);
      TS(61); bar();
    }
    {
      if (lw == 0) polln<0xffffu>(rw, fl + (512 + tg * 16) * FSTR, tag); else waitv<0>();
      TS(20);
      bar();
      bar();
      e4(15, 19); bar();
      e4(19, 23); waitv<20>(); bar();
      e4(23, 27); waitv<20>(); bar();
      waitv<0>(); bar();
    }
    if (w == 4) { TSOUT(16, 5); TSOUT(59, 3); }
  } else {
    const int hw = w - 6, nu = hw == 0 && b < 56 ? 3 : 2;
    if (hw == 0) {
      unsigned long long pc; unsigned back;
      asm volatile("s_getpc_b64 %0\n.Lfnet_pc:\n s_mov_b32 %1, .Lfnet_pc-_Z6k_fnet6Params" : "=s"(pc), "=s"(back));
      dma<0>(mkrsrc((const void*)(pc - back), CODELEN), smem + L0D, (b >> 3) * 1024 + lane * 16, 0);
    }
    bar();
#pragma unroll 1
    for (int n = 0; n < nu; n++) {
      const int i = n < 2 ? hw * 2 + n : 4, u = b + 256 * i;
      const bool isw1 = u < 540;
      const int uu = isw1 ? u : u - 540, kb = isw1 ? uu / 27 : uu / 10, cu = isw1 ? uu % 27 : uu % 10, ld = isw1 ? F_ * 2 : D_ * 2;
      const rsrc_t r = mkrsrc(isw1 ? p.w1 : p.w2, D_ * F_ * 2);
#pragma unroll
      for (int k = 0; k < 4; k++) dma<0>(r, smem + L0W + i * 4096 + k * 1024, (kb * 32 + k * 8 + (lane >> 3)) * ld + cu * 128 + (lane & 7) * 16, 0);
    }
    waitv<0>();
    TS(22);
    lds_wait(smem + L0C + 4, 4);
    const int qq = (lane & 15) >> 2, pp = lane & 3;
#pragma unroll 1
    for (int n = 0; n < nu; n++) {
      const int i = n < 2 ? hw * 2 + n : 4, u = b + 256 * i;
      const bool isw1 = u < 540;
      const int uu = isw1 ? u : u - 540, kb = isw1 ? uu / 27 : uu / 10, cu = isw1 ? uu % 27 : uu % 10;
      unsigned a[4];
#pragma unroll
      for (int t = 0; t < 4; t++) { const int T = cu * 4 + t;
        a[t] = (unsigned)(size_t)(smem + L0W + i * 4096 + (g * 8 + qq) * 128 + (piece_col(T, pp, isw1 ? split1728(T) : split640(T)) - cu * 64) * 2); }
      v2u v[8];
      asm volatile("ds_read_b64_tr_b16 %0, %8\n ds_read_b64_tr_b16 %1, %8 offset:512\n ds_read_b64_tr_b16 %2, %9\n ds_read_b64_tr_b16 %3, %9 offset:512\n"
                   "ds_read_b64_tr_b16 %4, %10\n ds_read_b64_tr_b16 %5, %10 offset:512\n ds_read_b64_tr_b16 %6, %11\n ds_read_b64_tr_b16 %7, %11 offset:512\n s_waitcnt lgkmcnt(0)"
                   : "=&v"(v[0]), "=&v"(v[1]), "=&v"(v[2]), "=&v"(v[3]), "=&v"(v[4]), "=&v"(v[5]), "=&v"(v[6]), "=&v"(v[7]) : "v"(a[0]), "v"(a[1]), "v"(a[2]), "v"(a[3]) : "memory");
      const int dst = (isw1 ? OFF_W1F + cu * 4 * 20480 : OFF_W2F + cu * 4 * 55296) + kb * 1024 + lane * 16;
#pragma unroll
      for (int t = 0; t < 4; t++) st16(rw, dst + t * (isw1 ? 20480 : 55296), v4u{v[2 * t][0], v[2 * t][1], v[2 * t + 1][0], v[2 * t + 1][1]});
      if (isw1) {
        float cs = 0;
#pragma unroll
        for (int t = 0; t < 4; t++) {
          const v2u v0 = v[2 * t], v1 = v[2 * t + 1];
          const float c1 = xsum32(xsum16((lo(v0[0]) + hi(v0[0])) + (lo(v0[1]) + hi(v0[1])) + (lo(v1[0]) + hi(v1[0])) + (lo(v1[1]) + hi(v1[1]))));
          cs = g == t ? c1 : cs;
        }
        st4(rw, OFF_CS + (kb * F_ + cu * 64 + lane) * 4, asu(cs));
      }
    }
    TS(23);
#pragma unroll
    for (int k = 0; k < 4; k++) {
      const int q = b + 256 * (hw * 3 + k);
      const rsrc_t r = hw || k < 3 ? mkrsrc(q < 800 ? p.dr + q * 1024 : p.di + (q - 800) * 1024, q < 1600 ? 1024 : 0)
                                   : mkrsrc(b & 32 ? p.b2 : p.b1, b & 32 ? D_ * 2 : F_ * 2);
      dma<0>(r, smem + L0D, lane * 16 + (hw || k < 3 ? 0 : (b >> 3 & 3) * 1024), 0);
    }
    waitv<4>();
    const unsigned tag = *(volatile LDS unsigned*)(smem + L0C + 16);
    unsigned old = 0;
    if (lane == 0) old = lds_add(smem + L0C + 8);
    if (__builtin_amdgcn_readfirstlane(old) == 1 && lane == 0) st4(rw, OFF_FWP + b * 4, tag);
    TS(21);
    bar(); bar(); bar();
    waitv<0>();
    if (w == 6) TSOUT(21, 3);
  }
#endif
}

extern "C" void kernel_launch(void* const* d_in, const int* in_sizes, int n_in, void* d_out, int out_size, void* d_ws, size_t ws_size,
                              hipStream_t stream) {
  Params p;
  p.x = (const char*)d_in[0]; p.cr = (const char*)d_in[1]; p.ci = (const char*)d_in[2]; p.dr = (const char*)d_in[3]; p.di = (const char*)d_in[4];
  p.w1 = (const char*)d_in[6]; p.b1 = (const char*)d_in[7]; p.w2 = (const char*)d_in[8]; p.b2 = (const char*)d_in[9];
  p.out = (char*)d_out; p.ws = (char*)d_ws;
  (void)hipFuncSetAttribute((const void*)k_fnet, hipFuncAttributeMaxDynamicSharedMemorySize, LDS_BYTES);
  k_fnet<<<256, 512, LDS_BYTES, stream>>>(p);
}
```

```cpp
#include <hip/hip_runtime.h>
#include <hip/hip_bf16.h>
#include <stdint.h>

#define CTH 8
#define MT1 2
#define NPRE 3
#define TD 8
#define TW 4
#define TX1 4
#define TX2 9
#define UD 6
#define REMAP3 1
#define CODELEN 24000

#define LDS __attribute__((address_space(3)))
typedef LDS char* ldsp;
typedef short v4s __attribute__((ext_vector_type(4)));
typedef short v8s __attribute__((ext_vector_type(8)));
typedef __bf16 v8b __attribute__((ext_vector_type(8)));
typedef __bf16 v2b __attribute__((ext_vector_type(2)));
typedef float v4f __attribute__((ext_vector_type(4)));
typedef float v2f __attribute__((ext_vector_type(2)));
typedef unsigned v4u __attribute__((ext_vector_type(4)));
typedef unsigned v2u __attribute__((ext_vector_type(2)));

constexpr int S_ = 2048, D_ = 640, F_ = 1728;
constexpr int OFF_E   = 0;
constexpr int FSTR    = 128;
constexpr int OFF_F0  = 4096;
constexpr int OFF_F0P = 1024;
constexpr int OFF_FWP = 2048;
constexpr int OFF_FG  = OFF_F0 + 256 * FSTR;
constexpr int OFF_LNF = 256 * 1024;
constexpr int OFF_PQF = OFF_LNF + 40 * 64 * 1024;
constexpr int OFF_HF  = OFF_PQF + 128 * 40 * 1024;
constexpr int OFF_HH  = OFF_HF + 128 * 20 * 1024;
constexpr int OFF_W1F = OFF_HH + 128 * 54 * 1024;
constexpr int OFF_W2F = OFF_W1F + 108 * 20 * 1024;
constexpr int OFF_CS  = OFF_W2F + 40 * 54 * 1024;
constexpr int OFF_ST  = OFF_CS + 20 * 1728 * 4;
constexpr int WS_BYTES = OFF_ST + 8 * 2048 * 8;
constexpr int OOB = 0x40000000;
constexpr int LDS_BYTES = 160 * 1024;

struct Params {
  const char* x; const char* cr; const char* ci; const char* dr; const char* di;
  const char* w1; const char* b1; const char* w2; const char* b2;
  char* out; char* ws;
};

#define TS(i) do { if (i == 0) ta_ = (unsigned)__builtin_amdgcn_s_memrealtime(); if (i == 0) tb_ = (unsigned)__builtin_amdgcn_s_memrealtime(); } while (0)
#define TSOUT(a, n)

#if defined(__HIP_DEVICE_COMPILE__)
typedef __amdgpu_buffer_rsrc_t rsrc_t;
constexpr int SC1 = 16;
__device__ __forceinline__ rsrc_t mkrsrc(const void* p, unsigned bytes) { return __builtin_amdgcn_make_buffer_rsrc((void*)p, 0, bytes, 0x00020000); }
template <int AUX> __device__ __forceinline__ void dma(rsrc_t r, ldsp dst, int voff, int soff) {
  __builtin_amdgcn_raw_ptr_buffer_load_lds(r, (LDS void*)dst, 16, voff, soff, 0, AUX);
}
__device__ __forceinline__ void st16(rsrc_t r, int off, v4u v) { __builtin_amdgcn_raw_buffer_store_b128(v, r, off, 0, SC1); }
__device__ __forceinline__ void st8(rsrc_t r, int off, v2u v) { __builtin_amdgcn_raw_buffer_store_b64(v, r, off, 0, SC1); }
__device__ __forceinline__ void st4(rsrc_t r, int off, unsigned v) { __builtin_amdgcn_raw_buffer_store_b32(v, r, off, 0, SC1); }
__device__ __forceinline__ v4u ld16(rsrc_t r, int off) { return __builtin_amdgcn_raw_buffer_load_b128(r, off, 0, SC1); }
__device__ __forceinline__ v2u ld8(rsrc_t r, int off, int soff) { return __builtin_amdgcn_raw_buffer_load_b64(r, off, soff, SC1); }
__device__ __forceinline__ unsigned ld4(rsrc_t r, int off, int soff) { return __builtin_amdgcn_raw_buffer_load_b32(r, off, soff, SC1); }
template <int N> __device__ __forceinline__ void waitv() { asm volatile("s_waitcnt vmcnt(%0)" ::"n"(N) : "memory"); }
__device__ __forceinline__ void bar() { asm volatile("" ::: "memory"); __builtin_amdgcn_s_barrier(); asm volatile("" ::: "memory"); }
__device__ __forceinline__ void waitl0() { __builtin_amdgcn_sched_barrier(0); __builtin_amdgcn_s_waitcnt(0xC07F); __builtin_amdgcn_sched_barrier(0); }
__device__ __forceinline__ v4f mfma(v8s a, v8s b, v4f c) {
  return __builtin_amdgcn_mfma_f32_16x16x32_bf16(__builtin_bit_cast(v8b, a), __builtin_bit_cast(v8b, b), c, 0, 0, 0);
}
__device__ __forceinline__ unsigned pk(float a, float b) { v2b r = {(__bf16)a, (__bf16)b}; return __builtin_bit_cast(unsigned, r); }
__device__ __forceinline__ float asf(unsigned u) { return __builtin_bit_cast(float, u); }
__device__ __forceinline__ unsigned asu(float f) { return __builtin_bit_cast(unsigned, f); }
__device__ __forceinline__ float lo(unsigned u) { return asf(u << 16); }
__device__ __forceinline__ float hi(unsigned u) { return asf(u & 0xffff0000u); }
template <int CTRL> __device__ __forceinline__ float dppadd(float v) { return v + asf(__builtin_amdgcn_update_dpp(0, asu(v), CTRL, 0xf, 0xf, true)); }
__device__ __forceinline__ float rowsum(float v) { return dppadd<0x121>(dppadd<0x122>(dppadd<0x124>(dppadd<0x128>(v)))); }
__device__ __forceinline__ float xsum16(float v) { const auto r = __builtin_amdgcn_permlane16_swap(asu(v), asu(v), false, false); return asf(r[0]) + asf(r[1]); }
__device__ __forceinline__ float xsum32(float v) { const auto r = __builtin_amdgcn_permlane32_swap(asu(v), asu(v), false, false); return asf(r[0]) + asf(r[1]); }
template <int NM, int ND> __device__ __forceinline__ void ilv() {
#pragma unroll
  for (int i = 0; i < NM; i++) { __builtin_amdgcn_sched_group_barrier(0x8, 1, 0); if (i < ND) __builtin_amdgcn_sched_group_barrier(0x100, 1, 0); }
}
__device__ __forceinline__ int piece_col(int T, int p, bool split) { return 16 * T + 4 * p + ((4 * p - 12 * (T & 1)) & ((int)split - 1)); }
__device__ __forceinline__ bool split640(int T) { return (T >> 1) % 5 == 2; }
__device__ __forceinline__ bool split1728(int T) { return (T >> 1) % 7 == 3 && T < 84; }
__device__ __forceinline__ unsigned lds_add(ldsp a) { return __hip_atomic_fetch_add((LDS unsigned*)a, 1u, __ATOMIC_RELAXED, __HIP_MEMORY_SCOPE_WORKGROUP); }
__device__ __forceinline__ void lds_wait(ldsp a, unsigned v) { while (*(volatile LDS unsigned*)a != v) __builtin_amdgcn_s_sleep(1); asm volatile("" ::: "memory"); }
constexpr int SPIN_CAP = 1 << 17;
__device__ __forceinline__ v4u ld16_async(const char* base, unsigned off) { v4u v; asm volatile("global_load_dwordx4 %0, %1, %2 sc1" : "=v"(v) : "v"(off), "s"(base) : "memory"); return v; }
__device__ __forceinline__ void sbar() { __builtin_amdgcn_sched_barrier(0); }
__device__ __forceinline__ bool eq4(v4u v, unsigned tag) { return __builtin_amdgcn_ballot_w64(v[0] != tag || v[1] != tag || v[2] != tag || v[3] != tag) == 0; }
__device__ __forceinline__ void poll256(rsrc_t r, int off, unsigned tag) {
#pragma unroll 1
  for (int it = 0; it < SPIN_CAP; it++) { asm volatile("" ::: "memory"); if (eq4(ld16(r, off), tag)) break; __builtin_amdgcn_s_sleep(2); }
  asm volatile("" ::: "memory");
}
template <unsigned MASK> __device__ __forceinline__ void polln(rsrc_t r, int off, unsigned tag) {
#pragma unroll 1
  for (int it = 0; it < SPIN_CAP; it++) { asm volatile("" ::: "memory"); if (((unsigned)__builtin_amdgcn_ballot_w64(ld4(r, off, 0) != tag) & MASK) == 0) break; __builtin_amdgcn_s_sleep(1); }
  asm volatile("" ::: "memory");
}

constexpr int K1 = 1024, L1S = 26 * K1, L1R = 90 * K1, L3X = 150 * K1;
constexpr int RS = 1312;
constexpr int L0R = 52 * K1, L0W = 130 * K1, L0C = 150 * K1, L0D = 152 * K1;

template <int MT>
__device__ __forceinline__ void consume1(ldsp smem, int w, int lane, v4f (&acc)[MT][5]) {
  const int g = lane >> 4, r = lane & 15;
  const int kh = MT == 4 ? w >> 1 : 0, rb = MT == 4 ? (w & 1) * 4 : w * 2;
  const int a0 = (rb * 16 + r) * 128 + (((kh * 4 + g) ^ (r >> 1)) * 16), a1 = MT == 4 ? a0 : a0 ^ 64;
  const int b0 = 16 * K1 + kh * K1 + lane * 16, b1 = MT == 4 ? b0 : b0 + K1;
  v8s pa[MT], pb[5], qa[MT], qb[5];
#pragma unroll
  for (int i = 0; i < MT; i++) { qa[i] = v8s{0, 0, 0, 0, 0, 0, 0, 0};
#pragma unroll
    for (int j = 0; j < 5; j++) acc[i][j] = v4f{0, 0, 0, 0}; }
#pragma unroll
  for (int j = 0; j < 5; j++) qb[j] = v8s{0, 0, 0, 0, 0, 0, 0, 0};
#define RD(A, B, ao, bo) { B[0] = *(LDS v8s*)(smem + so + bo); _Pragma("unroll") for (int i = 0; i < MT; i++) A[i] = *(LDS v8s*)(smem + so + ao + i * 2048); \
                           _Pragma("unroll") for (int j = 1; j < 5; j++) B[j] = *(LDS v8s*)(smem + so + bo + j * 2048); }
#define MM(A, B) { _Pragma("unroll") for (int j = 0; j < 5; j++) _Pragma("unroll") for (int i = 0; i < MT; i++) acc[i][j] = mfma(B[j], A[i], acc[i][j]); }
#define NEXT { bar(); so += L1S; if (so == 5 * L1S) so = 0; }
  int so = 3 * L1S;
#pragma unroll 1
  for (int s = 0; s < (MT == 4 ? 15 : 31); s++) {
    RD(pa, pb, a0, b0); MM(qa, qb); ilv<5 * MT, 5 + MT>(); sbar();
    if (MT == 4) NEXT
    RD(qa, qb, a1, b1); MM(pa, pb); ilv<5 * MT, 5 + MT>(); sbar();
    NEXT
  }
  RD(pa, pb, a0, b0); MM(qa, qb); ilv<5 * MT, 5 + MT>(); sbar();
  if (MT == 4) NEXT
  RD(qa, qb, a1, b1); MM(pa, pb); ilv<5 * MT, 5 + MT>();
  if (MT == 4) { waitl0(); bar(); } else sbar();
  MM(qa, qb);
#undef RD
#undef MM
#undef NEXT
}

template <int NJ, int NU, int ASTR, int AUS, class FB, class FS, class FX, class FT>
__device__ __forceinline__ void consume(ldsp bl0, rsrc_t rw, int va0, v4f (&acc)[2][NJ], FB boff, FS sync, FX extra, FT stamp) {
  v8s a[UD][2], bq[2][NJ];
  int va[2] = {va0, va0 + ASTR};
  ldsp bl[3] = {bl0, bl0 + 65536, bl0 + 131072};
  asm volatile("" : "+v"(va[0]), "+v"(va[1]), "+v"(bl[0]), "+v"(bl[1]), "+v"(bl[2]));
  bar();
  stamp(-1);
#define LDA(u) _Pragma("unroll") for (int i = 0; i < 2; i++) a[(u) % UD][i] = __builtin_bit_cast(v8s, __builtin_amdgcn_raw_buffer_load_b128(rw, va[i], (u) * AUS, SC1));
#define LDB(u) _Pragma("unroll") for (int j = 0; j < NJ; j++) bq[(u) & 1][j] = *(LDS v8s*)(bl[(boff(u) + j * 2048) >> 16] + ((boff(u) + j * 2048) & 0xffff));
  LDA(0) sbar(); LDB(0) sbar();
#pragma unroll
  for (int u = 1; u < UD - 1; u++) { LDA(u) sbar(); }
  extra();
  sbar();
#pragma unroll
  for (int u = 1; u <= NU; u++) {
    if (u < NU) { if (sync(u)) bar(); LDB(u) }
    const bool ld = u - 2 + UD < NU;
    if (ld) { LDA(u - 2 + UD) }
#pragma unroll
    for (int j = 0; j < NJ; j++)
#pragma unroll
      for (int i = 0; i < 2; i++) acc[i][j] = mfma(bq[(u - 1) & 1][j], a[(u - 1) % UD][i], u == 1 ? v4f{0, 0, 0, 0} : acc[i][j]);
#pragma unroll
    for (int k = 0; k < 2 * NJ; k++) {
      __builtin_amdgcn_sched_group_barrier(0x8, 1, 0);
      if (u < NU && k < NJ) __builtin_amdgcn_sched_group_barrier(0x100, 1, 0);
      if (ld && k >= NJ && k < NJ + 2) __builtin_amdgcn_sched_group_barrier(0x20, 1, 0);
    }
    sbar();
    stamp(u);
  }
#undef LDA
#undef LDB
}

template <int N>
__device__ __forceinline__ void pair_reduce(ldsp mine, ldsp other, int w, v4f (&acc)[2 * N][5], v4f (&o)[N][5]) {
  const int keep = w >> 1;
#pragma unroll
  for (int i = 0; i < N; i++)
#pragma unroll
    for (int j = 0; j < 5; j++) *(LDS v4f*)(mine + (i * 5 + j) * 1024) = keep ? acc[i][j] : acc[N + i][j];
  waitl0(); bar();
#pragma unroll
  for (int i = 0; i < N; i++)
#pragma unroll
    for (int j = 0; j < 5; j++) o[i][j] = (keep ? acc[N + i][j] : acc[i][j]) + *(LDS v4f*)(other + (i * 5 + j) * 1024);
}
#endif

__global__ __launch_bounds__(512) void k_fnet(Params p) {
#if defined(__HIP_DEVICE_COMPILE__)
  extern __shared__ __attribute__((aligned(16))) char smem_[];
  ldsp smem = (ldsp)smem_;
  const int lane = threadIdx.x & 63, w = __builtin_amdgcn_readfirstlane(threadIdx.x >> 6), b = blockIdx.x;
  const int tg = (b & 7) * 2 + (b >> 7), c = (b >> 3) & 15, hf = c >> 3, nt = c & 7, odd = nt & 1;
  const int tg3 = REMAP3 ? (b & 6) * 2 + (b >> 6) : tg, c3 = REMAP3 ? (b & 1) * 8 + nt : c;
  const bool seven = c3 < 12;
  const int odd3 = seven ? c3 & 1 : 0, T0 = seven ? 7 * c3 : 84 + 6 * (c3 - 12), Ts = odd3 ? T0 : T0 + 6;
  const int g = lane >> 4;
  const rsrc_t rw = mkrsrc(p.ws, WS_BYTES);
  unsigned ta_ = 0, tb_ = 0;
  if (w < 4) {
    TS(0);
    unsigned ep = ld4(rw, OFF_E, 0);
    const v2u* xr = (const v2u*)(p.x + (size_t)(b * 8 + w * 2 + (lane >> 5)) * 1280) + (lane & 31);
    v2u xv[5];
#pragma unroll
    for (int i = 0; i < 5; i++) xv[i] = xr[32 * i];
    if (w == 0 && lane < 4) *(LDS unsigned*)(smem + L0C + lane * 4) = 0;
    waitl0(); bar();
    asm volatile("" : "+v"(ep), "+v"(xv[0]), "+v"(xv[1]), "+v"(xv[2]), "+v"(xv[3]), "+v"(xv[4]));
    const unsigned tag = __builtin_amdgcn_readfirstlane(ep) + 1;
    {
      float v[20], s = 0;
#pragma unroll
      for (int i = 0; i < 5; i++) { v[4 * i] = lo(xv[i][0]); v[4 * i + 1] = hi(xv[i][0]); v[4 * i + 2] = lo(xv[i][1]); v[4 * i + 3] = hi(xv[i][1]); }
#pragma unroll
      for (int i = 0; i < 20; i++) s += v[i];
      const float mean = xsum16(rowsum(s)) * (1.f / 640.f);
      float q = 0;
#pragma unroll
      for (int i = 0; i < 20; i++) { v[i] -= mean; q += v[i] * v[i]; }
      const float rs = 1.0f / sqrtf(xsum16(rowsum(q)) * (1.f / 640.f) + 1e-5f);
#pragma unroll
      for (int i = 0; i < 5; i++)
        *(LDS v2u*)(smem + L0R + (w * 2 + (lane >> 5)) * RS + ((lane & 31) + 32 * i) * 8) = v2u{pk(v[4 * i] * rs, v[4 * i + 1] * rs), pk(v[4 * i + 2] * rs, v[4 * i + 3] * rs)};
      if (w == 0 && lane == 0) *(LDS unsigned*)(smem + L0C + 16) = tag;
      waitl0();
      if (lane == 0) lds_add(smem + L0C);
      lds_wait(smem + L0C, 4);
      TS(1);
      const int qq = (lane & 15) >> 2, pp = lane & 3;
#pragma unroll
      for (int i = 0; i < 3; i++) {
        const int T = 10 * w + 4 * i + (i < 2 ? g : g & 1);
        ldsp a = smem + L0R + qq * RS + piece_col(T, pp, split640(T)) * 2;
        const v4s v0 = __builtin_amdgcn_ds_read_tr16_b64_v4i16((LDS v4s*)a), v1 = __builtin_amdgcn_ds_read_tr16_b64_v4i16((LDS v4s*)(a + 4 * RS));
        st16(rw, i == 2 && g >= 2 ? OOB : OFF_LNF + T * 65536 + (b >> 2) * 1024 + (b & 3) * 256 + (lane & 15) * 16,
             __builtin_bit_cast(v4u, __builtin_shufflevector(v0, v1, 0, 1, 2, 3, 4, 5, 6, 7)));
      }
      waitv<0>();
      unsigned old = 0;
      if (lane == 0) old = lds_add(smem + L0C + 4);
      if (__builtin_amdgcn_readfirstlane(old) == 3 && lane < 2) st4(rw, lane ? OFF_F0P + b * 4 : OFF_F0 + b * FSTR, tag);
      TS(2);
    }
    const int kb0 = 5 * (nt >> 1), sp = ((odd * 2 + (g >> 1)) * 16 + (lane & 15)) * 16 + (g & 1) * 8;
    const int kh = w >> 1, tb0 = tg * 8 + hf * 4 + (w & 1) * 2, tb = tb0 + kh, t = tb * 16 + (lane & 15);
    const int cp = (kb0 + odd * 3) * 32 + g * 8, cs = (kb0 + 2) * 32 + odd * 16 + g * 4;
    sbar();
    const v4u x0 = *(const v4u*)(p.x + (size_t)t * 1280 + cp * 2), x1 = *(const v4u*)(p.x + (size_t)t * 1280 + cp * 2 + 64);
    const v2u x2 = *(const v2u*)(p.x + (size_t)t * 1280 + cs * 2);
    sbar();
    {
      v4f acc[MT1][5];
      bar(); TS(3);
      consume1<MT1>(smem, w, lane, acc);
      TS(4);
      v4f (&o)[2][5] = acc;
      const int tb1 = tg * 8 + w * 2;
      const float sg = hf ? -1.f : 1.f;
#pragma unroll
      for (int mi = 0; mi < 2; mi++) {
        const int dst = OFF_PQF + (tb1 + mi) * 40960 + (hf * 20 + kb0) * 1024;
#pragma unroll
        for (int u = 0; u < 2; u++) {
          const v4f a = o[mi][2 * u] * sg, c2 = o[mi][2 * u + 1] * sg;
          st16(rw, dst + (odd * 3 + u) * 1024 + lane * 16, v4u{pk(a[0], a[1]), pk(a[2], a[3]), pk(c2[0], c2[1]), pk(c2[2], c2[3])});
        }
        const v4f a = o[mi][4] * sg;
        st8(rw, dst + 2048 + sp, v2u{pk(a[0], a[1]), pk(a[2], a[3])});
      }
      TS(50); bar(); TS(51); waitv<0>(); TS(52); bar();
      if (w == 0 && lane == 0) st4(rw, OFF_FG + (tg * 16 + c) * FSTR, tag);
      TS(5);
    }
    v4u h0, h1; v2u h2;
    {
      v4f acc[2][5], o[1][5];
      const int r = lane & 15;
      consume<5, 20, 40960, 2048>(smem + r * 128 + ((kh * 4 + g) ^ (r >> 1)) * 16, rw, OFF_PQF + tb0 * 40960 + kh * 1024 + lane * 16, acc,
        [](int u) { return (130 + 10 * u) % 160 * K1; }, [](int u) { return u == 5 || u == 16; }, [] {}, [&](int u) { if (u < 0) TS(6); if (u == 1) TS(32); if (u == 4) TS(33); if (u == 9) TS(34); if (u == 12) TS(35); if (u == 15) TS(36); if (u == 18) TS(37); });
      TS(7);
      pair_reduce<1>(smem + (100 + 5 * w) * K1 + lane * 16, smem + (100 + 5 * (w ^ 2)) * K1 + lane * 16, w, acc, o);
      v4f (&z)[5] = o[0];
      z[0] += v4f{lo(x0[0]), hi(x0[0]), lo(x0[1]), hi(x0[1])}; z[1] += v4f{lo(x0[2]), hi(x0[2]), lo(x0[3]), hi(x0[3])};
      z[2] += v4f{lo(x1[0]), hi(x1[0]), lo(x1[1]), hi(x1[1])}; z[3] += v4f{lo(x1[2]), hi(x1[2]), lo(x1[3]), hi(x1[3])};
      z[4] += v4f{lo(x2[0]), hi(x2[0]), lo(x2[1]), hi(x2[1])};
      h0 = v4u{pk(z[0][0], z[0][1]), pk(z[0][2], z[0][3]), pk(z[1][0], z[1][1]), pk(z[1][2], z[1][3])};
      h1 = v4u{pk(z[2][0], z[2][1]), pk(z[2][2], z[2][3]), pk(z[3][0], z[3][1]), pk(z[3][2], z[3][3])};
      h2 = v2u{pk(z[4][0], z[4][1]), pk(z[4][2], z[4][3])};
      const int dst = OFF_HF + tb * 20480 + kb0 * 1024;
      st16(rw, dst + odd * 3072 + lane * 16, h0);
      st16(rw, dst + odd * 3072 + 1024 + lane * 16, h1);
      st8(rw, dst + 2048 + sp, h2);
      const v4f s4 = (z[0] + z[1]) + (z[2] + z[3]) + z[4];
      const float m = xsum32(xsum16((s4[0] + s4[1]) + (s4[2] + s4[3]))) * (1.f / 80.f);
      v4f q4 = {0, 0, 0, 0};
#pragma unroll
      for (int j = 0; j < 5; j++) { const v4f d = z[j] - m; q4 += d * d; }
      const float q = xsum32(xsum16((q4[0] + q4[1]) + (q4[2] + q4[3])));
      st8(rw, lane < 16 ? OFF_ST + nt * 16384 + t * 8 : OOB, v2u{asu(m), asu(q)});
      TS(53); bar(); TS(54); waitv<0>(); TS(55); bar();
      if (w == 0 && lane == 0) st4(rw, OFF_FG + (256 + tg * 16 + c) * FSTR, tag);
      TS(8);
    }
    {
      const int k3 = (T0 + odd3) >> 1;
      v4f acc[2][7];
      consume<7, 20, 20480, 1024>(smem + lane * 16, rw, OFF_HF + (tg3 * 8 + w * 2) * 20480 + lane * 16, acc,
        [](int u) { return (10 + 14 * (u >> 1) + (u & 1)) * K1; }, [](int u) { return u == 7 || u == 13; }, [] {}, [&](int u) { if (u < 0) TS(9); if (u == 1) TS(38); if (u == 4) TS(39); if (u == 8) TS(40); if (u == 12) TS(41); if (u == 15) TS(42); if (u == 18) TS(43); });
      TS(10);
#pragma unroll
      for (int mi = 0; mi < 2; mi++) {
        const v2f st = *(LDS v2f*)(smem + L3X + 1024 + (w * 32 + mi * 16 + (lane & 15)) * 8);
        const int dst = OFF_HH + (tg3 * 8 + w * 2 + mi) * 55296;
        unsigned r[14];
#pragma unroll
        for (int j = 0; j < 7; j++) {
          const v4f cs4 = *(LDS v4f*)(smem + L3X + (j * 16 + g * 4) * 4), bias = *(LDS v4f*)(smem + L3X + 512 + (j * 16 + g * 4) * 4);
          v4f z = acc[mi][j] * st[1] + (bias - cs4 * st[0]);
          z = __builtin_elementwise_max(z, v4f{0, 0, 0, 0}); z *= z;
          r[2 * j] = pk(z[0], z[1]); r[2 * j + 1] = pk(z[2], z[3]);
          if (j & 1) st16(rw, dst + (k3 + (j >> 1)) * 1024 + lane * 16, v4u{r[2 * j - 2], r[2 * j - 1], r[2 * j], r[2 * j + 1]});
        }
        st8(rw, seven ? dst + (Ts >> 1) * 1024 + ((odd3 * 2 + (g >> 1)) * 16 + (lane & 15)) * 16 + (g & 1) * 8 : OOB, v2u{r[12], r[13]});
      }
      TS(56); bar(); TS(57); waitv<0>(); TS(58); bar();
      if (w == 0 && lane == 0) st4(rw, OFF_FG + (512 + tg3 * 16 + c3) * FSTR, tag);
      TS(11);
    }
    {
      v4u c0, c1; v2u c2;
      v4f acc[2][5], o[1][5];
      consume<5, 27, 55296, 2048>(smem + kh * 1024 + lane * 16, rw, OFF_HH + tb0 * 55296 + kh * 1024 + lane * 16, acc,
        [](int u) { return u % 15 * 10 * K1; }, [](int u) { return u == 5 || u == 9 || u == 15 || u == 19 || u == 23; }, [&] {
          c0 = *(const v4u*)(p.b2 + cp * 2); c1 = *(const v4u*)(p.b2 + cp * 2 + 64); c2 = *(const v2u*)(p.b2 + cs * 2); }, [&](int u) { if (u < 0) TS(12); if (u == 1) TS(44); if (u == 6) TS(45); if (u == 11) TS(46); if (u == 16) TS(47); if (u == 21) TS(48); if (u == 25) TS(49); });
      TS(13);
      pair_reduce<1>(smem + w * 5120 + lane * 16, smem + (w ^ 2) * 5120 + lane * 16, w, acc, o);
      v4f (&z)[5] = o[0];
#define FIN(a, e, h, c) pk(a[e] + lo(h) + lo(c), a[e + 1] + hi(h) + hi(c))
      char* dst = p.out + (size_t)t * 1280;
      *(v4u*)(dst + cp * 2) = v4u{FIN(z[0], 0, h0[0], c0[0]), FIN(z[0], 2, h0[1], c0[1]), FIN(z[1], 0, h0[2], c0[2]), FIN(z[1], 2, h0[3], c0[3])};
      *(v4u*)(dst + cp * 2 + 64) = v4u{FIN(z[2], 0, h1[0], c1[0]), FIN(z[2], 2, h1[1], c1[1]), FIN(z[3], 0, h1[2], c1[2]), FIN(z[3], 2, h1[3], c1[3])};
      *(v2u*)(dst + cs * 2) = v2u{FIN(z[4], 0, h2[0], c2[0]), FIN(z[4], 2, h2[1], c2[1])};
#undef FIN
      TS(14);
      waitv<0>(); TS(15);
      { const unsigned te_ = (unsigned)__builtin_amdgcn_s_memrealtime(); for (int i_ = 0; i_ < 200000; i_++) { if ((int)((unsigned)__builtin_amdgcn_s_memrealtime() - te_) >= 1 * (int)(tb_ - ta_)) break; __builtin_amdgcn_s_sleep(1); } }
    }
  } else if (w < 6) {
    const int lw = w - 4;
    int vt[5];
#pragma unroll
    for (int i = 0; i < 5; i++) { const int j = (lw * 5 + i) >> 1; vt[i] = 5 * nt + (odd ? (j + 1) % 5 : j); }
    unsigned tag;
    int vd[5];
#pragma unroll
    for (int i = 0; i < 5; i++) {
      const int row = (lw * 5 + i) * 8 + (lane >> 3), j = row >> 4, T = 5 * nt + (odd ? (j + 1) % 5 : j), n = row & 15;
      vd[i] = (piece_col(T, n >> 2, j == 4) + (n & 3)) * 1280 + ((lane & 7) ^ ((row >> 1) & 7)) * 16;
    }
    auto e2 = [&](int s0, int s1) {
#pragma unroll 1
      for (int s = s0; s < s1; s++) {
        const rsrc_t r = mkrsrc(s < 10 ? p.dr : p.di - 1280, D_ * D_ * 2 + (s < 10 ? 0 : 1280));
        ldsp d = smem + ((s + 13) & 15) * 10240 + lw * 5120;
#pragma unroll
        for (int i = 0; i < 5; i++) dma<0>(r, d + i * 1024, vd[i], s * 128);
      }
    };
    {
      const rsrc_t ra = mkrsrc((hf ? p.ci : p.cr) + (size_t)tg * 128 * 4096, 128 * 4096);
      int va[8], vb[5];
#pragma unroll
      for (int i = 0; i < 8; i++) { const int row = (lw * 8 + i) * 8 + (lane >> 3); va[i] = row * 4096 + ((lane & 7) ^ ((row >> 1) & 7)) * 16; }
#pragma unroll
      for (int i = 0; i < 5; i++) vb[i] = OFF_LNF + vt[i] * 65536 + ((lw + i) & 1) * 1024 + lane * 16;
      auto l1 = [&](int s, int so) {
#pragma unroll
        for (int i = 0; i < 5; i++) dma<SC1>(rw, smem + so + (16 + lw * 5 + i) * 1024, vb[i],
          ((s + 4 * nt) & 31) * 2048
          );
      };
      auto e1 = [&](int s, int so, bool wa) {
        if (wa) {
#pragma unroll
          for (int i = 0; i < 8; i++) dma<0>(ra, smem + so + (lw * 8 + i) * 1024, va[i], ((s + 4 * nt) & 31) * 128);
        }
        l1(s, so);
      };
      bar();
#pragma unroll
      for (int s = 0; s < NPRE; s++)
#pragma unroll
        for (int i = 0; i < 8; i++) { dma<0>(ra, smem + (s + 3) % 5 * L1S + (lw * 8 + i) * 1024, va[i], (s + 4 * nt) * 128); if (s * 8 + i >= CTH - 1) waitv<CTH - 1>(); }
      lds_wait(smem + L0C, 4);
      tag = __builtin_amdgcn_readfirstlane(*(volatile LDS unsigned*)(smem + L0C + 16));
      TS(16);
      polln<0xffffffffu>(rw, lane < 32 ? OFF_F0 + (32 * nt + lane) * FSTR : OOB, tag);
      TS(17);
      constexpr int N2 = NPRE > 2 ? 5 : 13, N3 = NPRE > 3 ? 5 : 13;
      l1(0, 3 * L1S);
      v4u fp = ld16_async(p.ws, OFF_F0P + lane * 16);
      l1(1, 4 * L1S); e1(2, 0, NPRE < 3);
      waitv<6 + N2>(); bar();
      e1(3, L1S, NPRE < 4);
      waitv<N2 + N3>();
      asm volatile("" : "+v"(fp));
      if (lw == 0) {
        if (!eq4(fp, tag)) poll256(rw, OFF_F0P + lane * 16, tag);
        if (b == 0 && lane == 0) st4(rw, OFF_E, tag);
      }
      bar();
      e1(4, 2 * L1S, true); waitv<N3 + 13>(); bar();
      int so = 3 * L1S;
#pragma unroll 1
      for (int s = 5; s < 32; s++) {
        e1(s, so, true); so += L1S; if (so == 5 * L1S) so = 0;
        waitv<26>(); bar();
      }
      e2(0, 5); waitv<38>(); bar();
      e2(5, TD < 8 ? TD : 8); waitv<5 * (TD < 8 ? TD : 8)>(); bar();
      e2(8, TD);
      if (MT1 == 4) { bar(); bar(); }
      bar();
      e2(TD, 16);
      TS(59); bar();
    }
    int vb3[7];
#pragma unroll
    for (int i = 0; i < 7; i++) { const int f = lw * 7 + i, j = f >> 1, T = j < 6 ? T0 + odd3 + j : Ts;
      vb3[i] = (j < 6 || seven) ? OFF_W1F + T * 20480 + (f & 1) * 1024 + lane * 16 : OOB; }
    auto e3 = [&](int s0, int s1) {
#pragma unroll 1
      for (int s = s0; s < s1; s++) {
        ldsp d = smem + (10 + 14 * s + lw * 7) * K1;
#pragma unroll
        for (int i = 0; i < 7; i++) dma<SC1>(rw, d + i * 1024, vb3[i], s * 2048);
      }
    };
    const int fl = lane < 16 ? OFF_FG + lane * FSTR : OOB, i2 = lw * 64 + lane;
    unsigned cpart[20];
    unsigned short bb;
    {
      if (lw == 0) polln<0xffffu>(rw, fl + tg * 16 * FSTR, tag); else waitv<0>();
      TS(18);
      bar();
      v4u fp = ld16_async(p.ws, OFF_FWP + lane * 16);
      bar();
      e2(16, 20);
      waitv<0>();
      asm volatile("" : "+v"(fp));
      if (lw == 0 && !eq4(fp, tag)) poll256(rw, OFF_FWP + lane * 16, tag);
      bar();
      e3(0, TW);
      bar(); bar();
      const int j = i2 >> 4, T = j < 6 ? T0 + odd3 + j : Ts;
      const bool on = i2 < (seven ? 112 : 96);
      const int vc = on ? OFF_CS + (T * 16 + (i2 & 15)) * 4 : OOB;
      e3(TW, 10);
      bb = __builtin_amdgcn_raw_buffer_load_b16(mkrsrc(p.b1, F_ * 2), on ? (piece_col(T, (i2 & 15) >> 2, split1728(T)) + (i2 & 3)) * 2 : OOB, 0, 0);
#pragma unroll
      for (int k = 0; k < 20; k++) cpart[k] = ld4(rw, vc, k * F_ * 4);
      TS(60); bar();
    }
    int vb4[5];
#pragma unroll
    for (int i = 0; i < 5; i++) vb4[i] = OFF_W2F + vt[i] * 55296 + ((lw + i) & 1) * 1024 + lane * 16;
    auto e4 = [&](int s0, int s1) {
#pragma unroll 1
      for (int s = s0; s < s1; s++) {
        ldsp d = smem + (s >= 15 ? s - 15 : s) * 10240 + lw * 5120;
#pragma unroll
        for (int i = 0; i < 5; i++) dma<SC1>(rw, d + i * 1024, vb4[i], s * 2048);
      }
    };
    {
      if (lw == 0) polln<0xffffu>(rw, fl + (256 + tg3 * 16) * FSTR, tag); else waitv<0>();
      TS(19);
      bar();
      v2u sv[8];
#pragma unroll
      for (int k = 0; k < 8; k++) sv[k] = ld8(rw, OFF_ST + (tg3 * 128 + i2) * 8, k * 16384);
      e4(0, 1);
      float csum = 0, m = 0, q = 0;
#pragma unroll
      for (int k = 0; k < 20; k++) { asm volatile("" : "+v"(cpart[k])); csum += asf(cpart[k]); }
      const float bias = lo(bb);
#pragma unroll
      for (int k = 0; k < 8; k++) { asm volatile("" : "+v"(sv[k])); m += asf(sv[k][0]); q += asf(sv[k][1]); }
      m *= 0.125f;
#pragma unroll
      for (int k = 0; k < 8; k++) { const float d = asf(sv[k][0]) - m; q += 80.f * d * d; }
      const float rs = 1.0f / sqrtf(q * (1.f / 640.f) + 1e-5f);
      const unsigned a0 = (unsigned)(size_t)(smem + L3X + i2 * 4), a1 = (unsigned)(size_t)(smem + L3X + 1024 + i2 * 8);
      asm volatile("ds_write_b32 %0, %1\n ds_write_b32 %0, %2 offset:512\n ds_write_b64 %3, %4\n s_waitcnt lgkmcnt(0)" ::"v"(a0), "v"(csum), "v"(bias), "v"(a1), "v"(v2f{m * rs, rs}) : "memory");
      bar();
      e4(1, TX1);
      bar();
      e4(TX1, TX2);
      bar();
      e4(TX2, 15);
      TS(61); bar();
    }
    {
      if (lw == 0) polln<0xffffu>(rw, fl + (512 + tg * 16) * FSTR, tag); else waitv<0>();
      TS(20);
      bar();
      bar();
      e4(15, 19); bar();
      e4(19, 23); waitv<20>(); bar();
      e4(23, 27); waitv<20>(); bar();
      waitv<0>(); bar();
    }
    if (w == 4) { TSOUT(16, 5); TSOUT(59, 3); }
  } else {
    const int hw = w - 6, nu = hw == 0 && b < 56 ? 3 : 2;
    if (hw == 0) {
      unsigned long long pc; unsigned back;
      asm volatile("s_getpc_b64 %0\n.Lfnet_pc:\n s_mov_b32 %1, .Lfnet_pc-_Z6k_fnet6Params" : "=s"(pc), "=s"(back));
      dma<0>(mkrsrc((const void*)(pc - back), CODELEN), smem + L0D, (b >> 3) * 1024 + lane * 16, 0);
    }
    bar();
#pragma unroll 1
    for (int n = 0; n < nu; n++) {
      const int i = n < 2 ? hw * 2 + n : 4, u = b + 256 * i;
      const bool isw1 = u < 540;
      const int uu = isw1 ? u : u - 540, kb = isw1 ? uu / 27 : uu / 10, cu = isw1 ? uu % 27 : uu % 10, ld = isw1 ? F_ * 2 : D_ * 2;
      const rsrc_t r = mkrsrc(isw1 ? p.w1 : p.w2, D_ * F_ * 2);
#pragma unroll
      for (int k = 0; k < 4; k++) dma<0>(r, smem + L0W + i * 4096 + k * 1024, (kb * 32 + k * 8 + (lane >> 3)) * ld + cu * 128 + (lane & 7) * 16, 0);
    }
    waitv<0>();
    TS(22);
    lds_wait(smem + L0C + 4, 4);
    const int qq = (lane & 15) >> 2, pp = lane & 3;
#pragma unroll 1
    for (int n = 0; n < nu; n++) {
      const int i = n < 2 ? hw * 2 + n : 4, u = b + 256 * i;
      const bool isw1 = u < 540;
      const int uu = isw1 ? u : u - 540, kb = isw1 ? uu / 27 : uu / 10, cu = isw1 ? uu % 27 : uu % 10;
      unsigned a[4];
#pragma unroll
      for (int t = 0; t < 4; t++) { const int T = cu * 4 + t;
        a[t] = (unsigned)(size_t)(smem + L0W + i * 4096 + (g * 8 + qq) * 128 + (piece_col(T, pp, isw1 ? split1728(T) : split640(T)) - cu * 64) * 2); }
      v2u v[8];
      asm volatile("ds_read_b64_tr_b16 %0, %8\n ds_read_b64_tr_b16 %1, %8 offset:512\n ds_read_b64_tr_b16 %2, %9\n ds_read_b64_tr_b16 %3, %9 offset:512\n"
                   "ds_read_b64_tr_b16 %4, %10\n ds_read_b64_tr_b16 %5, %10 offset:512\n ds_read_b64_tr_b16 %6, %11\n ds_read_b64_tr_b16 %7, %11 offset:512\n s_waitcnt lgkmcnt(0)"
                   : "=&v"(v[0]), "=&v"(v[1]), "=&v"(v[2]), "=&v"(v[3]), "=&v"(v[4]), "=&v"(v[5]), "=&v"(v[6]), "=&v"(v[7]) : "v"(a[0]), "v"(a[1]), "v"(a[2]), "v"(a[3]) : "memory");
      const int dst = (isw1 ? OFF_W1F + cu * 4 * 20480 : OFF_W2F + cu * 4 * 55296) + kb * 1024 + lane * 16;
#pragma unroll
      for (int t = 0; t < 4; t++) st16(rw, dst + t * (isw1 ? 20480 : 55296), v4u{v[2 * t][0], v[2 * t][1], v[2 * t + 1][0], v[2 * t + 1][1]});
      if (isw1) {
        float cs = 0;
#pragma unroll
        for (int t = 0; t < 4; t++) {
          const v2u v0 = v[2 * t], v1 = v[2 * t + 1];
          const float c1 = xsum32(xsum16((lo(v0[0]) + hi(v0[0])) + (lo(v0[1]) + hi(v0[1])) + (lo(v1[0]) + hi(v1[0])) + (lo(v1[1]) + hi(v1[1]))));
          cs = g == t ? c1 : cs;
        }
        st4(rw, OFF_CS + (kb * F_ + cu * 64 + lane) * 4, asu(cs));
      }
    }
    TS(23);
#pragma unroll
    for (int k = 0; k < 4; k++) {
      const int q = b + 256 * (hw * 3 + k);
      const rsrc_t r = hw || k < 3 ? mkrsrc(q < 800 ? p.dr + q * 1024 : p.di + (q - 800) * 1024, q < 1600 ? 1024 : 0)
                                   : mkrsrc(b & 32 ? p.b2 : p.b1, b & 32 ? D_ * 2 : F_ * 2);
      dma<0>(r, smem + L0D, lane * 16 + (hw || k < 3 ? 0 : (b >> 3 & 3) * 1024), 0);
    }
    waitv<4>();
    const unsigned tag = *(volatile LDS unsigned*)(smem + L0C + 16);
    unsigned old = 0;
    if (lane == 0) old = lds_add(smem + L0C + 8);
    if (__builtin_amdgcn_readfirstlane(old) == 1 && lane == 0) st4(rw, OFF_FWP + b * 4, tag);
    TS(21);
    bar(); bar(); bar();
    waitv<0>();
    if (w == 6) TSOUT(21, 3);
  }
#endif
}

extern "C" void kernel_launch(void* const* d_in, const int* in_sizes, int n_in, void* d_out, int out_size, void* d_ws, size_t ws_size,
                              hipStream_t stream) {
  Params p;
  p.x = (const char*)d_in[0]; p.cr = (const char*)d_in[1]; p.ci = (const char*)d_in[2]; p.dr = (const char*)d_in[3]; p.di = (const char*)d_in[4];
  p.w1 = (const char*)d_in[6]; p.b1 = (const char*)d_in[7]; p.w2 = (const char*)d_in[8]; p.b2 = (const char*)d_in[9];
  p.out = (char*)d_out; p.ws = (char*)d_ws;
  (void)hipFuncSetAttribute((const void*)k_fnet, hipFuncAttributeMaxDynamicSharedMemorySize, LDS_BYTES);
  k_fnet<<<256, 512, LDS_BYTES, stream>>>(p);
}
```

```cpp
#include <hip/hip_runtime.h>
#include <hip/hip_bf16.h>
#include <stdint.h>

#define CTH 8
#define MT1 2
#define NPRE 3
#define TD 8
#define TW 4
#define TX1 4
#define TX2 9
#define UD 6
#define REMAP3 1
#define CODELEN 24000

#define LDS __attribute__((address_space(3)))
typedef LDS char* ldsp;
typedef short v4s __attribute__((ext_vector_type(4)));
typedef short v8s __attribute__((ext_vector_type(8)));
typedef __bf16 v8b __attribute__((ext_vector_type(8)));
typedef __bf16 v2b __attribute__((ext_vector_type(2)));
typedef float v4f __attribute__((ext_vector_type(4)));
typedef float v2f __attribute__((ext_vector_type(2)));
typedef unsigned v4u __attribute__((ext_vector_type(4)));
typedef unsigned v2u __attribute__((ext_vector_type(2)));

constexpr int S_ = 2048, D_ = 640, F_ = 1728;
constexpr int OFF_E   = 0;
constexpr int FSTR    = 128;
constexpr int OFF_F0  = 4096;
constexpr int OFF_F0P = 1024;
constexpr int OFF_FWP = 2048;
constexpr int OFF_FG  = OFF_F0 + 256 * FSTR;
constexpr int OFF_LNF = 256 * 1024;
constexpr int OFF_PQF = OFF_LNF + 40 * 64 * 1024;
constexpr int OFF_HF  = OFF_PQF + 128 * 40 * 1024;
constexpr int OFF_HH  = OFF_HF + 128 * 20 * 1024;
constexpr int OFF_W1F = OFF_HH + 128 * 54 * 1024;
constexpr int OFF_W2F = OFF_W1F + 108 * 20 * 1024;
constexpr int OFF_CS  = OFF_W2F + 40 * 54 * 1024;
constexpr int OFF_ST  = OFF_CS + 20 * 1728 * 4;
constexpr int WS_BYTES = OFF_ST + 8 * 2048 * 8;
constexpr int OOB = 0x40000000;
constexpr int LDS_BYTES = 160 * 1024;

struct Params {
  const char* x; const char* cr; const char* ci; const char* dr; const char* di;
  const char* w1; const char* b1; const char* w2; const char* b2;
  char* out; char* ws;
};

#define TS(i) do { if (i == 0) ta_ = (unsigned)__builtin_amdgcn_s_memrealtime(); } while (0)
#define TSOUT(a, n)

#if defined(__HIP_DEVICE_COMPILE__)
typedef __amdgpu_buffer_rsrc_t rsrc_t;
constexpr int SC1 = 16;
__device__ __forceinline__ rsrc_t mkrsrc(const void* p, unsigned bytes) { return __builtin_amdgcn_make_buffer_rsrc((void*)p, 0, bytes, 0x00020000); }
template <int AUX> __device__ __forceinline__ void dma(rsrc_t r, ldsp dst, int voff, int soff) {
  __builtin_amdgcn_raw_ptr_buffer_load_lds(r, (LDS void*)dst, 16, voff, soff, 0, AUX);
}
__device__ __forceinline__ void st16(rsrc_t r, int off, v4u v) { __builtin_amdgcn_raw_buffer_store_b128(v, r, off, 0, SC1); }
__device__ __forceinline__ void st8(rsrc_t r, int off, v2u v) { __builtin_amdgcn_raw_buffer_store_b64(v, r, off, 0, SC1); }
__device__ __forceinline__ void st4(rsrc_t r, int off, unsigned v) { __builtin_amdgcn_raw_buffer_store_b32(v, r, off, 0, SC1); }
__device__ __forceinline__ v4u ld16(rsrc_t r, int off) { return __builtin_amdgcn_raw_buffer_load_b128(r, off, 0, SC1); }
__device__ __forceinline__ v2u ld8(rsrc_t r, int off, int soff) { return __builtin_amdgcn_raw_buffer_load_b64(r, off, soff, SC1); }
__device__ __forceinline__ unsigned ld4(rsrc_t r, int off, int soff) { return __builtin_amdgcn_raw_buffer_load_b32(r, off, soff, SC1); }
template <int N> __device__ __forceinline__ void waitv() { asm volatile("s_waitcnt vmcnt(%0)" ::"n"(N) : "memory"); }
__device__ __forceinline__ void bar() { asm volatile("" ::: "memory"); __builtin_amdgcn_s_barrier(); asm volatile("" ::: "memory"); }
__device__ __forceinline__ void waitl0() { __builtin_amdgcn_sched_barrier(0); __builtin_amdgcn_s_waitcnt(0xC07F); __builtin_amdgcn_sched_barrier(0); }
__device__ __forceinline__ v4f mfma(v8s a, v8s b, v4f c) {
  return __builtin_amdgcn_mfma_f32_16x16x32_bf16(__builtin_bit_cast(v8b, a), __builtin_bit_cast(v8b, b), c, 0, 0, 0);
}
__device__ __forceinline__ unsigned pk(float a, float b) { v2b r = {(__bf16)a, (__bf16)b}; return __builtin_bit_cast(unsigned, r); }
__device__ __forceinline__ float asf(unsigned u) { return __builtin_bit_cast(float, u); }
__device__ __forceinline__ unsigned asu(float f) { return __builtin_bit_cast(unsigned, f); }
__device__ __forceinline__ float lo(unsigned u) { return asf(u << 16); }
__device__ __forceinline__ float hi(unsigned u) { return asf(u & 0xffff0000u); }
template <int CTRL> __device__ __forceinline__ float dppadd(float v) { return v + asf(__builtin_amdgcn_update_dpp(0, asu(v), CTRL, 0xf, 0xf, true)); }
__device__ __forceinline__ float rowsum(float v) { return dppadd<0x121>(dppadd<0x122>(dppadd<0x124>(dppadd<0x128>(v)))); }
__device__ __forceinline__ float xsum16(float v) { const auto r = __builtin_amdgcn_permlane16_swap(asu(v), asu(v), false, false); return asf(r[0]) + asf(r[1]); }
__device__ __forceinline__ float xsum32(float v) { const auto r = __builtin_amdgcn_permlane32_swap(asu(v), asu(v), false, false); return asf(r[0]) + asf(r[1]); }
template <int NM, int ND> __device__ __forceinline__ void ilv() {
#pragma unroll
  for (int i = 0; i < NM; i++) { __builtin_amdgcn_sched_group_barrier(0x8, 1, 0); if (i < ND) __builtin_amdgcn_sched_group_barrier(0x100, 1, 0); }
}
__device__ __forceinline__ int piece_col(int T, int p, bool split) { return 16 * T + 4 * p + ((4 * p - 12 * (T & 1)) & ((int)split - 1)); }
__device__ __forceinline__ bool split640(int T) { return (T >> 1) % 5 == 2; }
__device__ __forceinline__ bool split1728(int T) { return (T >> 1) % 7 == 3 && T < 84; }
__device__ __forceinline__ unsigned lds_add(ldsp a) { return __hip_atomic_fetch_add((LDS unsigned*)a, 1u, __ATOMIC_RELAXED, __HIP_MEMORY_SCOPE_WORKGROUP); }
__device__ __forceinline__ void lds_wait(ldsp a, unsigned v) { while (*(volatile LDS unsigned*)a != v) __builtin_amdgcn_s_sleep(1); asm volatile("" ::: "memory"); }
constexpr int SPIN_CAP = 1 << 17;
__device__ __forceinline__ v4u ld16_async(const char* base, unsigned off) { v4u v; asm volatile("global_load_dwordx4 %0, %1, %2 sc1" : "=v"(v) : "v"(off), "s"(base) : "memory"); return v; }
__device__ __forceinline__ void sbar() { __builtin_amdgcn_sched_barrier(0); }
__device__ __forceinline__ bool eq4(v4u v, unsigned tag) { return __builtin_amdgcn_ballot_w64(v[0] != tag || v[1] != tag || v[2] != tag || v[3] != tag) == 0; }
__device__ __forceinline__ void poll256(rsrc_t r, int off, unsigned tag) {
#pragma unroll 1
  for (int it = 0; it < SPIN_CAP; it++) { asm volatile("" ::: "memory"); if (eq4(ld16(r, off), tag)) break; __builtin_amdgcn_s_sleep(2); }
  asm volatile("" ::: "memory");
}
template <unsigned MASK> __device__ __forceinline__ void polln(rsrc_t r, int off, unsigned tag) {
#pragma unroll 1
  for (int it = 0; it < SPIN_CAP; it++) { asm volatile("" ::: "memory"); if (((unsigned)__builtin_amdgcn_ballot_w64(ld4(r, off, 0) != tag) & MASK) == 0) break; __builtin_amdgcn_s_sleep(1); }
  asm volatile("" ::: "memory");
}

constexpr int K1 = 1024, L1S = 26 * K1, L1R = 90 * K1, L3X = 150 * K1;
constexpr int RS = 1312;
constexpr int L0R = 52 * K1, L0W = 130 * K1, L0C = 150 * K1, L0D = 152 * K1;

template <int MT>
__device__ __forceinline__ void consume1(ldsp smem, int w, int lane, v4f (&acc)[MT][5]) {
  const int g = lane >> 4, r = lane & 15;
  const int kh = MT == 4 ? w >> 1 : 0, rb = MT == 4 ? (w & 1) * 4 : w * 2;
  const int a0 = (rb * 16 + r) * 128 + (((kh * 4 + g) ^ (r >> 1)) * 16), a1 = MT == 4 ? a0 : a0 ^ 64;
  const int b0 = 16 * K1 + kh * K1 + lane * 16, b1 = MT == 4 ? b0 : b0 + K1;
  v8s pa[MT], pb[5], qa[MT], qb[5];
#pragma unroll
  for (int i = 0; i < MT; i++) { qa[i] = v8s{0, 0, 0, 0, 0, 0, 0, 0};
#pragma unroll
    for (int j = 0; j < 5; j++) acc[i][j] = v4f{0, 0, 0, 0}; }
#pragma unroll
  for (int j = 0; j < 5; j++) qb[j] = v8s{0, 0, 0, 0, 0, 0, 0, 0};
#define RD(A, B, ao, bo) { B[0] = *(LDS v8s*)(smem + so + bo); _Pragma("unroll") for (int i = 0; i < MT; i++) A[i] = *(LDS v8s*)(smem + so + ao + i * 2048); \
                           _Pragma("unroll") for (int j = 1; j < 5; j++) B[j] = *(LDS v8s*)(smem + so + bo + j * 2048); }
#define MM(A, B) { _Pragma("unroll") for (int j = 0; j < 5; j++) _Pragma("unroll") for (int i = 0; i < MT; i++) acc[i][j] = mfma(B[j], A[i], acc[i][j]); }
#define NEXT { bar(); so += L1S; if (so == 5 * L1S) so = 0; }
  int so = 3 * L1S;
#pragma unroll 1
  for (int s = 0; s < (MT == 4 ? 15 : 31); s++) {
    RD(pa, pb, a0, b0); MM(qa, qb); ilv<5 * MT, 5 + MT>(); sbar();
    if (MT == 4) NEXT
    RD(qa, qb, a1, b1); MM(pa, pb); ilv<5 * MT, 5 + MT>(); sbar();
    NEXT
  }
  RD(pa, pb, a0, b0); MM(qa, qb); ilv<5 * MT, 5 + MT>(); sbar();
  if (MT == 4) NEXT
  RD(qa, qb, a1, b1); MM(pa, pb); ilv<5 * MT, 5 + MT>();
  if (MT == 4) { waitl0(); bar(); } else sbar();
  MM(qa, qb);
#undef RD
#undef MM
#undef NEXT
}

template <int NJ, int NU, int ASTR, int AUS, class FB, class FS, class FX, class FT>
__device__ __forceinline__ void consume(ldsp bl0, rsrc_t rw, int va0, v4f (&acc)[2][NJ], FB boff, FS sync, FX extra, FT stamp) {
  v8s a[UD][2], bq[2][NJ];
  int va[2] = {va0, va0 + ASTR};
  ldsp bl[3] = {bl0, bl0 + 65536, bl0 + 131072};
  asm volatile("" : "+v"(va[0]), "+v"(va[1]), "+v"(bl[0]), "+v"(bl[1]), "+v"(bl[2]));
  bar();
  stamp(-1);
#define LDA(u) _Pragma("unroll") for (int i = 0; i < 2; i++) a[(u) % UD][i] = __builtin_bit_cast(v8s, __builtin_amdgcn_raw_buffer_load_b128(rw, va[i], (u) * AUS, SC1));
#define LDB(u) _Pragma("unroll") for (int j = 0; j < NJ; j++) bq[(u) & 1][j] = *(LDS v8s*)(bl[(boff(u) + j * 2048) >> 16] + ((boff(u) + j * 2048) & 0xffff));
  LDA(0) sbar(); LDB(0) sbar();
#pragma unroll
  for (int u = 1; u < UD - 1; u++) { LDA(u) sbar(); }
  extra();
  sbar();
#pragma unroll
  for (int u = 1; u <= NU; u++) {
    if (u < NU) { if (sync(u)) bar(); LDB(u) }
    const bool ld = u - 2 + UD < NU;
    if (ld) { LDA(u - 2 + UD) }
#pragma unroll
    for (int j = 0; j < NJ; j++)
#pragma unroll
      for (int i = 0; i < 2; i++) acc[i][j] = mfma(bq[(u - 1) & 1][j], a[(u - 1) % UD][i], u == 1 ? v4f{0, 0, 0, 0} : acc[i][j]);
#pragma unroll
    for (int k = 0; k < 2 * NJ; k++) {
      __builtin_amdgcn_sched_group_barrier(0x8, 1, 0);
      if (u < NU && k < NJ) __builtin_amdgcn_sched_group_barrier(0x100, 1, 0);
      if (ld && k >= NJ && k < NJ + 2) __builtin_amdgcn_sched_group_barrier(0x20, 1, 0);
    }
    sbar();
    stamp(u);
  }
#undef LDA
#undef LDB
}

template <int N>
__device__ __forceinline__ void pair_reduce(ldsp mine, ldsp other, int w, v4f (&acc)[2 * N][5], v4f (&o)[N][5]) {
  const int keep = w >> 1;
#pragma unroll
  for (int i = 0; i < N; i++)
#pragma unroll
    for (int j = 0; j < 5; j++) *(LDS v4f*)(mine + (i * 5 + j) * 1024) = keep ? acc[i][j] : acc[N + i][j];
  waitl0(); bar();
#pragma unroll
  for (int i = 0; i < N; i++)
#pragma unroll
    for (int j = 0; j < 5; j++) o[i][j] = (keep ? acc[N + i][j] : acc[i][j]) + *(LDS v4f*)(other + (i * 5 + j) * 1024);
}
#endif

__global__ __launch_bounds__(512) void k_fnet(Params p) {
#if defined(__HIP_DEVICE_COMPILE__)
  extern __shared__ __attribute__((aligned(16))) char smem_[];
  ldsp smem = (ldsp)smem_;
  const int lane = threadIdx.x & 63, w = __builtin_amdgcn_readfirstlane(threadIdx.x >> 6), b = blockIdx.x;
  const int tg = (b & 7) * 2 + (b >> 7), c = (b >> 3) & 15, hf = c >> 3, nt = c & 7, odd = nt & 1;
  const int tg3 = REMAP3 ? (b & 6) * 2 + (b >> 6) : tg, c3 = REMAP3 ? (b & 1) * 8 + nt : c;
  const bool seven = c3 < 12;
  const int odd3 = seven ? c3 & 1 : 0, T0 = seven ? 7 * c3 : 84 + 6 * (c3 - 12), Ts = odd3 ? T0 : T0 + 6;
  const int g = lane >> 4;
  const rsrc_t rw = mkrsrc(p.ws, WS_BYTES);
  unsigned ta_ = 0;
  if (w < 4) {
    TS(0);
    unsigned ep = ld4(rw, OFF_E, 0);
    const v2u* xr = (const v2u*)(p.x + (size_t)(b * 8 + w * 2 + (lane >> 5)) * 1280) + (lane & 31);
    v2u xv[5];
#pragma unroll
    for (int i = 0; i < 5; i++) xv[i] = xr[32 * i];
    if (w == 0 && lane < 4) *(LDS unsigned*)(smem + L0C + lane * 4) = 0;
    waitl0(); bar();
    asm volatile("" : "+v"(ep), "+v"(xv[0]), "+v"(xv[1]), "+v"(xv[2]), "+v"(xv[3]), "+v"(xv[4]));
    const unsigned tag = __builtin_amdgcn_readfirstlane(ep) + 1;
    {
      float v[20], s = 0;
#pragma unroll
      for (int i = 0; i < 5; i++) { v[4 * i] = lo(xv[i][0]); v[4 * i + 1] = hi(xv[i][0]); v[4 * i + 2] = lo(xv[i][1]); v[4 * i + 3] = hi(xv[i][1]); }
#pragma unroll
      for (int i = 0; i < 20; i++) s += v[i];
      const float mean = xsum16(rowsum(s)) * (1.f / 640.f);
      float q = 0;
#pragma unroll
      for (int i = 0; i < 20; i++) { v[i] -= mean; q += v[i] * v[i]; }
      const float rs = 1.0f / sqrtf(xsum16(rowsum(q)) * (1.f / 640.f) + 1e-5f);
#pragma unroll
      for (int i = 0; i < 5; i++)
        *(LDS v2u*)(smem + L0R + (w * 2 + (lane >> 5)) * RS + ((lane & 31) + 32 * i) * 8) = v2u{pk(v[4 * i] * rs, v[4 * i + 1] * rs), pk(v[4 * i + 2] * rs, v[4 * i + 3] * rs)};
      if (w == 0 && lane == 0) *(LDS unsigned*)(smem + L0C + 16) = tag;
      waitl0();
      if (lane == 0) lds_add(smem + L0C);
      lds_wait(smem + L0C, 4);
      TS(1);
      const int qq = (lane & 15) >> 2, pp = lane & 3;
#pragma unroll
      for (int i = 0; i < 3; i++) {
        const int T = 10 * w + 4 * i + (i < 2 ? g : g & 1);
        ldsp a = smem + L0R + qq * RS + piece_col(T, pp, split640(T)) * 2;
        const v4s v0 = __builtin_amdgcn_ds_read_tr16_b64_v4i16((LDS v4s*)a), v1 = __builtin_amdgcn_ds_read_tr16_b64_v4i16((LDS v4s*)(a + 4 * RS));
        st16(rw, i == 2 && g >= 2 ? OOB : OFF_LNF + T * 65536 + (b >> 2) * 1024 + (b & 3) * 256 + (lane & 15) * 16,
             __builtin_bit_cast(v4u, __builtin_shufflevector(v0, v1, 0, 1, 2, 3, 4, 5, 6, 7)));
      }
      waitv<0>();
      unsigned old = 0;
      if (lane == 0) old = lds_add(smem + L0C + 4);
      if (__builtin_amdgcn_readfirstlane(old) == 3 && lane < 2) st4(rw, lane ? OFF_F0P + b * 4 : OFF_F0 + b * FSTR, tag);
      TS(2);
    }
    const int kb0 = 5 * (nt >> 1), sp = ((odd * 2 + (g >> 1)) * 16 + (lane & 15)) * 16 + (g & 1) * 8;
    const int kh = w >> 1, tb0 = tg * 8 + hf * 4 + (w & 1) * 2, tb = tb0 + kh, t = tb * 16 + (lane & 15);
    const int cp = (kb0 + odd * 3) * 32 + g * 8, cs = (kb0 + 2) * 32 + odd * 16 + g * 4;
    sbar();
    const v4u x0 = *(const v4u*)(p.x + (size_t)t * 1280 + cp * 2), x1 = *(const v4u*)(p.x + (size_t)t * 1280 + cp * 2 + 64);
    const v2u x2 = *(const v2u*)(p.x + (size_t)t * 1280 + cs * 2);
    sbar();
    {
      v4f acc[MT1][5];
      bar(); TS(3);
      consume1<MT1>(smem, w, lane, acc);
      TS(4);
      v4f (&o)[2][5] = acc;
      const int tb1 = tg * 8 + w * 2;
      const float sg = hf ? -1.f : 1.f;
#pragma unroll
      for (int mi = 0; mi < 2; mi++) {
        const int dst = OFF_PQF + (tb1 + mi) * 40960 + (hf * 20 + kb0) * 1024;
#pragma unroll
        for (int u = 0; u < 2; u++) {
          const v4f a = o[mi][2 * u] * sg, c2 = o[mi][2 * u + 1] * sg;
          st16(rw, dst + (odd * 3 + u) * 1024 + lane * 16, v4u{pk(a[0], a[1]), pk(a[2], a[3]), pk(c2[0], c2[1]), pk(c2[2], c2[3])});
        }
        const v4f a = o[mi][4] * sg;
        st8(rw, dst + 2048 + sp, v2u{pk(a[0], a[1]), pk(a[2], a[3])});
      }
      TS(50); bar(); TS(51); waitv<0>(); TS(52); bar();
      if (w == 0 && lane == 0) st4(rw, OFF_FG + (tg * 16 + c) * FSTR, tag);
      TS(5);
    }
    v4u h0, h1; v2u h2;
    {
      v4f acc[2][5], o[1][5];
      const int r = lane & 15;
      consume<5, 20, 40960, 2048>(smem + r * 128 + ((kh * 4 + g) ^ (r >> 1)) * 16, rw, OFF_PQF + tb0 * 40960 + kh * 1024 + lane * 16, acc,
        [](int u) { return (130 + 10 * u) % 160 * K1; }, [](int u) { return u == 5 || u == 16; }, [] {}, [&](int u) { if (u < 0) TS(6); if (u == 1) TS(32); if (u == 4) TS(33); if (u == 9) TS(34); if (u == 12) TS(35); if (u == 15) TS(36); if (u == 18) TS(37); });
      TS(7);
      pair_reduce<1>(smem + (100 + 5 * w) * K1 + lane * 16, smem + (100 + 5 * (w ^ 2)) * K1 + lane * 16, w, acc, o);
      v4f (&z)[5] = o[0];
      z[0] += v4f{lo(x0[0]), hi(x0[0]), lo(x0[1]), hi(x0[1])}; z[1] += v4f{lo(x0[2]), hi(x0[2]), lo(x0[3]), hi(x0[3])};
      z[2] += v4f{lo(x1[0]), hi(x1[0]), lo(x1[1]), hi(x1[1])}; z[3] += v4f{lo(x1[2]), hi(x1[2]), lo(x1[3]), hi(x1[3])};
      z[4] += v4f{lo(x2[0]), hi(x2[0]), lo(x2[1]), hi(x2[1])};
      h0 = v4u{pk(z[0][0], z[0][1]), pk(z[0][2], z[0][3]), pk(z[1][0], z[1][1]), pk(z[1][2], z[1][3])};
      h1 = v4u{pk(z[2][0], z[2][1]), pk(z[2][2], z[2][3]), pk(z[3][0], z[3][1]), pk(z[3][2], z[3][3])};
      h2 = v2u{pk(z[4][0], z[4][1]), pk(z[4][2], z[4][3])};
      const int dst = OFF_HF + tb * 20480 + kb0 * 1024;
      st16(rw, dst + odd * 3072 + lane * 16, h0);
      st16(rw, dst + odd * 3072 + 1024 + lane * 16, h1);
      st8(rw, dst + 2048 + sp, h2);
      const v4f s4 = (z[0] + z[1]) + (z[2] + z[3]) + z[4];
      const float m = xsum32(xsum16((s4[0] + s4[1]) + (s4[2] + s4[3]))) * (1.f / 80.f);
      v4f q4 = {0, 0, 0, 0};
#pragma unroll
      for (int j = 0; j < 5; j++) { const v4f d = z[j] - m; q4 += d * d; }
      const float q = xsum32(xsum16((q4[0] + q4[1]) + (q4[2] + q4[3])));
      st8(rw, lane < 16 ? OFF_ST + nt * 16384 + t * 8 : OOB, v2u{asu(m), asu(q)});
      TS(53); bar(); TS(54); waitv<0>(); TS(55); bar();
      if (w == 0 && lane == 0) st4(rw, OFF_FG + (256 + tg * 16 + c) * FSTR, tag);
      TS(8);
    }
    {
      const int k3 = (T0 + odd3) >> 1;
      v4f acc[2][7];
      consume<7, 20, 20480, 1024>(smem + lane * 16, rw, OFF_HF + (tg3 * 8 + w * 2) * 20480 + lane * 16, acc,
        [](int u) { return (10 + 14 * (u >> 1) + (u & 1)) * K1; }, [](int u) { return u == 7 || u == 13; }, [] {}, [&](int u) { if (u < 0) TS(9); if (u == 1) TS(38); if (u == 4) TS(39); if (u == 8) TS(40); if (u == 12) TS(41); if (u == 15) TS(42); if (u == 18) TS(43); });
      TS(10);
#pragma unroll
      for (int mi = 0; mi < 2; mi++) {
        const v2f st = *(LDS v2f*)(smem + L3X + 1024 + (w * 32 + mi * 16 + (lane & 15)) * 8);
        const int dst = OFF_HH + (tg3 * 8 + w * 2 + mi) * 55296;
        unsigned r[14];
#pragma unroll
        for (int j = 0; j < 7; j++) {
          const v4f cs4 = *(LDS v4f*)(smem + L3X + (j * 16 + g * 4) * 4), bias = *(LDS v4f*)(smem + L3X + 512 + (j * 16 + g * 4) * 4);
          v4f z = acc[mi][j] * st[1] + (bias - cs4 * st[0]);
          z = __builtin_elementwise_max(z, v4f{0, 0, 0, 0}); z *= z;
          r[2 * j] = pk(z[0], z[1]); r[2 * j + 1] = pk(z[2], z[3]);
          if (j & 1) st16(rw, dst + (k3 + (j >> 1)) * 1024 + lane * 16, v4u{r[2 * j - 2], r[2 * j - 1], r[2 * j], r[2 * j + 1]});
        }
        st8(rw, seven ? dst + (Ts >> 1) * 1024 + ((odd3 * 2 + (g >> 1)) * 16 + (lane & 15)) * 16 + (g & 1) * 8 : OOB, v2u{r[12], r[13]});
      }
      TS(56); bar(); TS(57); waitv<0>(); TS(58); bar();
      if (w == 0 && lane == 0) st4(rw, OFF_FG + (512 + tg3 * 16 + c3) * FSTR, tag);
      TS(11);
    }
    {
      v4u c0, c1; v2u c2;
      v4f acc[2][5], o[1][5];
      consume<5, 27, 55296, 2048>(smem + kh * 1024 + lane * 16, rw, OFF_HH + tb0 * 55296 + kh * 1024 + lane * 16, acc,
        [](int u) { return u % 15 * 10 * K1; }, [](int u) { return u == 5 || u == 9 || u == 15 || u == 19 || u == 23; }, [&] {
          c0 = *(const v4u*)(p.b2 + cp * 2); c1 = *(const v4u*)(p.b2 + cp * 2 + 64); c2 = *(const v2u*)(p.b2 + cs * 2); }, [&](int u) { if (u < 0) TS(12); if (u == 1) TS(44); if (u == 6) TS(45); if (u == 11) TS(46); if (u == 16) TS(47); if (u == 21) TS(48); if (u == 25) TS(49); });
      TS(13);
      pair_reduce<1>(smem + w * 5120 + lane * 16, smem + (w ^ 2) * 5120 + lane * 16, w, acc, o);
      v4f (&z)[5] = o[0];
#define FIN(a, e, h, c) pk(a[e] + lo(h) + lo(c), a[e + 1] + hi(h) + hi(c))
      char* dst = p.out + (size_t)t * 1280;
      *(v4u*)(dst + cp * 2) = v4u{FIN(z[0], 0, h0[0], c0[0]), FIN(z[0], 2, h0[1], c0[1]), FIN(z[1], 0, h0[2], c0[2]), FIN(z[1], 2, h0[3], c0[3])};
      *(v4u*)(dst + cp * 2 + 64) = v4u{FIN(z[2], 0, h1[0], c1[0]), FIN(z[2], 2, h1[1], c1[1]), FIN(z[3], 0, h1[2], c1[2]), FIN(z[3], 2, h1[3], c1[3])};
      *(v2u*)(dst + cs * 2) = v2u{FIN(z[4], 0, h2[0], c2[0]), FIN(z[4], 2, h2[1], c2[1])};
#undef FIN
      TS(14);
      waitv<0>();
      { const unsigned te_ = (unsigned)__builtin_amdgcn_s_memrealtime();
      }
    }
  } else if (w < 6) {
    const int lw = w - 4;
    int vt[5];
#pragma unroll
    for (int i = 0; i < 5; i++) { const int j = (lw * 5 + i) >> 1; vt[i] = 5 * nt + (odd ? (j + 1) % 5 : j); }
    unsigned tag;
    int vd[5];
#pragma unroll
    for (int i = 0; i < 5; i++) {
      const int row = (lw * 5 + i) * 8 + (lane >> 3), j = row >> 4, T = 5 * nt + (odd ? (j + 1) % 5 : j), n = row & 15;
      vd[i] = (piece_col(T, n >> 2, j == 4) + (n & 3)) * 1280 + ((lane & 7) ^ ((row >> 1) & 7)) * 16;
    }
    auto e2 = [&](int s0, int s1) {
#pragma unroll 1
      for (int s = s0; s < s1; s++) {
        const rsrc_t r = mkrsrc(s < 10 ? p.dr : p.di - 1280, D_ * D_ * 2 + (s < 10 ? 0 : 1280));
        ldsp d = smem + ((s + 13) & 15) * 10240 + lw * 5120;
#pragma unroll
        for (int i = 0; i < 5; i++) dma<0>(r, d + i * 1024, vd[i], s * 128);
      }
    };
    {
      const rsrc_t ra = mkrsrc((hf ? p.ci : p.cr) + (size_t)tg * 128 * 4096, 128 * 4096);
      int va[8], vb[5];
#pragma unroll
      for (int i = 0; i < 8; i++) { const int row = (lw * 8 + i) * 8 + (lane >> 3); va[i] = row * 4096 + ((lane & 7) ^ ((row >> 1) & 7)) * 16; }
#pragma unroll
      for (int i = 0; i < 5; i++) vb[i] = OFF_LNF + vt[i] * 65536 + ((lw + i) & 1) * 1024 + lane * 16;
      auto l1 = [&](int s, int so) {
#pragma unroll
        for (int i = 0; i < 5; i++) dma<SC1>(rw, smem + so + (16 + lw * 5 + i) * 1024, vb[i],
          ((s + 4 * nt) & 31) * 2048
          );
      };
      auto e1 = [&](int s, int so, bool wa) {
        if (wa) {
#pragma unroll
          for (int i = 0; i < 8; i++) dma<0>(ra, smem + so + (lw * 8 + i) * 1024, va[i], ((s + 4 * nt) & 31) * 128);
        }
        l1(s, so);
      };
      bar();
#pragma unroll
      for (int s = 0; s < NPRE; s++)
#pragma unroll
        for (int i = 0; i < 8; i++) { dma<0>(ra, smem + (s + 3) % 5 * L1S + (lw * 8 + i) * 1024, va[i], (s + 4 * nt) * 128); if (s * 8 + i >= CTH - 1) waitv<CTH - 1>(); }
      lds_wait(smem + L0C, 4);
      tag = __builtin_amdgcn_readfirstlane(*(volatile LDS unsigned*)(smem + L0C + 16));
      TS(16);
      polln<0xffffffffu>(rw, lane < 32 ? OFF_F0 + (32 * nt + lane) * FSTR : OOB, tag);
      TS(17);
      constexpr int N2 = NPRE > 2 ? 5 : 13, N3 = NPRE > 3 ? 5 : 13;
      l1(0, 3 * L1S);
      v4u fp = ld16_async(p.ws, OFF_F0P + lane * 16);
      l1(1, 4 * L1S); e1(2, 0, NPRE < 3);
      waitv<6 + N2>(); bar();
      e1(3, L1S, NPRE < 4);
      waitv<N2 + N3>();
      asm volatile("" : "+v"(fp));
      if (lw == 0) {
        if (!eq4(fp, tag)) poll256(rw, OFF_F0P + lane * 16, tag);
        if (b == 0 && lane == 0) st4(rw, OFF_E, tag);
      }
      bar();
      e1(4, 2 * L1S, true); waitv<N3 + 13>(); bar();
      int so = 3 * L1S;
#pragma unroll 1
      for (int s = 5; s < 32; s++) {
        e1(s, so, true); so += L1S; if (so == 5 * L1S) so = 0;
        waitv<26>(); bar();
      }
      e2(0, 5); waitv<38>(); bar();
      e2(5, TD < 8 ? TD : 8); waitv<5 * (TD < 8 ? TD : 8)>(); bar();
      e2(8, TD);
      if (MT1 == 4) { bar(); bar(); }
      bar();
      e2(TD, 16);
      TS(59); bar();
    }
    int vb3[7];
#pragma unroll
    for (int i = 0; i < 7; i++) { const int f = lw * 7 + i, j = f >> 1, T = j < 6 ? T0 + odd3 + j : Ts;
      vb3[i] = (j < 6 || seven) ? OFF_W1F + T * 20480 + (f & 1) * 1024 + lane * 16 : OOB; }
    auto e3 = [&](int s0, int s1) {
#pragma unroll 1
      for (int s = s0; s < s1; s++) {
        ldsp d = smem + (10 + 14 * s + lw * 7) * K1;
#pragma unroll
        for (int i = 0; i < 7; i++) dma<SC1>(rw, d + i * 1024, vb3[i], s * 2048);
      }
    };
    const int fl = lane < 16 ? OFF_FG + lane * FSTR : OOB, i2 = lw * 64 + lane;
    unsigned cpart[20];
    unsigned short bb;
    {
      if (lw == 0) polln<0xffffu>(rw, fl + tg * 16 * FSTR, tag); else waitv<0>();
      TS(18);
      bar();
      v4u fp = ld16_async(p.ws, OFF_FWP + lane * 16);
      bar();
      e2(16, 20);
      waitv<0>();
      asm volatile("" : "+v"(fp));
      if (lw == 0 && !eq4(fp, tag)) poll256(rw, OFF_FWP + lane * 16, tag);
      bar();
      e3(0, TW);
      bar(); bar();
      const int j = i2 >> 4, T = j < 6 ? T0 + odd3 + j : Ts;
      const bool on = i2 < (seven ? 112 : 96);
      const int vc = on ? OFF_CS + (T * 16 + (i2 & 15)) * 4 : OOB;
      e3(TW, 10);
      bb = __builtin_amdgcn_raw_buffer_load_b16(mkrsrc(p.b1, F_ * 2), on ? (piece_col(T, (i2 & 15) >> 2, split1728(T)) + (i2 & 3)) * 2 : OOB, 0, 0);
#pragma unroll
      for (int k = 0; k < 20; k++) cpart[k] = ld4(rw, vc, k * F_ * 4);
      TS(60); bar();
    }
    int vb4[5];
#pragma unroll
    for (int i = 0; i < 5; i++) vb4[i] = OFF_W2F + vt[i] * 55296 + ((lw + i) & 1) * 1024 + lane * 16;
    auto e4 = [&](int s0, int s1) {
#pragma unroll 1
      for (int s = s0; s < s1; s++) {
        ldsp d = smem + (s >= 15 ? s - 15 : s) * 10240 + lw * 5120;
#pragma unroll
        for (int i = 0; i < 5; i++) dma<SC1>(rw, d + i * 1024, vb4[i], s * 2048);
      }
    };
    {
      if (lw == 0) polln<0xffffu>(rw, fl + (256 + tg3 * 16) * FSTR, tag); else waitv<0>();
      TS(19);
      bar();
      v2u sv[8];
#pragma unroll
      for (int k = 0; k < 8; k++) sv[k] = ld8(rw, OFF_ST + (tg3 * 128 + i2) * 8, k * 16384);
      e4(0, 1);
      float csum = 0, m = 0, q = 0;
#pragma unroll
      for (int k = 0; k < 20; k++) { asm volatile("" : "+v"(cpart[k])); csum += asf(cpart[k]); }
      const float bias = lo(bb);
#pragma unroll
      for (int k = 0; k < 8; k++) { asm volatile("" : "+v"(sv[k])); m += asf(sv[k][0]); q += asf(sv[k][1]); }
      m *= 0.125f;
#pragma unroll
      for (int k = 0; k < 8; k++) { const float d = asf(sv[k][0]) - m; q += 80.f * d * d; }
      const float rs = 1.0f / sqrtf(q * (1.f / 640.f) + 1e-5f);
      const unsigned a0 = (unsigned)(size_t)(smem + L3X + i2 * 4), a1 = (unsigned)(size_t)(smem + L3X + 1024 + i2 * 8);
      asm volatile("ds_write_b32 %0, %1\n ds_write_b32 %0, %2 offset:512\n ds_write_b64 %3, %4\n s_waitcnt lgkmcnt(0)" ::"v"(a0), "v"(csum), "v"(bias), "v"(a1), "v"(v2f{m * rs, rs}) : "memory");
      bar();
      e4(1, TX1);
      bar();
      e4(TX1, TX2);
      bar();
      e4(TX2, 15);
      TS(61); bar();
    }
    {
      if (lw == 0) polln<0xffffu>(rw, fl + (512 + tg * 16) * FSTR, tag); else waitv<0>();
      TS(20);
      bar();
      bar();
      e4(15, 19); bar();
      e4(19, 23); waitv<20>(); bar();
      e4(23, 27); waitv<20>(); bar();
      waitv<0>(); bar();
    }
    if (w == 4) { TSOUT(16, 5); TSOUT(59, 3); }
  } else {
    const int hw = w - 6, nu = hw == 0 && b < 56 ? 3 : 2;
    if (hw == 0) {
      unsigned long long pc; unsigned back;
      asm volatile("s_getpc_b64 %0\n.Lfnet_pc:\n s_mov_b32 %1, .Lfnet_pc-_Z6k_fnet6Params" : "=s"(pc), "=s"(back));
      dma<0>(mkrsrc((const void*)(pc - back), CODELEN), smem + L0D, (b >> 3) * 1024 + lane * 16, 0);
    }
    bar();
#pragma unroll 1
    for (int n = 0; n < nu; n++) {
      const int i = n < 2 ? hw * 2 + n : 4, u = b + 256 * i;
      const bool isw1 = u < 540;
      const int uu = isw1 ? u : u - 540, kb = isw1 ? uu / 27 : uu / 10, cu = isw1 ? uu % 27 : uu % 10, ld = isw1 ? F_ * 2 : D_ * 2;
      const rsrc_t r = mkrsrc(isw1 ? p.w1 : p.w2, D_ * F_ * 2);
#pragma unroll
      for (int k = 0; k < 4; k++) dma<0>(r, smem + L0W + i * 4096 + k * 1024, (kb * 32 + k * 8 + (lane >> 3)) * ld + cu * 128 + (lane & 7) * 16, 0);
    }
    waitv<0>();
    TS(22);
    lds_wait(smem + L0C + 4, 4);
    const int qq = (lane & 15) >> 2, pp = lane & 3;
#pragma unroll 1
    for (int n = 0; n < nu; n++) {
      const int i = n < 2 ? hw * 2 + n : 4, u = b + 256 * i;
      const bool isw1 = u < 540;
      const int uu = isw1 ? u : u - 540, kb = isw1 ? uu / 27 : uu / 10, cu = isw1 ? uu % 27 : uu % 10;
      unsigned a[4];
#pragma unroll
      for (int t = 0; t < 4; t++) { const int T = cu * 4 + t;
        a[t] = (unsigned)(size_t)(smem + L0W + i * 4096 + (g * 8 + qq) * 128 + (piece_col(T, pp, isw1 ? split1728(T) : split640(T)) - cu * 64) * 2); }
      v2u v[8];
      asm volatile("ds_read_b64_tr_b16 %0, %8\n ds_read_b64_tr_b16 %1, %8 offset:512\n ds_read_b64_tr_b16 %2, %9\n ds_read_b64_tr_b16 %3, %9 offset:512\n"
                   "ds_read_b64_tr_b16 %4, %10\n ds_read_b64_tr_b16 %5, %10 offset:512\n ds_read_b64_tr_b16 %6, %11\n ds_read_b64_tr_b16 %7, %11 offset:512\n s_waitcnt lgkmcnt(0)"
                   : "=&v"(v[0]), "=&v"(v[1]), "=&v"(v[2]), "=&v"(v[3]), "=&v"(v[4]), "=&v"(v[5]), "=&v"(v[6]), "=&v"(v[7]) : "v"(a[0]), "v"(a[1]), "v"(a[2]), "v"(a[3]) : "memory");
      const int dst = (isw1 ? OFF_W1F + cu * 4 * 20480 : OFF_W2F + cu * 4 * 55296) + kb * 1024 + lane * 16;
#pragma unroll
      for (int t = 0; t < 4; t++) st16(rw, dst + t * (isw1 ? 20480 : 55296), v4u{v[2 * t][0], v[2 * t][1], v[2 * t + 1][0], v[2 * t + 1][1]});
      if (isw1) {
        float cs = 0;
#pragma unroll
        for (int t = 0; t < 4; t++) {
          const v2u v0 = v[2 * t], v1 = v[2 * t + 1];
          const float c1 = xsum32(xsum16((lo(v0[0]) + hi(v0[0])) + (lo(v0[1]) + hi(v0[1])) + (lo(v1[0]) + hi(v1[0])) + (lo(v1[1]) + hi(v1[1]))));
          cs = g == t ? c1 : cs;
        }
        st4(rw, OFF_CS + (kb * F_ + cu * 64 + lane) * 4, asu(cs));
      }
    }
    TS(23);
#pragma unroll
    for (int k = 0; k < 4; k++) {
      const int q = b + 256 * (hw * 3 + k);
      const rsrc_t r = hw || k < 3 ? mkrsrc(q < 800 ? p.dr + q * 1024 : p.di + (q - 800) * 1024, q < 1600 ? 1024 : 0)
                                   : mkrsrc(b & 32 ? p.b2 : p.b1, b & 32 ? D_ * 2 : F_ * 2);
      dma<0>(r, smem + L0D, lane * 16 + (hw || k < 3 ? 0 : (b >> 3 & 3) * 1024), 0);
    }
    waitv<4>();
    const unsigned tag = *(volatile LDS unsigned*)(smem + L0C + 16);
    unsigned old = 0;
    if (lane == 0) old = lds_add(smem + L0C + 8);
    if (__builtin_amdgcn_readfirstlane(old) == 1 && lane == 0) st4(rw, OFF_FWP + b * 4, tag);
    TS(21);
    bar(); bar(); bar();
    waitv<0>();
    if (w == 6) TSOUT(21, 3);
  }
#endif
}

__global__ void k_pre(char* ws) { if (threadIdx.x == 0) *(unsigned*)(ws + (64u << 20)) = (unsigned)__builtin_amdgcn_s_memrealtime(); }
__global__ void k_post(char* ws) {
  const unsigned t0 = (unsigned)__builtin_amdgcn_s_memrealtime();
  int d = 1 << 30;
  for (int i = 0; i < 4; i++) { const int e = (int)(t0 - ((unsigned*)(ws + (64u << 20)))[64 + threadIdx.x + 64 * i]); d = e < d ? e : d; }
  for (int o = 1; o < 64; o <<= 1) { const int e = __shfl_xor(d, o); d = e < d ? e : d; }
  if (d > 3000 || d < 0) d = 0;
  for (int i_ = 0; i_ < 200000; i_++) { if ((int)((unsigned)__builtin_amdgcn_s_memrealtime() - t0) >= 0 * d) break; __builtin_amdgcn_s_sleep(1); }
}
extern "C" void kernel_launch(void* const* d_in, const int* in_sizes, int n_in, void* d_out, int out_size, void* d_ws, size_t ws_size,
                              hipStream_t stream) {
  Params p;
  p.x = (const char*)d_in[0]; p.cr = (const char*)d_in[1]; p.ci = (const char*)d_in[2]; p.dr = (const char*)d_in[3]; p.di = (const char*)d_in[4];
  p.w1 = (const char*)d_in[6]; p.b1 = (const char*)d_in[7]; p.w2 = (const char*)d_in[8]; p.b2 = (const char*)d_in[9];
  p.out = (char*)d_out; p.ws = (char*)d_ws;
  (void)hipFuncSetAttribute((const void*)k_fnet, hipFuncAttributeMaxDynamicSharedMemorySize, LDS_BYTES);
  k_fnet<<<256, 512, LDS_BYTES, stream>>>(p);
}
```

```cpp
#include <hip/hip_runtime.h>
#include <hip/hip_bf16.h>
#include <stdint.h>

#define CTH 8
#define MT1 2
#define NPRE 3
#define TD 8
#define TW 4
#define TX1 4
#define TX2 9
#define UD 6
#define REMAP3 1
#define CODELEN 24000

#define LDS __attribute__((address_space(3)))
typedef LDS char* ldsp;
typedef short v4s __attribute__((ext_vector_type(4)));
typedef short v8s __attribute__((ext_vector_type(8)));
typedef __bf16 v8b __attribute__((ext_vector_type(8)));
typedef __bf16 v2b __attribute__((ext_vector_type(2)));
typedef float v4f __attribute__((ext_vector_type(4)));
typedef float v2f __attribute__((ext_vector_type(2)));
typedef unsigned v4u __attribute__((ext_vector_type(4)));
typedef unsigned v2u __attribute__((ext_vector_type(2)));

constexpr int S_ = 2048, D_ = 640, F_ = 1728;
constexpr int OFF_E   = 0;
constexpr int FSTR    = 128;
constexpr int OFF_F0  = 4096;
constexpr int OFF_F0P = 1024;
constexpr int OFF_FWP = 2048;
constexpr int OFF_FG  = OFF_F0 + 256 * FSTR;
constexpr int OFF_LNF = 256 * 1024;
constexpr int OFF_PQF = OFF_LNF + 40 * 64 * 1024;
constexpr int OFF_HF  = OFF_PQF + 128 * 40 * 1024;
constexpr int OFF_HH  = OFF_HF + 128 * 20 * 1024;
constexpr int OFF_W1F = OFF_HH + 128 * 54 * 1024;
constexpr int OFF_W2F = OFF_W1F + 108 * 20 * 1024;
constexpr int OFF_CS  = OFF_W2F + 40 * 54 * 1024;
constexpr int OFF_ST  = OFF_CS + 20 * 1728 * 4;
constexpr int WS_BYTES = OFF_ST + 8 * 2048 * 8;
constexpr int OOB = 0x40000000;
constexpr int LDS_BYTES = 160 * 1024;

struct Params {
  const char* x; const char* cr; const char* ci; const char* dr; const char* di;
  const char* w1; const char* b1; const char* w2; const char* b2;
  char* out; char* ws;
};

#define TS(i) do { if (i == 100) ta_ = (unsigned)__builtin_amdgcn_s_memrealtime(); if (i == 100) tb_ = (unsigned)__builtin_amdgcn_s_memrealtime(); } while (0)
#define TSOUT(a, n)

#if defined(__HIP_DEVICE_COMPILE__)
typedef __amdgpu_buffer_rsrc_t rsrc_t;
constexpr int SC1 = 16;
__device__ __forceinline__ rsrc_t mkrsrc(const void* p, unsigned bytes) { return __builtin_amdgcn_make_buffer_rsrc((void*)p, 0, bytes, 0x00020000); }
template <int AUX> __device__ __forceinline__ void dma(rsrc_t r, ldsp dst, int voff, int soff) {
  __builtin_amdgcn_raw_ptr_buffer_load_lds(r, (LDS void*)dst, 16, voff, soff, 0, AUX);
}
__device__ __forceinline__ void st16(rsrc_t r, int off, v4u v) { __builtin_amdgcn_raw_buffer_store_b128(v, r, off, 0, SC1); }
__device__ __forceinline__ void st8(rsrc_t r, int off, v2u v) { __builtin_amdgcn_raw_buffer_store_b64(v, r, off, 0, SC1); }
__device__ __forceinline__ void st4(rsrc_t r, int off, unsigned v) { __builtin_amdgcn_raw_buffer_store_b32(v, r, off, 0, SC1); }
__device__ __forceinline__ v4u ld16(rsrc_t r, int off) { return __builtin_amdgcn_raw_buffer_load_b128(r, off, 0, SC1); }
__device__ __forceinline__ v2u ld8(rsrc_t r, int off, int soff) { return __builtin_amdgcn_raw_buffer_load_b64(r, off, soff, SC1); }
__device__ __forceinline__ unsigned ld4(rsrc_t r, int off, int soff) { return __builtin_amdgcn_raw_buffer_load_b32(r, off, soff, SC1); }
template <int N> __device__ __forceinline__ void waitv() { asm volatile("s_waitcnt vmcnt(%0)" ::"n"(N) : "memory"); }
__device__ __forceinline__ void bar() { asm volatile("" ::: "memory"); __builtin_amdgcn_s_barrier(); asm volatile("" ::: "memory"); }
__device__ __forceinline__ void waitl0() { __builtin_amdgcn_sched_barrier(0); __builtin_amdgcn_s_waitcnt(0xC07F); __builtin_amdgcn_sched_barrier(0); }
__device__ __forceinline__ v4f mfma(v8s a, v8s b, v4f c) {
  return __builtin_amdgcn_mfma_f32_16x16x32_bf16(__builtin_bit_cast(v8b, a), __builtin_bit_cast(v8b, b), c, 0, 0, 0);
}
__device__ __forceinline__ unsigned pk(float a, float b) { v2b r = {(__bf16)a, (__bf16)b}; return __builtin_bit_cast(unsigned, r); }
__device__ __forceinline__ float asf(unsigned u) { return __builtin_bit_cast(float, u); }
__device__ __forceinline__ unsigned asu(float f) { return __builtin_bit_cast(unsigned, f); }
__device__ __forceinline__ float lo(unsigned u) { return asf(u << 16); }
__device__ __forceinline__ float hi(unsigned u) { return asf(u & 0xffff0000u); }
template <int CTRL> __device__ __forceinline__ float dppadd(float v) { return v + asf(__builtin_amdgcn_update_dpp(0, asu(v), CTRL, 0xf, 0xf, true)); }
__device__ __forceinline__ float rowsum(float v) { return dppadd<0x121>(dppadd<0x122>(dppadd<0x124>(dppadd<0x128>(v)))); }
__device__ __forceinline__ float xsum16(float v) { const auto r = __builtin_amdgcn_permlane16_swap(asu(v), asu(v), false, false); return asf(r[0]) + asf(r[1]); }
__device__ __forceinline__ float xsum32(float v) { const auto r = __builtin_amdgcn_permlane32_swap(asu(v), asu(v), false, false); return asf(r[0]) + asf(r[1]); }
template <int NM, int ND> __device__ __forceinline__ void ilv() {
#pragma unroll
  for (int i = 0; i < NM; i++) { __builtin_amdgcn_sched_group_barrier(0x8, 1, 0); if (i < ND) __builtin_amdgcn_sched_group_barrier(0x100, 1, 0); }
}
__device__ __forceinline__ int piece_col(int T, int p, bool split) { return 16 * T + 4 * p + ((4 * p - 12 * (T & 1)) & ((int)split - 1)); }
__device__ __forceinline__ bool split640(int T) { return (T >> 1) % 5 == 2; }
__device__ __forceinline__ bool split1728(int T) { return (T >> 1) % 7 == 3 && T < 84; }
__device__ __forceinline__ unsigned lds_add(ldsp a) { return __hip_atomic_fetch_add((LDS unsigned*)a, 1u, __ATOMIC_RELAXED, __HIP_MEMORY_SCOPE_WORKGROUP); }
__device__ __forceinline__ void lds_wait(ldsp a, unsigned v) { while (*(volatile LDS unsigned*)a != v) __builtin_amdgcn_s_sleep(1); asm volatile("" ::: "memory"); }
constexpr int SPIN_CAP = 1 << 17;
__device__ __forceinline__ v4u ld16_async(const char* base, unsigned off) { v4u v; asm volatile("global_load_dwordx4 %0, %1, %2 sc1" : "=v"(v) : "v"(off), "s"(base) : "memory"); return v; }
__device__ __forceinline__ void sbar() { __builtin_amdgcn_sched_barrier(0); }
__device__ __forceinline__ bool eq4(v4u v, unsigned tag) { return __builtin_amdgcn_ballot_w64(v[0] != tag || v[1] != tag || v[2] != tag || v[3] != tag) == 0; }
__device__ __forceinline__ void poll256(rsrc_t r, int off, unsigned tag) {
#pragma unroll 1
  for (int it = 0; it < SPIN_CAP; it++) { asm volatile("" ::: "memory"); if (eq4(ld16(r, off), tag)) break; __builtin_amdgcn_s_sleep(2); }
  asm volatile("" ::: "memory");
}
template <unsigned MASK> __device__ __forceinline__ void polln(rsrc_t r, int off, unsigned tag) {
#pragma unroll 1
  for (int it = 0; it < SPIN_CAP; it++) { asm volatile("" ::: "memory"); if (((unsigned)__builtin_amdgcn_ballot_w64(ld4(r, off, 0) != tag) & MASK) == 0) break; __builtin_amdgcn_s_sleep(1); }
  asm volatile("" ::: "memory");
}

constexpr int K1 = 1024, L1S = 26 * K1, L1R = 90 * K1, L3X = 150 * K1;
constexpr int RS = 1312;
constexpr int L0R = 52 * K1, L0W = 130 * K1, L0C = 150 * K1, L0D = 152 * K1;

template <int MT>
__device__ __forceinline__ void consume1(ldsp smem, int w, int lane, v4f (&acc)[MT][5]) {
  const int g = lane >> 4, r = lane & 15;
  const int kh = MT == 4 ? w >> 1 : 0, rb = MT == 4 ? (w & 1) * 4 : w * 2;
  const int a0 = (rb * 16 + r) * 128 + (((kh * 4 + g) ^ (r >> 1)) * 16), a1 = MT == 4 ? a0 : a0 ^ 64;
  const int b0 = 16 * K1 + kh * K1 + lane * 16, b1 = MT == 4 ? b0 : b0 + K1;
  v8s pa[MT], pb[5], qa[MT], qb[5];
#pragma unroll
  for (int i = 0; i < MT; i++) { qa[i] = v8s{0, 0, 0, 0, 0, 0, 0, 0};
#pragma unroll
    for (int j = 0; j < 5; j++) acc[i][j] = v4f{0, 0, 0, 0}; }
#pragma unroll
  for (int j = 0; j < 5; j++) qb[j] = v8s{0, 0, 0, 0, 0, 0, 0, 0};
#define RD(A, B, ao, bo) { B[0] = *(LDS v8s*)(smem + so + bo); _Pragma("unroll") for (int i = 0; i < MT; i++) A[i] = *(LDS v8s*)(smem + so + ao + i * 2048); \
                           _Pragma("unroll") for (int j = 1; j < 5; j++) B[j] = *(LDS v8s*)(smem + so + bo + j * 2048); }
#define MM(A, B) { _Pragma("unroll") for (int j = 0; j < 5; j++) _Pragma("unroll") for (int i = 0; i < MT; i++) acc[i][j] = mfma(B[j], A[i], acc[i][j]); }
#define NEXT { bar(); so += L1S; if (so == 5 * L1S) so = 0; }
  int so = 3 * L1S;
#pragma unroll 1
  for (int s = 0; s < (MT == 4 ? 15 : 31); s++) {
    RD(pa, pb, a0, b0); MM(qa, qb); ilv<5 * MT, 5 + MT>(); sbar();
    if (MT == 4) NEXT
    RD(qa, qb, a1, b1); MM(pa, pb); ilv<5 * MT, 5 + MT>(); sbar();
    NEXT
  }
  RD(pa, pb, a0, b0); MM(qa, qb); ilv<5 * MT, 5 + MT>(); sbar();
  if (MT == 4) NEXT
  RD(qa, qb, a1, b1); MM(pa, pb); ilv<5 * MT, 5 + MT>();
  if (MT == 4) { waitl0(); bar(); } else sbar();
  MM(qa, qb);
#undef RD
#undef MM
#undef NEXT
}

template <int NJ, int NU, int ASTR, int AUS, class FB, class FS, class FX, class FT>
__device__ __forceinline__ void consume(ldsp bl0, rsrc_t rw, int va0, v4f (&acc)[2][NJ], FB boff, FS sync, FX extra, FT stamp) {
  v8s a[UD][2], bq[2][NJ];
  int va[2] = {va0, va0 + ASTR};
  ldsp bl[3] = {bl0, bl0 + 65536, bl0 + 131072};
  asm volatile("" : "+v"(va[0]), "+v"(va[1]), "+v"(bl[0]), "+v"(bl[1]), "+v"(bl[2]));
  bar();
  stamp(-1);
#define LDA(u) _Pragma("unroll") for (int i = 0; i < 2; i++) a[(u) % UD][i] = __builtin_bit_cast(v8s, __builtin_amdgcn_raw_buffer_load_b128(rw, va[i], (u) * AUS, SC1));
#define LDB(u) _Pragma("unroll") for (int j = 0; j < NJ; j++) bq[(u) & 1][j] = *(LDS v8s*)(bl[(boff(u) + j * 2048) >> 16] + ((boff(u) + j * 2048) & 0xffff));
  LDA(0) sbar(); LDB(0) sbar();
#pragma unroll
  for (int u = 1; u < UD - 1; u++) { LDA(u) sbar(); }
  extra();
  sbar();
#pragma unroll
  for (int u = 1; u <= NU; u++) {
    if (u < NU) { if (sync(u)) bar(); LDB(u) }
    const bool ld = u - 2 + UD < NU;
    if (ld) { LDA(u - 2 + UD) }
#pragma unroll
    for (int j = 0; j < NJ; j++)
#pragma unroll
      for (int i = 0; i < 2; i++) acc[i][j] = mfma(bq[(u - 1) & 1][j], a[(u - 1) % UD][i], u == 1 ? v4f{0, 0, 0, 0} : acc[i][j]);
#pragma unroll
    for (int k = 0; k < 2 * NJ; k++) {
      __builtin_amdgcn_sched_group_barrier(0x8, 1, 0);
      if (u < NU && k < NJ) __builtin_amdgcn_sched_group_barrier(0x100, 1, 0);
      if (ld && k >= NJ && k < NJ + 2) __builtin_amdgcn_sched_group_barrier(0x20, 1, 0);
    }
    sbar();
    stamp(u);
  }
#undef LDA
#undef LDB
}

template <int N>
__device__ __forceinline__ void pair_reduce(ldsp mine, ldsp other, int w, v4f (&acc)[2 * N][5], v4f (&o)[N][5]) {
  const int keep = w >> 1;
#pragma unroll
  for (int i = 0; i < N; i++)
#pragma unroll
    for (int j = 0; j < 5; j++) *(LDS v4f*)(mine + (i * 5 + j) * 1024) = keep ? acc[i][j] : acc[N + i][j];
  waitl0(); bar();
#pragma unroll
  for (int i = 0; i < N; i++)
#pragma unroll
    for (int j = 0; j < 5; j++) o[i][j] = (keep ? acc[N + i][j] : acc[i][j]) + *(LDS v4f*)(other + (i * 5 + j) * 1024);
}
#endif

__global__ __launch_bounds__(512) void k_fnet(Params p) {
#if defined(__HIP_DEVICE_COMPILE__)
  extern __shared__ __attribute__((aligned(16))) char smem_[];
  ldsp smem = (ldsp)smem_;
  unsigned ta_ = 0, tb_ = 0; TS(100); sbar();
  const int lane = threadIdx.x & 63, w = __builtin_amdgcn_readfirstlane(threadIdx.x >> 6), b = blockIdx.x;
  const int tg = (b & 7) * 2 + (b >> 7), c = (b >> 3) & 15, hf = c >> 3, nt = c & 7, odd = nt & 1;
  const int tg3 = REMAP3 ? (b & 6) * 2 + (b >> 6) : tg, c3 = REMAP3 ? (b & 1) * 8 + nt : c;
  const bool seven = c3 < 12;
  const int odd3 = seven ? c3 & 1 : 0, T0 = seven ? 7 * c3 : 84 + 6 * (c3 - 12), Ts = odd3 ? T0 : T0 + 6;
  const int g = lane >> 4;
  const rsrc_t rw = mkrsrc(p.ws, WS_BYTES);
  { const char* q_ = p.ws; asm volatile("" : "+s"(q_)); sbar(); TS(101); sbar(); }
  if (w < 4) {
    TS(0);
    unsigned ep = ld4(rw, OFF_E, 0);
    const v2u* xr = (const v2u*)(p.x + (size_t)(b * 8 + w * 2 + (lane >> 5)) * 1280) + (lane & 31);
    v2u xv[5];
#pragma unroll
    for (int i = 0; i < 5; i++) xv[i] = xr[32 * i];
    if (w == 0 && lane < 4) *(LDS unsigned*)(smem + L0C + lane * 4) = 0;
    waitl0(); bar();
    asm volatile("" : "+v"(ep), "+v"(xv[0]), "+v"(xv[1]), "+v"(xv[2]), "+v"(xv[3]), "+v"(xv[4]));
    const unsigned tag = __builtin_amdgcn_readfirstlane(ep) + 1;
    {
      float v[20], s = 0;
#pragma unroll
      for (int i = 0; i < 5; i++) { v[4 * i] = lo(xv[i][0]); v[4 * i + 1] = hi(xv[i][0]); v[4 * i + 2] = lo(xv[i][1]); v[4 * i + 3] = hi(xv[i][1]); }
#pragma unroll
      for (int i = 0; i < 20; i++) s += v[i];
      const float mean = xsum16(rowsum(s)) * (1.f / 640.f);
      float q = 0;
#pragma unroll
      for (int i = 0; i < 20; i++) { v[i] -= mean; q += v[i] * v[i]; }
      const float rs = 1.0f / sqrtf(xsum16(rowsum(q)) * (1.f / 640.f) + 1e-5f);
#pragma unroll
      for (int i = 0; i < 5; i++)
        *(LDS v2u*)(smem + L0R + (w * 2 + (lane >> 5)) * RS + ((lane & 31) + 32 * i) * 8) = v2u{pk(v[4 * i] * rs, v[4 * i + 1] * rs), pk(v[4 * i + 2] * rs, v[4 * i + 3] * rs)};
      if (w == 0 && lane == 0) *(LDS unsigned*)(smem + L0C + 16) = tag;
      waitl0();
      if (lane == 0) lds_add(smem + L0C);
      lds_wait(smem + L0C, 4);
      TS(1);
      const int qq = (lane & 15) >> 2, pp = lane & 3;
#pragma unroll
      for (int i = 0; i < 3; i++) {
        const int T = 10 * w + 4 * i + (i < 2 ? g : g & 1);
        ldsp a = smem + L0R + qq * RS + piece_col(T, pp, split640(T)) * 2;
        const v4s v0 = __builtin_amdgcn_ds_read_tr16_b64_v4i16((LDS v4s*)a), v1 = __builtin_amdgcn_ds_read_tr16_b64_v4i16((LDS v4s*)(a + 4 * RS));
        st16(rw, i == 2 && g >= 2 ? OOB : OFF_LNF + T * 65536 + (b >> 2) * 1024 + (b & 3) * 256 + (lane & 15) * 16,
             __builtin_bit_cast(v4u, __builtin_shufflevector(v0, v1, 0, 1, 2, 3, 4, 5, 6, 7)));
      }
      waitv<0>();
      unsigned old = 0;
      if (lane == 0) old = lds_add(smem + L0C + 4);
      if (__builtin_amdgcn_readfirstlane(old) == 3 && lane < 2) st4(rw, lane ? OFF_F0P + b * 4 : OFF_F0 + b * FSTR, tag);
      TS(2);
    }
    const int kb0 = 5 * (nt >> 1), sp = ((odd * 2 + (g >> 1)) * 16 + (lane & 15)) * 16 + (g & 1) * 8;
    const int kh = w >> 1, tb0 = tg * 8 + hf * 4 + (w & 1) * 2, tb = tb0 + kh, t = tb * 16 + (lane & 15);
    const int cp = (kb0 + odd * 3) * 32 + g * 8, cs = (kb0 + 2) * 32 + odd * 16 + g * 4;
    sbar();
    const v4u x0 = *(const v4u*)(p.x + (size_t)t * 1280 + cp * 2), x1 = *(const v4u*)(p.x + (size_t)t * 1280 + cp * 2 + 64);
    const v2u x2 = *(const v2u*)(p.x + (size_t)t * 1280 + cs * 2);
    sbar();
    {
      v4f acc[MT1][5];
      bar(); TS(3);
      consume1<MT1>(smem, w, lane, acc);
      TS(4);
      v4f (&o)[2][5] = acc;
      const int tb1 = tg * 8 + w * 2;
      const float sg = hf ? -1.f : 1.f;
#pragma unroll
      for (int mi = 0; mi < 2; mi++) {
        const int dst = OFF_PQF + (tb1 + mi) * 40960 + (hf * 20 + kb0) * 1024;
#pragma unroll
        for (int u = 0; u < 2; u++) {
          const v4f a = o[mi][2 * u] * sg, c2 = o[mi][2 * u + 1] * sg;
          st16(rw, dst + (odd * 3 + u) * 1024 + lane * 16, v4u{pk(a[0], a[1]), pk(a[2], a[3]), pk(c2[0], c2[1]), pk(c2[2], c2[3])});
        }
        const v4f a = o[mi][4] * sg;
        st8(rw, dst + 2048 + sp, v2u{pk(a[0], a[1]), pk(a[2], a[3])});
      }
      TS(50); bar(); TS(51); waitv<0>(); TS(52); bar();
      if (w == 0 && lane == 0) st4(rw, OFF_FG + (tg * 16 + c) * FSTR, tag);
      TS(5);
    }
    v4u h0, h1; v2u h2;
    {
      v4f acc[2][5], o[1][5];
      const int r = lane & 15;
      consume<5, 20, 40960, 2048>(smem + r * 128 + ((kh * 4 + g) ^ (r >> 1)) * 16, rw, OFF_PQF + tb0 * 40960 + kh * 1024 + lane * 16, acc,
        [](int u) { return (130 + 10 * u) % 160 * K1; }, [](int u) { return u == 5 || u == 16; }, [] {}, [&](int u) { if (u < 0) TS(6); if (u == 1) TS(32); if (u == 4) TS(33); if (u == 9) TS(34); if (u == 12) TS(35); if (u == 15) TS(36); if (u == 18) TS(37); });
      TS(7);
      pair_reduce<1>(smem + (100 + 5 * w) * K1 + lane * 16, smem + (100 + 5 * (w ^ 2)) * K1 + lane * 16, w, acc, o);
      v4f (&z)[5] = o[0];
      z[0] += v4f{lo(x0[0]), hi(x0[0]), lo(x0[1]), hi(x0[1])}; z[1] += v4f{lo(x0[2]), hi(x0[2]), lo(x0[3]), hi(x0[3])};
      z[2] += v4f{lo(x1[0]), hi(x1[0]), lo(x1[1]), hi(x1[1])}; z[3] += v4f{lo(x1[2]), hi(x1[2]), lo(x1[3]), hi(x1[3])};
      z[4] += v4f{lo(x2[0]), hi(x2[0]), lo(x2[1]), hi(x2[1])};
      h0 = v4u{pk(z[0][0], z[0][1]), pk(z[0][2], z[0][3]), pk(z[1][0], z[1][1]), pk(z[1][2], z[1][3])};
      h1 = v4u{pk(z[2][0], z[2][1]), pk(z[2][2], z[2][3]), pk(z[3][0], z[3][1]), pk(z[3][2], z[3][3])};
      h2 = v2u{pk(z[4][0], z[4][1]), pk(z[4][2], z[4][3])};
      const int dst = OFF_HF + tb * 20480 + kb0 * 1024;
      st16(rw, dst + odd * 3072 + lane * 16, h0);
      st16(rw, dst + odd * 3072 + 1024 + lane * 16, h1);
      st8(rw, dst + 2048 + sp, h2);
      const v4f s4 = (z[0] + z[1]) + (z[2] + z[3]) + z[4];
      const float m = xsum32(xsum16((s4[0] + s4[1]) + (s4[2] + s4[3]))) * (1.f / 80.f);
      v4f q4 = {0, 0, 0, 0};
#pragma unroll
      for (int j = 0; j < 5; j++) { const v4f d = z[j] - m; q4 += d * d; }
      const float q = xsum32(xsum16((q4[0] + q4[1]) + (q4[2] + q4[3])));
      st8(rw, lane < 16 ? OFF_ST + nt * 16384 + t * 8 : OOB, v2u{asu(m), asu(q)});
      TS(53); bar(); TS(54); waitv<0>(); TS(55); bar();
      if (w == 0 && lane == 0) st4(rw, OFF_FG + (256 + tg * 16 + c) * FSTR, tag);
      TS(8);
    }
    {
      const int k3 = (T0 + odd3) >> 1;
      v4f acc[2][7];
      consume<7, 20, 20480, 1024>(smem + lane * 16, rw, OFF_HF + (tg3 * 8 + w * 2) * 20480 + lane * 16, acc,
        [](int u) { return (10 + 14 * (u >> 1) + (u & 1)) * K1; }, [](int u) { return u == 7 || u == 13; }, [] {}, [&](int u) { if (u < 0) TS(9); if (u == 1) TS(38); if (u == 4) TS(39); if (u == 8) TS(40); if (u == 12) TS(41); if (u == 15) TS(42); if (u == 18) TS(43); });
      TS(10);
#pragma unroll
      for (int mi = 0; mi < 2; mi++) {
        const v2f st = *(LDS v2f*)(smem + L3X + 1024 + (w * 32 + mi * 16 + (lane & 15)) * 8);
        const int dst = OFF_HH + (tg3 * 8 + w * 2 + mi) * 55296;
        unsigned r[14];
#pragma unroll
        for (int j = 0; j < 7; j++) {
          const v4f cs4 = *(LDS v4f*)(smem + L3X + (j * 16 + g * 4) * 4), bias = *(LDS v4f*)(smem + L3X + 512 + (j * 16 + g * 4) * 4);
          v4f z = acc[mi][j] * st[1] + (bias - cs4 * st[0]);
          z = __builtin_elementwise_max(z, v4f{0, 0, 0, 0}); z *= z;
          r[2 * j] = pk(z[0], z[1]); r[2 * j + 1] = pk(z[2], z[3]);
          if (j & 1) st16(rw, dst + (k3 + (j >> 1)) * 1024 + lane * 16, v4u{r[2 * j - 2], r[2 * j - 1], r[2 * j], r[2 * j + 1]});
        }
        st8(rw, seven ? dst + (Ts >> 1) * 1024 + ((odd3 * 2 + (g >> 1)) * 16 + (lane & 15)) * 16 + (g & 1) * 8 : OOB, v2u{r[12], r[13]});
      }
      TS(56); bar(); TS(57); waitv<0>(); TS(58); bar();
      if (w == 0 && lane == 0) st4(rw, OFF_FG + (512 + tg3 * 16 + c3) * FSTR, tag);
      TS(11);
    }
    {
      v4u c0, c1; v2u c2;
      v4f acc[2][5], o[1][5];
      consume<5, 27, 55296, 2048>(smem + kh * 1024 + lane * 16, rw, OFF_HH + tb0 * 55296 + kh * 1024 + lane * 16, acc,
        [](int u) { return u % 15 * 10 * K1; }, [](int u) { return u == 5 || u == 9 || u == 15 || u == 19 || u == 23; }, [&] {
          c0 = *(const v4u*)(p.b2 + cp * 2); c1 = *(const v4u*)(p.b2 + cp * 2 + 64); c2 = *(const v2u*)(p.b2 + cs * 2); }, [&](int u) { if (u < 0) TS(12); if (u == 1) TS(44); if (u == 6) TS(45); if (u == 11) TS(46); if (u == 16) TS(47); if (u == 21) TS(48); if (u == 25) TS(49); });
      TS(13);
      pair_reduce<1>(smem + w * 5120 + lane * 16, smem + (w ^ 2) * 5120 + lane * 16, w, acc, o);
      v4f (&z)[5] = o[0];
#define FIN(a, e, h, c) pk(a[e] + lo(h) + lo(c), a[e + 1] + hi(h) + hi(c))
      char* dst = p.out + (size_t)t * 1280;
      *(v4u*)(dst + cp * 2) = v4u{FIN(z[0], 0, h0[0], c0[0]), FIN(z[0], 2, h0[1], c0[1]), FIN(z[1], 0, h0[2], c0[2]), FIN(z[1], 2, h0[3], c0[3])};
      *(v4u*)(dst + cp * 2 + 64) = v4u{FIN(z[2], 0, h1[0], c1[0]), FIN(z[2], 2, h1[1], c1[1]), FIN(z[3], 0, h1[2], c1[2]), FIN(z[3], 2, h1[3], c1[3])};
      *(v2u*)(dst + cs * 2) = v2u{FIN(z[4], 0, h2[0], c2[0]), FIN(z[4], 2, h2[1], c2[1])};
#undef FIN
      TS(14);
      waitv<0>(); TS(15);
      { const unsigned te_ = (unsigned)__builtin_amdgcn_s_memrealtime(); for (int i_ = 0; i_ < 200000; i_++) { if ((int)((unsigned)__builtin_amdgcn_s_memrealtime() - te_) >= 1 * (int)(tb_ - ta_)) break; __builtin_amdgcn_s_sleep(1); } }
    }
  } else if (w < 6) {
    const int lw = w - 4;
    int vt[5];
#pragma unroll
    for (int i = 0; i < 5; i++) { const int j = (lw * 5 + i) >> 1; vt[i] = 5 * nt + (odd ? (j + 1) % 5 : j); }
    unsigned tag;
    int vd[5];
#pragma unroll
    for (int i = 0; i < 5; i++) {
      const int row = (lw * 5 + i) * 8 + (lane >> 3), j = row >> 4, T = 5 * nt + (odd ? (j + 1) % 5 : j), n = row & 15;
      vd[i] = (piece_col(T, n >> 2, j == 4) + (n & 3)) * 1280 + ((lane & 7) ^ ((row >> 1) & 7)) * 16;
    }
    auto e2 = [&](int s0, int s1) {
#pragma unroll 1
      for (int s = s0; s < s1; s++) {
        const rsrc_t r = mkrsrc(s < 10 ? p.dr : p.di - 1280, D_ * D_ * 2 + (s < 10 ? 0 : 1280));
        ldsp d = smem + ((s + 13) & 15) * 10240 + lw * 5120;
#pragma unroll
        for (int i = 0; i < 5; i++) dma<0>(r, d + i * 1024, vd[i], s * 128);
      }
    };
    {
      const rsrc_t ra = mkrsrc((hf ? p.ci : p.cr) + (size_t)tg * 128 * 4096, 128 * 4096);
      int va[8], vb[5];
#pragma unroll
      for (int i = 0; i < 8; i++) { const int row = (lw * 8 + i) * 8 + (lane >> 3); va[i] = row * 4096 + ((lane & 7) ^ ((row >> 1) & 7)) * 16; }
#pragma unroll
      for (int i = 0; i < 5; i++) vb[i] = OFF_LNF + vt[i] * 65536 + ((lw + i) & 1) * 1024 + lane * 16;
      auto l1 = [&](int s, int so) {
#pragma unroll
        for (int i = 0; i < 5; i++) dma<SC1>(rw, smem + so + (16 + lw * 5 + i) * 1024, vb[i],
          ((s + 4 * nt) & 31) * 2048
          );
      };
      auto e1 = [&](int s, int so, bool wa) {
        if (wa) {
#pragma unroll
          for (int i = 0; i < 8; i++) dma<0>(ra, smem + so + (lw * 8 + i) * 1024, va[i], ((s + 4 * nt) & 31) * 128);
        }
        l1(s, so);
      };
      bar();
#pragma unroll
      for (int s = 0; s < NPRE; s++)
#pragma unroll
        for (int i = 0; i < 8; i++) { dma<0>(ra, smem + (s + 3) % 5 * L1S + (lw * 8 + i) * 1024, va[i], (s + 4 * nt) * 128); if (s * 8 + i >= CTH - 1) waitv<CTH - 1>(); }
      lds_wait(smem + L0C, 4);
      tag = __builtin_amdgcn_readfirstlane(*(volatile LDS unsigned*)(smem + L0C + 16));
      TS(16);
      polln<0xffffffffu>(rw, lane < 32 ? OFF_F0 + (32 * nt + lane) * FSTR : OOB, tag);
      TS(17);
      constexpr int N2 = NPRE > 2 ? 5 : 13, N3 = NPRE > 3 ? 5 : 13;
      l1(0, 3 * L1S);
      v4u fp = ld16_async(p.ws, OFF_F0P + lane * 16);
      l1(1, 4 * L1S); e1(2, 0, NPRE < 3);
      waitv<6 + N2>(); bar();
      e1(3, L1S, NPRE < 4);
      waitv<N2 + N3>();
      asm volatile("" : "+v"(fp));
      if (lw == 0) {
        if (!eq4(fp, tag)) poll256(rw, OFF_F0P + lane * 16, tag);
        if (b == 0 && lane == 0) st4(rw, OFF_E, tag);
      }
      bar();
      e1(4, 2 * L1S, true); waitv<N3 + 13>(); bar();
      int so = 3 * L1S;
#pragma unroll 1
      for (int s = 5; s < 32; s++) {
        e1(s, so, true); so += L1S; if (so == 5 * L1S) so = 0;
        waitv<26>(); bar();
      }
      e2(0, 5); waitv<38>(); bar();
      e2(5, TD < 8 ? TD : 8); waitv<5 * (TD < 8 ? TD : 8)>(); bar();
      e2(8, TD);
      if (MT1 == 4) { bar(); bar(); }
      bar();
      e2(TD, 16);
      TS(59); bar();
    }
    int vb3[7];
#pragma unroll
    for (int i = 0; i < 7; i++) { const int f = lw * 7 + i, j = f >> 1, T = j < 6 ? T0 + odd3 + j : Ts;
      vb3[i] = (j < 6 || seven) ? OFF_W1F + T * 20480 + (f & 1) * 1024 + lane * 16 : OOB; }
    auto e3 = [&](int s0, int s1) {
#pragma unroll 1
      for (int s = s0; s < s1; s++) {
        ldsp d = smem + (10 + 14 * s + lw * 7) * K1;
#pragma unroll
        for (int i = 0; i < 7; i++) dma<SC1>(rw, d + i * 1024, vb3[i], s * 2048);
      }
    };
    const int fl = lane < 16 ? OFF_FG + lane * FSTR : OOB, i2 = lw * 64 + lane;
    unsigned cpart[20];
    unsigned short bb;
    {
      if (lw == 0) polln<0xffffu>(rw, fl + tg * 16 * FSTR, tag); else waitv<0>();
      TS(18);
      bar();
      v4u fp = ld16_async(p.ws, OFF_FWP + lane * 16);
      bar();
      e2(16, 20);
      waitv<0>();
      asm volatile("" : "+v"(fp));
      if (lw == 0 && !eq4(fp, tag)) poll256(rw, OFF_FWP + lane * 16, tag);
      bar();
      e3(0, TW);
      bar(); bar();
      const int j = i2 >> 4, T = j < 6 ? T0 + odd3 + j : Ts;
      const bool on = i2 < (seven ? 112 : 96);
      const int vc = on ? OFF_CS + (T * 16 + (i2 & 15)) * 4 : OOB;
      e3(TW, 10);
      bb = __builtin_amdgcn_raw_buffer_load_b16(mkrsrc(p.b1, F_ * 2), on ? (piece_col(T, (i2 & 15) >> 2, split1728(T)) + (i2 & 3)) * 2 : OOB, 0, 0);
#pragma unroll
      for (int k = 0; k < 20; k++) cpart[k] = ld4(rw, vc, k * F_ * 4);
      TS(60); bar();
    }
    int vb4[5];
#pragma unroll
    for (int i = 0; i < 5; i++) vb4[i] = OFF_W2F + vt[i] * 55296 + ((lw + i) & 1) * 1024 + lane * 16;
    auto e4 = [&](int s0, int s1) {
#pragma unroll 1
      for (int s = s0; s < s1; s++) {
        ldsp d = smem + (s >= 15 ? s - 15 : s) * 10240 + lw * 5120;
#pragma unroll
        for (int i = 0; i < 5; i++) dma<SC1>(rw, d + i * 1024, vb4[i], s * 2048);
      }
    };
    {
      if (lw == 0) polln<0xffffu>(rw, fl + (256 + tg3 * 16) * FSTR, tag); else waitv<0>();
      TS(19);
      bar();
      v2u sv[8];
#pragma unroll
      for (int k = 0; k < 8; k++) sv[k] = ld8(rw, OFF_ST + (tg3 * 128 + i2) * 8, k * 16384);
      e4(0, 1);
      float csum = 0, m = 0, q = 0;
#pragma unroll
      for (int k = 0; k < 20; k++) { asm volatile("" : "+v"(cpart[k])); csum += asf(cpart[k]); }
      const float bias = lo(bb);
#pragma unroll
      for (int k = 0; k < 8; k++) { asm volatile("" : "+v"(sv[k])); m += asf(sv[k][0]); q += asf(sv[k][1]); }
      m *= 0.125f;
#pragma unroll
      for (int k = 0; k < 8; k++) { const float d = asf(sv[k][0]) - m; q += 80.f * d * d; }
      const float rs = 1.0f / sqrtf(q * (1.f / 640.f) + 1e-5f);
      const unsigned a0 = (unsigned)(size_t)(smem + L3X + i2 * 4), a1 = (unsigned)(size_t)(smem + L3X + 1024 + i2 * 8);
      asm volatile("ds_write_b32 %0, %1\n ds_write_b32 %0, %2 offset:512\n ds_write_b64 %3, %4\n s_waitcnt lgkmcnt(0)" ::"v"(a0), "v"(csum), "v"(bias), "v"(a1), "v"(v2f{m * rs, rs}) : "memory");
      bar();
      e4(1, TX1);
      bar();
      e4(TX1, TX2);
      bar();
      e4(TX2, 15);
      TS(61); bar();
    }
    {
      if (lw == 0) polln<0xffffu>(rw, fl + (512 + tg * 16) * FSTR, tag); else waitv<0>();
      TS(20);
      bar();
      bar();
      e4(15, 19); bar();
      e4(19, 23); waitv<20>(); bar();
      e4(23, 27); waitv<20>(); bar();
      waitv<0>(); bar();
    }
    if (w == 4) { TSOUT(16, 5); TSOUT(59, 3); }
  } else {
    const int hw = w - 6, nu = hw == 0 && b < 56 ? 3 : 2;
    if (hw == 0) {
      unsigned long long pc; unsigned back;
      asm volatile("s_getpc_b64 %0\n.Lfnet_pc:\n s_mov_b32 %1, .Lfnet_pc-_Z6k_fnet6Params" : "=s"(pc), "=s"(back));
      dma<0>(mkrsrc((const void*)(pc - back), CODELEN), smem + L0D, (b >> 3) * 1024 + lane * 16, 0);
    }
    bar();
#pragma unroll 1
    for (int n = 0; n < nu; n++) {
      const int i = n < 2 ? hw * 2 + n : 4, u = b + 256 * i;
      const bool isw1 = u < 540;
      const int uu = isw1 ? u : u - 540, kb = isw1 ? uu / 27 : uu / 10, cu = isw1 ? uu % 27 : uu % 10, ld = isw1 ? F_ * 2 : D_ * 2;
      const rsrc_t r = mkrsrc(isw1 ? p.w1 : p.w2, D_ * F_ * 2);
#pragma unroll
      for (int k = 0; k < 4; k++) dma<0>(r, smem + L0W + i * 4096 + k * 1024, (kb * 32 + k * 8 + (lane >> 3)) * ld + cu * 128 + (lane & 7) * 16, 0);
    }
    waitv<0>();
    TS(22);
    lds_wait(smem + L0C + 4, 4);
    const int qq = (lane & 15) >> 2, pp = lane & 3;
#pragma unroll 1
    for (int n = 0; n < nu; n++) {
      const int i = n < 2 ? hw * 2 + n : 4, u = b + 256 * i;
      const bool isw1 = u < 540;
      const int uu = isw1 ? u : u - 540, kb = isw1 ? uu / 27 : uu / 10, cu = isw1 ? uu % 27 : uu % 10;
      unsigned a[4];
#pragma unroll
      for (int t = 0; t < 4; t++) { const int T = cu * 4 + t;
        a[t] = (unsigned)(size_t)(smem + L0W + i * 4096 + (g * 8 + qq) * 128 + (piece_col(T, pp, isw1 ? split1728(T) : split640(T)) - cu * 64) * 2); }
      v2u v[8];
      asm volatile("ds_read_b64_tr_b16 %0, %8\n ds_read_b64_tr_b16 %1, %8 offset:512\n ds_read_b64_tr_b16 %2, %9\n ds_read_b64_tr_b16 %3, %9 offset:512\n"
                   "ds_read_b64_tr_b16 %4, %10\n ds_read_b64_tr_b16 %5, %10 offset:512\n ds_read_b64_tr_b16 %6, %11\n ds_read_b64_tr_b16 %7, %11 offset:512\n s_waitcnt lgkmcnt(0)"
                   : "=&v"(v[0]), "=&v"(v[1]), "=&v"(v[2]), "=&v"(v[3]), "=&v"(v[4]), "=&v"(v[5]), "=&v"(v[6]), "=&v"(v[7]) : "v"(a[0]), "v"(a[1]), "v"(a[2]), "v"(a[3]) : "memory");
      const int dst = (isw1 ? OFF_W1F + cu * 4 * 20480 : OFF_W2F + cu * 4 * 55296) + kb * 1024 + lane * 16;
#pragma unroll
      for (int t = 0; t < 4; t++) st16(rw, dst + t * (isw1 ? 20480 : 55296), v4u{v[2 * t][0], v[2 * t][1], v[2 * t + 1][0], v[2 * t + 1][1]});
      if (isw1) {
        float cs = 0;
#pragma unroll
        for (int t = 0; t < 4; t++) {
          const v2u v0 = v[2 * t], v1 = v[2 * t + 1];
          const float c1 = xsum32(xsum16((lo(v0[0]) + hi(v0[0])) + (lo(v0[1]) + hi(v0[1])) + (lo(v1[0]) + hi(v1[0])) + (lo(v1[1]) + hi(v1[1]))));
          cs = g == t ? c1 : cs;
        }
        st4(rw, OFF_CS + (kb * F_ + cu * 64 + lane) * 4, asu(cs));
      }
    }
    TS(23);
#pragma unroll
    for (int k = 0; k < 4; k++) {
      const int q = b + 256 * (hw * 3 + k);
      const rsrc_t r = hw || k < 3 ? mkrsrc(q < 800 ? p.dr + q * 1024 : p.di + (q - 800) * 1024, q < 1600 ? 1024 : 0)
                                   : mkrsrc(b & 32 ? p.b2 : p.b1, b & 32 ? D_ * 2 : F_ * 2);
      dma<0>(r, smem + L0D, lane * 16 + (hw || k < 3 ? 0 : (b >> 3 & 3) * 1024), 0);
    }
    waitv<4>();
    const unsigned tag = *(volatile LDS unsigned*)(smem + L0C + 16);
    unsigned old = 0;
    if (lane == 0) old = lds_add(smem + L0C + 8);
    if (__builtin_amdgcn_readfirstlane(old) == 1 && lane == 0) st4(rw, OFF_FWP + b * 4, tag);
    TS(21);
    bar(); bar(); bar();
    waitv<0>();
    if (w == 6) TSOUT(21, 3);
  }
#endif
}

extern "C" void kernel_launch(void* const* d_in, const int* in_sizes, int n_in, void* d_out, int out_size, void* d_ws, size_t ws_size,
                              hipStream_t stream) {
  Params p;
  p.x = (const char*)d_in[0]; p.cr = (const char*)d_in[1]; p.ci = (const char*)d_in[2]; p.dr = (const char*)d_in[3]; p.di = (const char*)d_in[4];
  p.w1 = (const char*)d_in[6]; p.b1 = (const char*)d_in[7]; p.w2 = (const char*)d_in[8]; p.b2 = (const char*)d_in[9];
  p.out = (char*)d_out; p.ws = (char*)d_ws;
  (void)hipFuncSetAttribute((const void*)k_fnet, hipFuncAttributeMaxDynamicSharedMemorySize, LDS_BYTES);
  k_fnet<<<256, 512, LDS_BYTES, stream>>>(p);
}
```

```cpp
#include <hip/hip_runtime.h>
#include <hip/hip_bf16.h>
#include <stdint.h>

#define CTH 8
#define MT1 2
#define NPRE 3
#define TD 8
#define TW 4
#define TX1 4
#define TX2 9
#define UD 6
#define HK3 8
#define RB 3
#define ROT 3
#define NS2 4
#define NS4 0
#define US2 16
#define US3 13
#define LP 0
#define LP1 3
#define SP 1
#define SP1 0
#define HV 0
#define HS0 3
#define HSD 2
#define EPUB 1
#define REMAP3 1
#define CODELEN 32000

#define LDS __attribute__((address_space(3)))
typedef LDS char* ldsp;
typedef short v4s __attribute__((ext_vector_type(4)));
typedef short v8s __attribute__((ext_vector_type(8)));
typedef __bf16 v8b __attribute__((ext_vector_type(8)));
typedef __bf16 v2b __attribute__((ext_vector_type(2)));
typedef float v4f __attribute__((ext_vector_type(4)));
typedef float v2f __attribute__((ext_vector_type(2)));
typedef unsigned v4u __attribute__((ext_vector_type(4)));
typedef unsigned v2u __attribute__((ext_vector_type(2)));

constexpr int S_ = 2048, D_ = 640, F_ = 1728;
constexpr int OFF_E   = 0;
constexpr int FSTR    = 128;
constexpr int OFF_F0  = 4096;
constexpr int OFF_F0P = 1024;
constexpr int OFF_FWP = 2048;
constexpr int OFF_FG  = OFF_F0 + 256 * FSTR;
constexpr int OFF_LNF = 256 * 1024;
constexpr int OFF_PQF = OFF_LNF + 40 * 64 * 1024;
constexpr int OFF_HF  = OFF_PQF + 128 * 40 * 1024;
constexpr int OFF_HH  = OFF_HF + 128 * 20 * 1024;
constexpr int OFF_W1F = OFF_HH + 128 * 54 * 1024;
constexpr int OFF_W2F = OFF_W1F + 108 * 20 * 1024;
constexpr int OFF_CS  = OFF_W2F + 40 * 54 * 1024;
constexpr int OFF_ST  = OFF_CS + 20 * 1728 * 4;
constexpr int WS_BYTES = OFF_ST + 8 * 2048 * 8;
constexpr int OOB = 0x40000000;
constexpr int LDS_BYTES = 160 * 1024;

struct Params {
  const char* x; const char* cr; const char* ci; const char* dr; const char* di;
  const char* w1; const char* b1; const char* w2; const char* b2;
  char* out; char* ws;
};

#define TS(i) do {} while (0)
#define TSOUT(a, n)

#if defined(__HIP_DEVICE_COMPILE__)
typedef __amdgpu_buffer_rsrc_t rsrc_t;
constexpr int SC1 = 16;
__device__ __forceinline__ rsrc_t mkrsrc(const void* p, unsigned bytes) { return __builtin_amdgcn_make_buffer_rsrc((void*)p, 0, bytes, 0x00020000); }
template <int AUX> __device__ __forceinline__ void dma(rsrc_t r, ldsp dst, int voff, int soff) {
  __builtin_amdgcn_raw_ptr_buffer_load_lds(r, (LDS void*)dst, 16, voff, soff, 0, AUX);
}
__device__ __forceinline__ void st16(rsrc_t r, int off, v4u v) { __builtin_amdgcn_raw_buffer_store_b128(v, r, off, 0, SC1); }
__device__ __forceinline__ void st8(rsrc_t r, int off, v2u v) { __builtin_amdgcn_raw_buffer_store_b64(v, r, off, 0, SC1); }
__device__ __forceinline__ void st4(rsrc_t r, int off, unsigned v) { __builtin_amdgcn_raw_buffer_store_b32(v, r, off, 0, SC1); }
__device__ __forceinline__ v4u ld16(rsrc_t r, int off) { return __builtin_amdgcn_raw_buffer_load_b128(r, off, 0, SC1); }
__device__ __forceinline__ v2u ld8(rsrc_t r, int off, int soff) { return __builtin_amdgcn_raw_buffer_load_b64(r, off, soff, SC1); }
__device__ __forceinline__ unsigned ld4(rsrc_t r, int off, int soff) { return __builtin_amdgcn_raw_buffer_load_b32(r, off, soff, SC1); }
template <int N> __device__ __forceinline__ void waitv() { asm volatile("s_waitcnt vmcnt(%0)" ::"n"(N) : "memory"); }
__device__ __forceinline__ void bar() { asm volatile("" ::: "memory"); __builtin_amdgcn_s_barrier(); asm volatile("" ::: "memory"); }
__device__ __forceinline__ void waitl0() { __builtin_amdgcn_sched_barrier(0); __builtin_amdgcn_s_waitcnt(0xC07F); __builtin_amdgcn_sched_barrier(0); }
__device__ __forceinline__ v4f mfma(v8s a, v8s b, v4f c) {
  return __builtin_amdgcn_mfma_f32_16x16x32_bf16(__builtin_bit_cast(v8b, a), __builtin_bit_cast(v8b, b), c, 0, 0, 0);
}
__device__ __forceinline__ unsigned pk(float a, float b) { v2b r = {(__bf16)a, (__bf16)b}; return __builtin_bit_cast(unsigned, r); }
__device__ __forceinline__ float asf(unsigned u) { return __builtin_bit_cast(float, u); }
__device__ __forceinline__ unsigned asu(float f) { return __builtin_bit_cast(unsigned, f); }
__device__ __forceinline__ float lo(unsigned u) { return asf(u << 16); }
__device__ __forceinline__ float hi(unsigned u) { return asf(u & 0xffff0000u); }
template <int CTRL> __device__ __forceinline__ float dppadd(float v) { return v + asf(__builtin_amdgcn_update_dpp(0, asu(v), CTRL, 0xf, 0xf, true)); }
__device__ __forceinline__ float rowsum(float v) { return dppadd<0x121>(dppadd<0x122>(dppadd<0x124>(dppadd<0x128>(v)))); }
__device__ __forceinline__ float xsum16(float v) { const auto r = __builtin_amdgcn_permlane16_swap(asu(v), asu(v), false, false); return asf(r[0]) + asf(r[1]); }
__device__ __forceinline__ float xsum32(float v) { const auto r = __builtin_amdgcn_permlane32_swap(asu(v), asu(v), false, false); return asf(r[0]) + asf(r[1]); }
template <int NM, int ND> __device__ __forceinline__ void ilv() {
#pragma unroll
  for (int i = 0; i < NM; i++) { __builtin_amdgcn_sched_group_barrier(0x8, 1, 0); if (SP1 ? i % 3 != 2 : i < ND) __builtin_amdgcn_sched_group_barrier(0x100, 1, 0); }
}
__device__ __forceinline__ int piece_col(int T, int p, bool split) { return 16 * T + 4 * p + ((4 * p - 12 * (T & 1)) & ((int)split - 1)); }
__device__ __forceinline__ bool split640(int T) { return (T >> 1) % 5 == 2; }
__device__ __forceinline__ bool split1728(int T) { return (T >> 1) % 7 == 3 && T < 84; }
__device__ __forceinline__ unsigned lds_add(ldsp a) { return __hip_atomic_fetch_add((LDS unsigned*)a, 1u, __ATOMIC_RELAXED, __HIP_MEMORY_SCOPE_WORKGROUP); }
__device__ __forceinline__ void lds_wait(ldsp a, unsigned v) { while (*(volatile LDS unsigned*)a != v) __builtin_amdgcn_s_sleep(1); asm volatile("" ::: "memory"); }
constexpr int SPIN_CAP = 1 << 17;
__device__ __forceinline__ v4u ld16_async(const char* base, unsigned off) { v4u v; asm volatile("global_load_dwordx4 %0, %1, %2 sc1" : "=v"(v) : "v"(off), "s"(base) : "memory"); return v; }
template <int SO> __device__ __forceinline__ void ld4_async(unsigned& d, v4u r, int voff) {
  int t; asm volatile("s_mov_b32 %1, %4\n buffer_load_dword %0, %2, %3, %1 offen sc1" : "=v"(d), "=&s"(t) : "v"(voff), "s"(r), "n"(SO));
}
__device__ __forceinline__ void ld2_async(unsigned& d, v4u r, int voff) { asm volatile("buffer_load_ushort %0, %1, %2, 0 offen" : "=v"(d) : "v"(voff), "s"(r)); }
__device__ __forceinline__ v4u srd(const void* p, unsigned bytes) { return v4u{(unsigned)(size_t)p, (unsigned)((size_t)p >> 32) & 0xffffu, bytes, 0x00020000u}; }
__device__ __forceinline__ void sbar() { __builtin_amdgcn_sched_barrier(0); }
__device__ __forceinline__ bool eq4(v4u v, unsigned tag) { return __builtin_amdgcn_ballot_w64(v[0] != tag || v[1] != tag || v[2] != tag || v[3] != tag) == 0; }
__device__ __forceinline__ void poll256(rsrc_t r, int off, unsigned tag) {
#pragma unroll 1
  for (int it = 0; it < SPIN_CAP; it++) { asm volatile("" ::: "memory"); if (eq4(ld16(r, off), tag)) break; __builtin_amdgcn_s_sleep(2); }
  asm volatile("" ::: "memory");
}
template <unsigned long long MASK> __device__ __forceinline__ void polln(rsrc_t r, int off, unsigned tag) {
#pragma unroll 1
  for (int it = 0; it < SPIN_CAP; it++) { asm volatile("" ::: "memory"); if ((__builtin_amdgcn_ballot_w64(ld4(r, off, 0) != tag) & MASK) == 0) break; __builtin_amdgcn_s_sleep(1); }
  asm volatile("" ::: "memory");
}

template <int V> struct IC { static constexpr int v = V; };
template <int A, int B, class F> __device__ __forceinline__ void sfor(F f) { if constexpr (A < B) { f(IC<A>{}); sfor<A + 1, B>(f); } }

constexpr int K1 = 1024, L1S = 26 * K1, L1R = 90 * K1, L3X = 150 * K1;
constexpr int RS = 1312;
constexpr int L0R = 52 * K1, L0W = 130 * K1, L0C = 150 * K1, L0D = 152 * K1;

template <int MT>
__device__ __forceinline__ void consume1(ldsp smem, int w, int lane, v4f (&acc)[MT][5]) {
  const int g = lane >> 4, r = lane & 15;
  const int kh = MT == 4 ? w >> 1 : 0, rb = MT == 4 ? (w & 1) * 4 : w * 2;
  const int a0 = (rb * 16 + r) * 128 + (((kh * 4 + g) ^ (r >> 1)) * 16), a1 = MT == 4 ? a0 : a0 ^ 64;
  const int b0 = 16 * K1 + kh * K1 + lane * 16, b1 = MT == 4 ? b0 : b0 + K1;
  v8s pa[MT], pb[5], qa[MT], qb[5];
#pragma unroll
  for (int i = 0; i < MT; i++) { qa[i] = v8s{0, 0, 0, 0, 0, 0, 0, 0};
#pragma unroll
    for (int j = 0; j < 5; j++) acc[i][j] = v4f{0, 0, 0, 0}; }
#pragma unroll
  for (int j = 0; j < 5; j++) qb[j] = v8s{0, 0, 0, 0, 0, 0, 0, 0};
#define RD(A, B, ao, bo) { B[0] = *(LDS v8s*)(smem + so + bo); _Pragma("unroll") for (int i = 0; i < MT; i++) A[i] = *(LDS v8s*)(smem + so + ao + i * 2048); \
                           _Pragma("unroll") for (int j = 1; j < 5; j++) B[j] = *(LDS v8s*)(smem + so + bo + j * 2048); }
#define MM(A, B) { _Pragma("unroll") for (int j = 0; j < 5; j++) _Pragma("unroll") for (int i = 0; i < MT; i++) acc[i][j] = mfma(B[j], A[i], acc[i][j]); }
#define NEXT { bar(); so += L1S; if (so == 5 * L1S) so = 0; }
  int so = 3 * L1S;
#pragma unroll 1
  for (int s = 0; s < (MT == 4 ? 15 : 31); s++) {
    RD(pa, pb, a0, b0); MM(qa, qb); ilv<5 * MT, 5 + MT>(); sbar();
    if (MT == 4) NEXT
    RD(qa, qb, a1, b1); MM(pa, pb); ilv<5 * MT, 5 + MT>(); sbar();
    NEXT
  }
  RD(pa, pb, a0, b0); MM(qa, qb); ilv<5 * MT, 5 + MT>(); sbar();
  if (MT == 4) NEXT
  RD(qa, qb, a1, b1); MM(pa, pb); ilv<5 * MT, 5 + MT>();
  if (MT == 4) { waitl0(); bar(); } else sbar();
  MM(qa, qb);
#undef RD
#undef MM
#undef NEXT
}

template <int NJ, int NU, bool PAIR, int BL, int NR, class FB, class FS, class FX, class FH, class FT>
__device__ __forceinline__ void consume(ldsp bl0, rsrc_t rw, int va0, int va1, ldsp mine, v4f (&acc)[2][NJ], const v8s (&rb)[NR + 1][NJ], const int (&so)[BL], FB boff, FS sync, FX extra, FH hook, FT stamp) {
  v8s a[UD][2], bq[2][NJ];
  int va[2] = {va0, va1};
  ldsp bl[3] = {bl0, bl0 + 65536, bl0 + 131072};
  asm volatile("" : "+v"(va[0]), "+v"(va[1]), "+v"(bl[0]), "+v"(bl[1]), "+v"(bl[2]));
  bar();
  stamp(-1);
#define LDA(u) _Pragma("unroll") for (int i = 0; i < 2; i++) a[(u) % UD][i] = __builtin_bit_cast(v8s, \
    __builtin_amdgcn_raw_buffer_load_b128(rw, va[i] + (PAIR ? (u) / BL * BL * 2048 : ((u) & 1) * 1024), so[(PAIR ? (u) : (u) >> 1) % BL], SC1));
#define LDB(u) _Pragma("unroll") for (int j = 0; j < NJ; j++) bq[(u) & 1][j] = *(LDS v8s*)(bl[(boff(u) + j * 2048) >> 16] + ((boff(u) + j * 2048) & 0xffff));
  LDA(0) sbar(); if (!NR) { LDB(0) } sbar();
#pragma unroll
  for (int u = 1; u < UD - 1; u++) { LDA(u) sbar(); }
  extra();
  sbar();
  sfor<1, NU + 1>([&](auto uc) {
    constexpr int u = uc.v;
    if (u < NU) { if (sync(u)) bar(); if (u >= NR) { LDB(u) } }
    const bool ld = u - 2 + UD < NU;
    if (ld) { LDA(u - 2 + UD) }
    hook(u);
    if constexpr (PAIR && u == NU) {
#pragma unroll
      for (int j = 0; j < NJ; j++) acc[1][j] = mfma(bq[(u - 1) & 1][j], a[(u - 1) % UD][1], acc[1][j]);
      sbar();
#pragma unroll
      for (int j = 0; j < NJ; j++) {
        acc[0][j] = mfma(bq[(u - 1) & 1][j], a[(u - 1) % UD][0], acc[0][j]);
        *(LDS v4f*)(mine + j * 1024) = acc[1][j];
        sbar();
      }
      stamp(u);
      return;
    }
#pragma unroll
    for (int j = 0; j < NJ; j++) {
      v8s bf;
      if (u <= NR) bf = rb[u <= NR ? u - 1 : 0][j]; else bf = bq[(u - 1) & 1][j];
#pragma unroll
      for (int i = 0; i < 2; i++) acc[i][j] = mfma(bf, a[(u - 1) % UD][i], u == 1 && (i || !PAIR) ? v4f{0, 0, 0, 0} : acc[i][j]);
    }
#pragma unroll
    for (int k = 0; k < 2 * NJ; k++) {
      __builtin_amdgcn_sched_group_barrier(0x8, 1, 0);
      if (u < NU && u >= NR && (SP == 1 ? (k & 1) : SP == 2 ? !(k & 1) : k < NJ)) __builtin_amdgcn_sched_group_barrier(0x100, 1, 0);
      if (ld && (SP == 1 ? k == 2 || k == 6 : SP == 2 ? k == 3 || k == 7 : k >= NJ && k < NJ + 2)) __builtin_amdgcn_sched_group_barrier(0x20, 1, 0);
    }
    sbar();
    stamp(u);
  });
#undef LDA
#undef LDB
}

__device__ __forceinline__ void pair_add(ldsp other, v4f (&acc)[2][5], v4f (&o)[5]) {
#pragma unroll
  for (int j = 0; j < 5; j++) o[j] = *(LDS v4f*)(other + j * 1024);
  sbar();
#pragma unroll
  for (int j = 0; j < 5; j++) o[j] += acc[0][j];
}
#endif

__global__ __launch_bounds__(512) void k_fnet(Params p) {
#if defined(__HIP_DEVICE_COMPILE__)
  extern __shared__ __attribute__((aligned(16))) char smem_[];
  ldsp smem = (ldsp)smem_;
  const int lane = threadIdx.x & 63, w = __builtin_amdgcn_readfirstlane(threadIdx.x >> 6), b = blockIdx.x;
  const int tg = (b & 7) * 2 + (b >> 7), c = (b >> 3) & 15, hf = c >> 3, nt = c & 7, odd = nt & 1;
  const int tg3 = REMAP3 ? (b & 6) * 2 + (b >> 6) : tg, c3 = REMAP3 ? (b & 1) * 8 + nt : c;
  const bool seven = c3 < 12;
  const int odd3 = seven ? c3 & 1 : 0, T0 = seven ? 7 * c3 : 84 + 6 * (c3 - 12), Ts = odd3 ? T0 : T0 + 6;
  const int rot = ROT >= 2 ? nt + 2 * (b >> 6) : ROT ? nt : 0, rot10 = ROT == 3 ? 0 : rot % 10, rot9 = rot % 9;
  const int g = lane >> 4;
  const rsrc_t rw = mkrsrc(p.ws, WS_BYTES);
  if (__builtin_expect(w < 4, 1)) {
    TS(0);
    unsigned ep = ld4(rw, OFF_E, 0);
    const v2u* xr = (const v2u*)(p.x + (size_t)(b * 8 + w * 2 + (lane >> 5)) * 1280) + (lane & 31);
    v2u xv[5];
#pragma unroll
    for (int i = 0; i < 5; i++) xv[i] = xr[32 * i];
    if (w == 0 && lane < 4) *(LDS unsigned*)(smem + L0C + lane * 4) = 0;
    waitl0(); bar();
    asm volatile("" : "+v"(ep), "+v"(xv[0]), "+v"(xv[1]), "+v"(xv[2]), "+v"(xv[3]), "+v"(xv[4]));
    const unsigned tag = __builtin_amdgcn_readfirstlane(ep) + 1;
    {
      float v[20], s = 0;
#pragma unroll
      for (int i = 0; i < 5; i++) { v[4 * i] = lo(xv[i][0]); v[4 * i + 1] = hi(xv[i][0]); v[4 * i + 2] = lo(xv[i][1]); v[4 * i + 3] = hi(xv[i][1]); }
#pragma unroll
      for (int i = 0; i < 20; i++) s += v[i];
      const float mean = xsum16(rowsum(s)) * (1.f / 640.f);
      float q = 0;
#pragma unroll
      for (int i = 0; i < 20; i++) { v[i] -= mean; q += v[i] * v[i]; }
      const float rs = 1.0f / sqrtf(xsum16(rowsum(q)) * (1.f / 640.f) + 1e-5f);
#pragma unroll
      for (int i = 0; i < 5; i++)
        *(LDS v2u*)(smem + L0R + (w * 2 + (lane >> 5)) * RS + ((lane & 31) + 32 * i) * 8) = v2u{pk(v[4 * i] * rs, v[4 * i + 1] * rs), pk(v[4 * i + 2] * rs, v[4 * i + 3] * rs)};
      if (w == 0 && lane == 0) *(LDS unsigned*)(smem + L0C + 16) = tag;
      waitl0();
      if (lane == 0) lds_add(smem + L0C);
      lds_wait(smem + L0C, 4);
      TS(1);
      const int qq = (lane & 15) >> 2, pp = lane & 3;
#pragma unroll
      for (int i = 0; i < 3; i++) {
        const int T = 10 * w + 4 * i + (i < 2 ? g : g & 1);
        ldsp a = smem + L0R + qq * RS + piece_col(T, pp, split640(T)) * 2;
        const v4s v0 = __builtin_amdgcn_ds_read_tr16_b64_v4i16((LDS v4s*)a), v1 = __builtin_amdgcn_ds_read_tr16_b64_v4i16((LDS v4s*)(a + 4 * RS));
        st16(rw, i == 2 && g >= 2 ? OOB : OFF_LNF + T * 65536 + (b >> 2) * 1024 + (b & 3) * 256 + (lane & 15) * 16,
             __builtin_bit_cast(v4u, __builtin_shufflevector(v0, v1, 0, 1, 2, 3, 4, 5, 6, 7)));
      }
      waitv<0>();
      unsigned old = 0;
      if (lane == 0) old = lds_add(smem + L0C + 4);
      if (__builtin_amdgcn_readfirstlane(old) == 3 && lane < 2) st4(rw, lane ? OFF_F0P + b * 4 : OFF_F0 + b * FSTR, tag);
      TS(2);
    }
#define PUB(i) st4(rw, lane == 0 ? OFF_FG + (i) * FSTR + w * 4 : OOB, tag)
    const int kb0 = 5 * (nt >> 1), sp = ((odd * 2 + (g >> 1)) * 16 + (lane & 15)) * 16 + (g & 1) * 8;
    const int kh = w >> 1, tb0 = tg * 8 + hf * 4 + (w & 1) * 2, tb = tb0 + kh, t = tb * 16 + (lane & 15);
    const int cp = (kb0 + odd * 3) * 32 + g * 8, cs = (kb0 + 2) * 32 + odd * 16 + g * 4;
    sbar();
    const v4u x0 = *(const v4u*)(p.x + (size_t)t * 1280 + cp * 2), x1 = *(const v4u*)(p.x + (size_t)t * 1280 + cp * 2 + 64);
    const v2u x2 = *(const v2u*)(p.x + (size_t)t * 1280 + cs * 2);
    sbar();
    {
      v4f acc[MT1][5];
      bar(); TS(3);
      consume1<MT1>(smem, w, lane, acc);
      TS(4);
      v4f (&o)[2][5] = acc;
      const int tb1 = tg * 8 + w * 2;
      const float sg = hf ? -1.f : 1.f;
#pragma unroll
      for (int mi = 0; mi < 2; mi++) {
        const int dst = OFF_PQF + (tb1 + mi) * 40960 + (hf * 20 + kb0) * 1024;
#pragma unroll
        for (int u = 0; u < 2; u++) {
          const v4f a = o[mi][2 * u] * sg, c2 = o[mi][2 * u + 1] * sg;
          st16(rw, dst + (odd * 3 + u) * 1024 + lane * 16, v4u{pk(a[0], a[1]), pk(a[2], a[3]), pk(c2[0], c2[1]), pk(c2[2], c2[3])});
        }
        const v4f a = o[mi][4] * sg;
        st8(rw, dst + 2048 + sp, v2u{pk(a[0], a[1]), pk(a[2], a[3])});
      }
      TS(50); bar(); TS(51); waitv<0>(); TS(52);
      PUB(tg * 16 + c);
      TS(5);
    }
    v4u h0, h1, c0, c1; v2u h2, c2;
    int so10[10];
#pragma unroll
    for (int i = 0; i < 10; i++) { so10[i] = (i + rot10 < 10 ? i + rot10 : i + rot10 - 10) * 2048; asm volatile("" : "+s"(so10[i])); }
    const v8s none5[1][5] = {}, none7[1][7] = {};
#define UNP(a, b) v4f{lo(a), hi(a), lo(b), hi(b)}
    {
      v4f acc[2][5], z[5];
      const int r = lane & 15;
      acc[0][0] = UNP(x0[0], x0[1]); acc[0][1] = UNP(x0[2], x0[3]); acc[0][2] = UNP(x1[0], x1[1]); acc[0][3] = UNP(x1[2], x1[3]); acc[0][4] = UNP(x2[0], x2[1]);
      asm volatile("" : "+v"(acc[0][0]), "+v"(acc[0][1]), "+v"(acc[0][2]), "+v"(acc[0][3]), "+v"(acc[0][4]));
      consume<5, 20, true, 10, 0>(smem + r * 128 + ((kh * 4 + g) ^ (r >> 1)) * 16, rw, OFF_PQF + tb * 40960 + kh * 1024 + lane * 16,
        OFF_PQF + (tb ^ 1) * 40960 + kh * 1024 + lane * 16, smem + (100 + 5 * w) * K1 + lane * 16, acc, none5, so10,
        [](int u) { return (130 + 10 * u) % 160 * K1; }, [](int u) { return u == 5 || u == US2; }, [] {}, [](int) {}, [&](int u) { if (u < 0) TS(6); if (u == 1) TS(32); if (u == 4) TS(33); if (u == 9) TS(34); if (u == 12) TS(35); if (u == 15) TS(36); if (u == 18) TS(37); });
      TS(7);
      waitl0(); TS(62); bar(); TS(63);
      pair_add(smem + (100 + 5 * (w ^ 2)) * K1 + lane * 16, acc, z);
      h0 = v4u{pk(z[0][0], z[0][1]), pk(z[0][2], z[0][3]), pk(z[1][0], z[1][1]), pk(z[1][2], z[1][3])};
      h1 = v4u{pk(z[2][0], z[2][1]), pk(z[2][2], z[2][3]), pk(z[3][0], z[3][1]), pk(z[3][2], z[3][3])};
      h2 = v2u{pk(z[4][0], z[4][1]), pk(z[4][2], z[4][3])};
      const int dst = OFF_HF + tb * 20480 + kb0 * 1024;
      st16(rw, dst + odd * 3072 + lane * 16, h0);
      st16(rw, dst + odd * 3072 + 1024 + lane * 16, h1);
      st8(rw, dst + 2048 + sp, h2);
      const v4f s4 = (z[0] + z[1]) + (z[2] + z[3]) + z[4];
      const float m = xsum32(xsum16((s4[0] + s4[1]) + (s4[2] + s4[3]))) * (1.f / 80.f);
      v4f q4 = {0, 0, 0, 0};
#pragma unroll
      for (int j = 0; j < 5; j++) { const v4f d = z[j] - m; q4 += d * d; }
      const float q = xsum32(xsum16((q4[0] + q4[1]) + (q4[2] + q4[3])));
      st8(rw, lane < 16 ? OFF_ST + nt * 16384 + t * 8 : OOB, v2u{asu(m), asu(q)});
      TS(53); bar(); TS(54); waitv<0>(); TS(55);
      PUB(256 + tg * 16 + c);
      TS(8);
    }
    {
      const int k3 = (T0 + odd3) >> 1;
      v4f acc[2][7];
      v8s ea[2], eb[7];
      float st[2];
      consume<7, 20, false, 10, 0>(smem + lane * 16, rw, OFF_HF + (tg3 * 8 + w * 2) * 20480 + lane * 16, OFF_HF + (tg3 * 8 + w * 2 + 1) * 20480 + lane * 16, smem, acc, none7, so10,
        [](int u) { return (10 + 14 * (u >> 1) + (u & 1)) * K1; }, [](int u) { return u == 7 || u == US3; }, [&] {
          c0 = *(const v4u*)(p.b2 + cp * 2); c1 = *(const v4u*)(p.b2 + cp * 2 + 64); c2 = *(const v2u*)(p.b2 + cs * 2); },
        [&](int u) {
          if (u == HK3) {
#pragma unroll
            for (int i = 0; i < 2; i++) ea[i] = *(LDS v8s*)(smem + L3X + (g ? 4608 + lane * 16 : 2048 + (w * 32 + lane) * 16) + i * 256);
#pragma unroll
            for (int j = 0; j < 7; j++) eb[j] = *(LDS v8s*)(smem + L3X + (lane & 15) * 16 + j * 256);
          }
          if (u == HK3 + 1) {
#pragma unroll
            for (int j = 0; j < 7; j++)
#pragma unroll
              for (int i = 0; i < 2; i++) acc[i][j] = mfma(eb[j], ea[i], acc[i][j]);
            sbar();
          }
          if (u == 19) { st[0] = *(LDS float*)(smem + L3X + 4096 + (w * 32 + (lane & 15)) * 4); st[1] = *(LDS float*)(smem + L3X + 4160 + (w * 32 + (lane & 15)) * 4); } },
        [&](int u) { if (u < 0) TS(9); if (u == 1) TS(38); if (u == 4) TS(39); if (u == 8) TS(40); if (u == 12) TS(41); if (u == 15) TS(42); if (u == 18) TS(43); });
      TS(10);
#pragma unroll
      for (int mi = 0; mi < 2; mi++) {
        const int dst = OFF_HH + (tg3 * 8 + w * 2 + mi) * 55296;
        unsigned r[14];
#pragma unroll
        for (int j = 0; j < 7; j++) {
          v4f z = acc[mi][j] * st[mi];
          z = __builtin_elementwise_max(z, v4f{0, 0, 0, 0}); z *= z;
          r[2 * j] = pk(z[0], z[1]); r[2 * j + 1] = pk(z[2], z[3]);
          if (j & 1) st16(rw, dst + (k3 + (j >> 1)) * 1024 + lane * 16, v4u{r[2 * j - 2], r[2 * j - 1], r[2 * j], r[2 * j + 1]});
        }
        st8(rw, seven ? dst + (Ts >> 1) * 1024 + ((odd3 * 2 + (g >> 1)) * 16 + (lane & 15)) * 16 + (g & 1) * 8 : OOB, v2u{r[12], r[13]});
      }
      TS(56); bar(); TS(57); waitv<0>(); TS(58);
      PUB(512 + tg3 * 16 + c3);
      TS(11);
    }
    {
      v4f acc[2][5], z[5];
      v8s rb[RB + 1][5];
      int so9[9];
#pragma unroll
      for (int i = 0; i < 9; i++) { so9[i] = (i + rot9 < 9 ? i + rot9 : i + rot9 - 9) * 2048; asm volatile("" : "+s"(so9[i])); }
      sbar();
#pragma unroll
      for (int u = 0; u < RB; u++)
#pragma unroll
        for (int j = 0; j < 5; j++)
          rb[u][j] = __builtin_bit_cast(v8s, __builtin_amdgcn_raw_buffer_load_b128(rw, OFF_W2F + kh * 1024 + lane * 16, (5 * nt + (odd ? (j + 1) % 5 : j)) * 55296 + so9[u], SC1));
      sbar();
      acc[0][0] = UNP(h0[0], h0[1]) + UNP(c0[0], c0[1]); acc[0][1] = UNP(h0[2], h0[3]) + UNP(c0[2], c0[3]); acc[0][2] = UNP(h1[0], h1[1]) + UNP(c1[0], c1[1]);
      acc[0][3] = UNP(h1[2], h1[3]) + UNP(c1[2], c1[3]); acc[0][4] = UNP(h2[0], h2[1]) + UNP(c2[0], c2[1]);
      asm volatile("" : "+v"(acc[0][0]), "+v"(acc[0][1]), "+v"(acc[0][2]), "+v"(acc[0][3]), "+v"(acc[0][4]));
      consume<5, 27, true, 9, RB>(smem + kh * 1024 + lane * 16, rw, OFF_HH + tb * 55296 + kh * 1024 + lane * 16, OFF_HH + (tb ^ 1) * 55296 + kh * 1024 + lane * 16,
        smem + (120 + 5 * w) * K1 + lane * 16, acc, rb, so9,
        [](int u) { return (u + 15 - RB) % 15 * 10 * K1; }, [](int u) { return u == 5 + RB || u == 9 + RB || u == 15 + RB || (NS4 < 8 && u == 19 + RB) || u == 23 + RB; }, [] {}, [](int) {}, [&](int u) { if (u < 0) TS(12); if (u == 1) TS(44); if (u == 6) TS(45); if (u == 11) TS(46); if (u == 16) TS(47); if (u == 21) TS(48); if (u == 25) TS(49); });
      TS(13);
      waitl0(); bar();
      pair_add(smem + (120 + 5 * (w ^ 2)) * K1 + lane * 16, acc, z);
      char* dst = p.out + (size_t)t * 1280;
      *(v4u*)(dst + cp * 2) = v4u{pk(z[0][0], z[0][1]), pk(z[0][2], z[0][3]), pk(z[1][0], z[1][1]), pk(z[1][2], z[1][3])};
      *(v4u*)(dst + cp * 2 + 64) = v4u{pk(z[2][0], z[2][1]), pk(z[2][2], z[2][3]), pk(z[3][0], z[3][1]), pk(z[3][2], z[3][3])};
      *(v2u*)(dst + cs * 2) = v2u{pk(z[4][0], z[4][1]), pk(z[4][2], z[4][3])};
      TS(14);
    }
  } else if (w < 6) {
    const int lw = w - 4;
    int vt[5];
#pragma unroll
    for (int i = 0; i < 5; i++) { const int j = (lw * 5 + i) >> 1; vt[i] = 5 * nt + (odd ? (j + 1) % 5 : j); }
    unsigned tag;
    v4u sg[10][5];
    int vd[5];
#pragma unroll
    for (int i = 0; i < 5; i++) {
      const int row = (lw * 5 + i) * 8 + (lane >> 3), j = row >> 4, T = 5 * nt + (odd ? (j + 1) % 5 : j), n = row & 15;
      vd[i] = (piece_col(T, n >> 2, j == 4) + (n & 3)) * 1280 + ((lane & 7) ^ ((row >> 1) & 7)) * 16;
    }
    auto e2 = [&](int s0, int s1) {
#pragma unroll 1
      for (int s = s0; s < s1; s++) {
        const rsrc_t r = mkrsrc(s < 10 ? p.dr : p.di - 1280, D_ * D_ * 2 + (s < 10 ? 0 : 1280));
        ldsp d = smem + ((s + 13) & 15) * 10240 + lw * 5120;
        const int q = s + rot10;
#pragma unroll
        for (int i = 0; i < 5; i++) dma<0>(r, d + i * 1024, vd[i], (q < (s < 10 ? 10 : 20) ? q : q - 10) * 128);
      }
    };
    {
      const rsrc_t ra = mkrsrc((hf ? p.ci : p.cr) + (size_t)tg * 128 * 4096, 128 * 4096);
      int va[8], vb[5];
#pragma unroll
      for (int i = 0; i < 8; i++) { const int row = (lw * 8 + i) * 8 + (lane >> 3); va[i] = row * 4096 + ((lane & 7) ^ ((row >> 1) & 7)) * 16; }
#pragma unroll
      for (int i = 0; i < 5; i++) vb[i] = OFF_LNF + vt[i] * 65536 + ((lw + i) & 1) * 1024 + lane * 16;
      auto l1 = [&](int s, int so) {
#pragma unroll
        for (int i = 0; i < 5; i++) dma<SC1>(rw, smem + so + (16 + lw * 5 + i) * 1024, vb[i],
          ((s + 4 * nt) & 31) * 2048
          );
      };
      auto e1 = [&](int s, int so, bool wa) {
        if (wa) {
#pragma unroll
          for (int i = 0; i < 8; i++) dma<0>(ra, smem + so + (lw * 8 + i) * 1024, va[i], ((s + 4 * nt) & 31) * 128);
        }
        l1(s, so);
      };
      bar();
#pragma unroll
      for (int s = 0; s < NPRE; s++)
#pragma unroll
        for (int i = 0; i < 8; i++) { dma<0>(ra, smem + (s + 3) % 5 * L1S + (lw * 8 + i) * 1024, va[i], (s + 4 * nt) * 128); if (s * 8 + i >= CTH - 1) waitv<CTH - 1>(); }
      if (HV && lw == 0) *(volatile LDS unsigned*)(smem + L0C + 12) = 1;
      lds_wait(smem + L0C, 4);
      tag = __builtin_amdgcn_readfirstlane(*(volatile LDS unsigned*)(smem + L0C + 16));
      TS(16);
      polln<0xffffffffull>(rw, lane < 32 ? OFF_F0 + (32 * nt + lane) * FSTR : OOB, tag);
      TS(17);
      constexpr int N2 = NPRE > 2 ? 5 : 13, N3 = NPRE > 3 ? 5 : 13;
      l1(0, 3 * L1S);
      v4u fp = ld16_async(p.ws, OFF_F0P + lane * 16);
      l1(1, 4 * L1S); e1(2, 0, NPRE < 3);
      waitv<6 + N2>(); bar();
      e1(3, L1S, NPRE < 4);
      waitv<N2 + N3>();
      asm volatile("" : "+v"(fp));
      if (lw == 0) {
        if (!eq4(fp, tag)) poll256(rw, OFF_F0P + lane * 16, tag);
        if (b == 0 && lane == 0) st4(rw, OFF_E, tag);
      }
      bar();
      e1(4, 2 * L1S, true); waitv<N3 + 13>(); bar();
      int so = 3 * L1S;
#pragma unroll 1
      for (int s = 5; s < 32; s++) {
        e1(s, so, true); so += L1S; if (so == 5 * L1S) so = 0;
        waitv<26>(); bar();
      }
      e2(0, 5); waitv<38>(); bar();
      e2(5, TD < 8 ? TD : 8); waitv<5 * (TD < 8 ? TD : 8)>(); bar();
      e2(8, TD);
      if (MT1 == 4) { bar(); bar(); }
      bar();
      __builtin_amdgcn_s_setprio(LP);
      e2(TD, 16);
      sbar();
#pragma unroll
      for (int k = 0; k < NS2; k++)
#pragma unroll
        for (int i = 0; i < 5; i++) sg[k][i] = __builtin_amdgcn_raw_buffer_load_b128(mkrsrc(p.di, D_ * D_ * 2), vd[i], (6 + k + rot10) % 10 * 128, 0);
      sbar();
      TS(59); if (!EPUB) bar();
    }
    auto put = [&](int k0, int k1, int slot) {
#pragma unroll
      for (int k = k0; k < k1; k++)
#pragma unroll
        for (int i = 0; i < 5; i++) *(LDS v4u*)(smem + (slot + k - k0) % 16 * 10240 + lw * 5120 + i * 1024 + lane * 16) = sg[k][i];
      waitl0();
    };
    int vb3[7];
#pragma unroll
    for (int i = 0; i < 7; i++) { const int f = lw * 7 + i, j = f >> 1, T = j < 6 ? T0 + odd3 + j : Ts;
      vb3[i] = (j < 6 || seven) ? OFF_W1F + T * 20480 + (f & 1) * 1024 + lane * 16 : OOB; }
    auto e3 = [&](int s0, int s1) {
#pragma unroll 1
      for (int s = s0; s < s1; s++) {
        ldsp d = smem + (10 + 14 * s + lw * 7) * K1;
#pragma unroll
        for (int i = 0; i < 7; i++) dma<SC1>(rw, d + i * 1024, vb3[i], (s + rot10 < 10 ? s + rot10 : s + rot10 - 10) * 2048);
      }
    };
    const int fl = OFF_FG + (lane >> 2) * FSTR + (lane & 3) * 4, i2 = lw * 64 + lane;
    {
      if (lw == 0) polln<~0ull>(rw, fl + tg * 16 * FSTR, tag); else waitv<0>();
      TS(18);
      bar();
      v4u fp = ld16_async(p.ws, OFF_FWP + lane * 16);
      bar();
      if (NS2) put(0, 4, 13); else e2(16, 20);
      waitv<0>();
      asm volatile("" : "+v"(fp));
      if (lw == 0 && !eq4(fp, tag)) poll256(rw, OFF_FWP + lane * 16, tag);
      bar();
      e3(0, TW);
      const int j = i2 >> 4, T = j < 6 ? T0 + odd3 + j : Ts;
      const bool on = i2 < (seven ? 112 : 96);
      const int vc = on ? OFF_CS + (T * 16 + (i2 & 15)) * 4 : OOB, vbias = on ? (piece_col(T, (i2 & 15) >> 2, split1728(T)) + (i2 & 3)) * 2 : OOB;
      unsigned cpart[20], bb;
      bar();
      ld2_async(bb, srd(p.b1, F_ * 2), vbias);
      sfor<0, 20>([&](auto k) { ld4_async<k.v * F_ * 4>(cpart[k.v], srd(p.ws, WS_BYTES), vc); });
      bar();
      e3(TW, 10);
      waitv<7 * (10 - TW)>();
      float cq[4] = {0, 0, 0, 0};
#pragma unroll
      for (int k = 0; k < 20; k++) { asm volatile("" : "+v"(cpart[k])); cq[k & 3] += asf(cpart[k]); }
      asm volatile("" : "+v"(bb));
      const float csum = (cq[0] + cq[1]) + (cq[2] + cq[3]);
      const unsigned c1 = pk(csum, csum), c2 = pk(csum - lo(c1), csum - lo(c1)), c3 = pk(0, csum - lo(c1) - lo(c2));
      const v4u fb = {(c1 & 0xffff) | (c2 & 0xffff0000u), (c1 & 0xffff) | c3, (c2 & 0xffff) | (c1 & 0xffff0000u), bb * 0x10001u};
      asm volatile("ds_write_b128 %0, %1\n ds_write_b128 %0, %2 offset:4608" ::"v"((unsigned)(size_t)(smem + L3X + i2 * 16)), "v"(fb), "v"(v4u{0, 0, 0, 0}) : "memory");
      TS(60); if (!EPUB) bar();
    }
    int vb4[5];
#pragma unroll
    for (int i = 0; i < 5; i++) vb4[i] = OFF_W2F + vt[i] * 55296 + ((lw + i) & 1) * 1024 + lane * 16;
    auto e4 = [&](int s0, int s1) {
#pragma unroll 1
      for (int s = s0; s < s1; s++) {
        ldsp d = smem + (s >= 15 ? s - 15 : s) * 10240 + lw * 5120;
        const int u = s + RB, q = u + rot9, e = u < 9 ? 9 : u < 18 ? 18 : 27;
#pragma unroll
        for (int i = 0; i < 5; i++) dma<SC1>(rw, d + i * 1024, vb4[i], (q < e ? q : q - 9) * 2048);
      }
    };
    {
      if (lw == 0) polln<~0ull>(rw, fl + (256 + tg3 * 16) * FSTR, tag); else waitv<0>();
      TS(19);
      bar();
      v2u sv[8];
#pragma unroll
      for (int k = 0; k < 8; k++) sv[k] = ld8(rw, OFF_ST + (tg3 * 128 + i2) * 8, k * 16384);
      if (LP1) __builtin_amdgcn_s_setprio(LP1);
#pragma unroll
      for (int k = 0; k < 8; k++) asm volatile("" : "+v"(sv[k]));
      const float m = (((asf(sv[0][0]) + asf(sv[1][0])) + (asf(sv[2][0]) + asf(sv[3][0]))) + ((asf(sv[4][0]) + asf(sv[5][0])) + (asf(sv[6][0]) + asf(sv[7][0])))) * 0.125f;
      float q[2] = {0, 0}, dq[2] = {0, 0};
#pragma unroll
      for (int k = 0; k < 8; k++) { const float d = asf(sv[k][0]) - m; dq[k & 1] += d * d; q[k & 1] += asf(sv[k][1]); }
      const float var = (q[0] + q[1] + 80.f * (dq[0] + dq[1])) * (1.f / 640.f) + 1e-5f, rs = __builtin_amdgcn_rsqf(var), sd = var * rs;
      const unsigned a1 = pk(-m, -m), as1 = pk(-m - lo(a1), sd), as2 = pk(-m - lo(a1) - lo(as1), sd - hi(as1));
      const v4u fa = {a1, (as1 & 0xffff) | (a1 & 0xffff0000u), (as1 & 0xffff) | as2 << 16, as1 >> 16 | (as2 & 0xffff0000u)};
      asm volatile("ds_write_b128 %0, %1 offset:2048\n ds_write_b32 %2, %3\n s_waitcnt lgkmcnt(0)"
                   ::"v"((unsigned)(size_t)(smem + L3X + i2 * 16)), "v"(fa), "v"((unsigned)(size_t)(smem + L3X + 4096 + i2 * 4)), "v"(rs) : "memory");
      if (LP1) __builtin_amdgcn_s_setprio(0);
      bar();
      e4(0, TX1);
      bar();
      e4(TX1, TX2);
      bar();
      e4(TX2, 15);
      sbar();
#pragma unroll
      for (int k = 0; k < NS4; k++) {
        const int u = 15 + k + RB, q = u + rot9, so = (q < (u < 18 ? 18 : 27) ? q : q - 9) * 2048;
#pragma unroll
        for (int i = 0; i < 5; i++) sg[k][i] = __builtin_amdgcn_raw_buffer_load_b128(rw, vb4[i], so, SC1);
      }
      sbar();
      TS(61); if (!EPUB) bar();
    }
    {
      if (lw == 0) polln<~0ull>(rw, fl + (512 + tg * 16) * FSTR, tag); else waitv<0>();
      TS(20);
      bar();
      bar();
      if (NS4) put(0, 4, 0); else e4(15, 19);
      bar();
      if (NS4 >= 8) {
        put(4, 8, 4); bar();
        if (RB < 4) { put(8, NS4, 8); e4(15 + NS4, 27 - RB); waitv<0>(); bar(); }
      } else {
        e4(19, 23); waitv<20>(); bar();
        e4(23, 27 - RB); waitv<5 * (4 - RB)>(); bar();
        if (RB < 4) { waitv<0>(); bar(); }
      }
    }
    if (w == 4) { TSOUT(16, 5); TSOUT(59, 3); }
  } else {
    const int hw = w - 6, nu = hw == HV && b < 56 ? 3 : 2;
    if (hw == 0) {
      unsigned long long pc; unsigned back;
      asm volatile("s_getpc_b64 %0\n.Lfnet_pc:\n s_mov_b32 %1, .Lfnet_pc-_Z6k_fnet6Params" : "=s"(pc), "=s"(back));
      dma<0>(mkrsrc((const void*)(pc - back), CODELEN), smem + L0D, (b >> 3) * 1024 + lane * 16, 0);
    }
    auto touch = [&] {
#pragma unroll
    for (int k = 0; k < 4; k++) {
      const int q = b + 256 * (hw * 3 + k);
      const rsrc_t r = hw || k < 3 ? mkrsrc(q < 800 ? p.dr + q * 1024 : p.di + (q - 800) * 1024, q < 1600 ? 1024 : 0)
                                   : mkrsrc(b & 32 ? p.b2 : p.b1, b & 32 ? D_ * 2 : F_ * 2);
      dma<0>(r, smem + L0D, lane * 16 + (hw || k < 3 ? 0 : (b >> 3 & 3) * 1024), 0);
    }
    };
    bar();
    if (HV) lds_wait(smem + L0C + 12, 1);
#pragma unroll 1
    for (int n = 0; n < nu; n++) {
      const int i = n < 2 ? hw * 2 + n : 4, u = b + 256 * i;
      const bool isw1 = u < 540;
      const int uu = isw1 ? u : u - 540, kb = isw1 ? uu / 27 : uu / 10, cu = isw1 ? uu % 27 : uu % 10, ld = isw1 ? F_ * 2 : D_ * 2;
      const rsrc_t r = mkrsrc(isw1 ? p.w1 : p.w2, D_ * F_ * 2);
#pragma unroll
      for (int k = 0; k < 4; k++) dma<0>(r, smem + L0W + i * 4096 + k * 1024, (kb * 32 + k * 8 + (lane >> 3)) * ld + cu * 128 + (lane & 7) * 16, 0);
    }
    if (HV) {
      touch();
#pragma unroll 1
      for (int n = 0; n <= HS0; n++) bar();
    }
    waitv<0>();
    TS(22);
    if (!HV) lds_wait(smem + L0C + 4, 4);
    const int qq = (lane & 15) >> 2, pp = lane & 3;
#pragma unroll 1
    for (int n = 0; n < nu; n++) {
      const int i = n < 2 ? hw * 2 + n : 4, u = b + 256 * i;
      const bool isw1 = u < 540;
      const int uu = isw1 ? u : u - 540, kb = isw1 ? uu / 27 : uu / 10, cu = isw1 ? uu % 27 : uu % 10;
      unsigned a[4];
#pragma unroll
      for (int t = 0; t < 4; t++) { const int T = cu * 4 + t;
        a[t] = (unsigned)(size_t)(smem + L0W + i * 4096 + (g * 8 + qq) * 128 + (piece_col(T, pp, isw1 ? split1728(T) : split640(T)) - cu * 64) * 2); }
      v2u v[8];
      asm volatile("ds_read_b64_tr_b16 %0, %8\n ds_read_b64_tr_b16 %1, %8 offset:512\n ds_read_b64_tr_b16 %2, %9\n ds_read_b64_tr_b16 %3, %9 offset:512\n"
                   "ds_read_b64_tr_b16 %4, %10\n ds_read_b64_tr_b16 %5, %10 offset:512\n ds_read_b64_tr_b16 %6, %11\n ds_read_b64_tr_b16 %7, %11 offset:512\n s_waitcnt lgkmcnt(0)"
                   : "=&v"(v[0]), "=&v"(v[1]), "=&v"(v[2]), "=&v"(v[3]), "=&v"(v[4]), "=&v"(v[5]), "=&v"(v[6]), "=&v"(v[7]) : "v"(a[0]), "v"(a[1]), "v"(a[2]), "v"(a[3]) : "memory");
      const int dst = (isw1 ? OFF_W1F + cu * 4 * 20480 : OFF_W2F + cu * 4 * 55296) + kb * 1024 + lane * 16;
#pragma unroll
      for (int t = 0; t < 4; t++) st16(rw, dst + t * (isw1 ? 20480 : 55296), v4u{v[2 * t][0], v[2 * t][1], v[2 * t + 1][0], v[2 * t + 1][1]});
      if (isw1) {
        float cs = 0;
#pragma unroll
        for (int t = 0; t < 4; t++) {
          const v2u v0 = v[2 * t], v1 = v[2 * t + 1];
          const float c1 = xsum32(xsum16((lo(v0[0]) + hi(v0[0])) + (lo(v0[1]) + hi(v0[1])) + (lo(v1[0]) + hi(v1[0])) + (lo(v1[1]) + hi(v1[1]))));
          cs = g == t ? c1 : cs;
        }
        st4(rw, OFF_CS + (kb * F_ + cu * 64 + lane) * 4, asu(cs));
      }
#pragma unroll 1
      for (int k = 0; k < HV * HSD; k++) bar();
    }
    TS(23);
    if (!HV) touch();
    if (HV) { bar(); bar(); waitv<0>(); } else waitv<4>();
    const unsigned tag = *(volatile LDS unsigned*)(smem + L0C + 16);
    unsigned old = 0;
    if (lane == 0) old = lds_add(smem + L0C + 8);
    if (__builtin_amdgcn_readfirstlane(old) == 1 && lane == 0) st4(rw, OFF_FWP + b * 4, tag);
    TS(21);
    if (!HV) { bar(); bar(); bar(); }
    waitv<0>();
    if (w == 6) TSOUT(21, 3);
  }
#endif
}

extern "C" void kernel_launch(void* const* d_in, const int* in_sizes, int n_in, void* d_out, int out_size, void* d_ws, size_t ws_size,
                              hipStream_t stream) {
  Params p;
  p.x = (const char*)d_in[0]; p.cr = (const char*)d_in[1]; p.ci = (const char*)d_in[2]; p.dr = (const char*)d_in[3]; p.di = (const char*)d_in[4];
  p.w1 = (const char*)d_in[6]; p.b1 = (const char*)d_in[7]; p.w2 = (const char*)d_in[8]; p.b2 = (const char*)d_in[9];
  p.out = (char*)d_out; p.ws = (char*)d_ws;
  (void)hipFuncSetAttribute((const void*)k_fnet, hipFuncAttributeMaxDynamicSharedMemorySize, LDS_BYTES);
  k_fnet<<<256, 512, LDS_BYTES, stream>>>(p);
}
```

```cpp
#include <hip/hip_runtime.h>
#include <hip/hip_bf16.h>
#include <stdint.h>

#define CTH 8
#define MT1 2
#define NPRE 4
#define TD 10
#define TW 4
#define TX1 4
#define TX2 9
#define TX3 12
#define US4 17
#define PD1 130
#define PD2 75
#define PD3 130
#define PSL 4
#define RBE 0
#define KB1 4
#define K2 8
#define KB3 NS4
#define FW4 0
#define APOLL 1
#define AP0 1
#define HM 0
#define ST 0
#define UD 5
#define HK3 14
#define RB 3
#define ROT 0
#define NS2 4
#define NS4 4
#define US2 16
#define US3 13
#define LP 0
#define LP1 3
#define SP 1
#define SP1 0
#define R1 1
#define HV 0
#define HW 0
#define PI 32
#define HS0 3
#define HSD 2
#define REMAP3 1
#define CODELEN 32768

#define LDS __attribute__((address_space(3)))
typedef LDS char* ldsp;
typedef short v4s __attribute__((ext_vector_type(4)));
typedef short v8s __attribute__((ext_vector_type(8)));
typedef __bf16 v8b __attribute__((ext_vector_type(8)));
typedef __bf16 v2b __attribute__((ext_vector_type(2)));
typedef float v4f __attribute__((ext_vector_type(4)));
typedef float v2f __attribute__((ext_vector_type(2)));
typedef unsigned v4u __attribute__((ext_vector_type(4)));
typedef unsigned v2u __attribute__((ext_vector_type(2)));

constexpr int S_ = 2048, D_ = 640, F_ = 1728;
constexpr int OFF_E   = 0;
constexpr int FSTR    = 128;
constexpr int OFF_F0  = 4096;
constexpr int OFF_F0P = 1024;
constexpr int OFF_FWP = 2048;
constexpr int OFF_LNF = 256 * 1024;
constexpr int OFF_PQF = OFF_LNF + 40 * 64 * 1024;
constexpr int OFF_HF  = OFF_PQF + 128 * 40 * 1024;
constexpr int OFF_HH  = OFF_HF + 128 * 20 * 1024;
constexpr int OFF_W1F = OFF_HH + 128 * 54 * 1024;
constexpr int OFF_W2F = OFF_W1F + 108 * 20 * 1024;
constexpr int OFF_CS  = OFF_W2F + 40 * 54 * 1024;
constexpr int OFF_ST  = OFF_CS + 20 * 1728 * 4;
constexpr int OFF_FG  = OFF_ST + 8 * 2048 * 8;
constexpr int FM = FW4 ? 4 * FSTR : FSTR, FWV = FW4 ? FSTR : 4;
constexpr int WS_BYTES = OFF_FG + 768 * FM;
constexpr int OOB = 0x40000000;
constexpr int LDS_BYTES = 160 * 1024;

struct Params {
  const char* x; const char* cr; const char* ci; const char* dr; const char* di;
  const char* w1; const char* b1; const char* w2; const char* b2;
  char* out; char* ws;
};

#define TS(i) do {} while (0)
#define TSOUT(a, n)
#define PC(k)

#if defined(__HIP_DEVICE_COMPILE__)
typedef __amdgpu_buffer_rsrc_t rsrc_t;
constexpr int SC1 = 16;
__device__ __forceinline__ rsrc_t mkrsrc(const void* p, unsigned bytes) { return __builtin_amdgcn_make_buffer_rsrc((void*)p, 0, bytes, 0x00020000); }
template <int AUX> __device__ __forceinline__ void dma(rsrc_t r, ldsp dst, int voff, int soff) {
  __builtin_amdgcn_raw_ptr_buffer_load_lds(r, (LDS void*)dst, 16, voff, soff, 0, AUX);
}
__device__ __forceinline__ void st16(rsrc_t r, int off, v4u v) { __builtin_amdgcn_raw_buffer_store_b128(v, r, off, 0, SC1); }
__device__ __forceinline__ void st8(rsrc_t r, int off, v2u v) { __builtin_amdgcn_raw_buffer_store_b64(v, r, off, 0, SC1); }
__device__ __forceinline__ void st4(rsrc_t r, int off, unsigned v) { __builtin_amdgcn_raw_buffer_store_b32(v, r, off, 0, SC1); }
__device__ __forceinline__ v4u ld16(rsrc_t r, int off) { return __builtin_amdgcn_raw_buffer_load_b128(r, off, 0, SC1); }
__device__ __forceinline__ v2u ld8(rsrc_t r, int off, int soff) { return __builtin_amdgcn_raw_buffer_load_b64(r, off, soff, SC1); }
__device__ __forceinline__ unsigned ld4(rsrc_t r, int off, int soff) { return __builtin_amdgcn_raw_buffer_load_b32(r, off, soff, SC1); }
template <int N> __device__ __forceinline__ void waitv() { asm volatile("s_waitcnt vmcnt(%0)" ::"n"(N) : "memory"); }
__device__ __forceinline__ void bar() { asm volatile("" ::: "memory"); __builtin_amdgcn_s_barrier(); asm volatile("" ::: "memory"); }
__device__ __forceinline__ void waitl0() { __builtin_amdgcn_sched_barrier(0); __builtin_amdgcn_s_waitcnt(0xC07F); __builtin_amdgcn_sched_barrier(0); }
__device__ __forceinline__ v4f mfma(v8s a, v8s b, v4f c) {
  return __builtin_amdgcn_mfma_f32_16x16x32_bf16(__builtin_bit_cast(v8b, a), __builtin_bit_cast(v8b, b), c, 0, 0, 0);
}
__device__ __forceinline__ unsigned pk(float a, float b) { v2b r = {(__bf16)a, (__bf16)b}; return __builtin_bit_cast(unsigned, r); }
__device__ __forceinline__ float asf(unsigned u) { return __builtin_bit_cast(float, u); }
__device__ __forceinline__ unsigned asu(float f) { return __builtin_bit_cast(unsigned, f); }
__device__ __forceinline__ float lo(unsigned u) { return asf(u << 16); }
__device__ __forceinline__ float hi(unsigned u) { return asf(u & 0xffff0000u); }
template <int CTRL> __device__ __forceinline__ float dppadd(float v) { return v + asf(__builtin_amdgcn_update_dpp(0, asu(v), CTRL, 0xf, 0xf, true)); }
__device__ __forceinline__ float rowsum(float v) { return dppadd<0x121>(dppadd<0x122>(dppadd<0x124>(dppadd<0x128>(v)))); }
__device__ __forceinline__ float xsum16(float v) { const auto r = __builtin_amdgcn_permlane16_swap(asu(v), asu(v), false, false); return asf(r[0]) + asf(r[1]); }
__device__ __forceinline__ float xsum32(float v) { const auto r = __builtin_amdgcn_permlane32_swap(asu(v), asu(v), false, false); return asf(r[0]) + asf(r[1]); }
template <int NM, int ND> __device__ __forceinline__ void ilv() {
#pragma unroll
  for (int i = 0; i < NM; i++) { __builtin_amdgcn_sched_group_barrier(0x8, 1, 0); if (SP1 ? i % 3 != 2 : i < ND) __builtin_amdgcn_sched_group_barrier(0x100, 1, 0); }
}
__device__ __forceinline__ int piece_col(int T, int p, bool split) { return 16 * T + 4 * p + ((4 * p - 12 * (T & 1)) & ((int)split - 1)); }
__device__ __forceinline__ bool split640(int T) { return (T >> 1) % 5 == 2; }
__device__ __forceinline__ bool split1728(int T) { return (T >> 1) % 7 == 3 && T < 84; }
__device__ __forceinline__ unsigned lds_add(ldsp a) { return __hip_atomic_fetch_add((LDS unsigned*)a, 1u, __ATOMIC_RELAXED, __HIP_MEMORY_SCOPE_WORKGROUP); }
__device__ __forceinline__ void lds_wait(ldsp a, unsigned v) { while (*(volatile LDS unsigned*)a != v) __builtin_amdgcn_s_sleep(1); asm volatile("" ::: "memory"); }
constexpr int SPIN_CAP = 1 << 17;
__device__ __forceinline__ v4u ld16_async(const char* base, unsigned off) { v4u v; asm volatile("global_load_dwordx4 %0, %1, %2 sc1" : "=v"(v) : "v"(off), "s"(base) : "memory"); return v; }
template <int SO> __device__ __forceinline__ void ld4_async(unsigned& d, v4u r, int voff) {
  int t; asm volatile("s_mov_b32 %1, %4\n buffer_load_dword %0, %2, %3, %1 offen sc1" : "=v"(d), "=&s"(t) : "v"(voff), "s"(r), "n"(SO));
}
__device__ __forceinline__ void look_async(unsigned& d, v4u r, int voff) {
  if (APOLL) { d = 0; asm volatile("buffer_atomic_or %0, %1, %2, 0 offen sc0 sc1" : "+v"(d) : "v"(voff), "s"(r)); }
  else asm volatile("buffer_load_dword %0, %1, %2, 0 offen sc1" : "=v"(d) : "v"(voff), "s"(r));
}
__device__ __forceinline__ void ld2_async(unsigned& d, v4u r, int voff) { asm volatile("buffer_load_ushort %0, %1, %2, 0 offen" : "=v"(d) : "v"(voff), "s"(r)); }
__device__ __forceinline__ v4u srd(const void* p, unsigned bytes) { return v4u{(unsigned)(size_t)p, (unsigned)((size_t)p >> 32) & 0xffffu, bytes, 0x00020000u}; }
__device__ __forceinline__ void sbar() { __builtin_amdgcn_sched_barrier(0); }
__device__ __forceinline__ unsigned now() { return (unsigned)__builtin_amdgcn_s_memrealtime(); }
template <int D> __device__ __forceinline__ void until(unsigned t0) {
#pragma unroll 1
  for (int it = 0; it < 64 && now() - t0 < D; it++) __builtin_amdgcn_s_sleep(1);
}
__device__ __forceinline__ bool eq4(v4u v, unsigned tag) { return __builtin_amdgcn_ballot_w64(v[0] != tag || v[1] != tag || v[2] != tag || v[3] != tag) == 0; }
__device__ __forceinline__ void poll256(rsrc_t r, int off, unsigned tag) {
#pragma unroll 1
  for (int it = 0; it < SPIN_CAP; it++) { asm volatile("" ::: "memory"); if (eq4(ld16(r, off), tag)) break; __builtin_amdgcn_s_sleep(2); }
  asm volatile("" ::: "memory");
}
template <unsigned long long MASK> __device__ __forceinline__ int polln(rsrc_t r, int off, unsigned tag) {
  int it;
#pragma unroll 1
  for (it = 0; it < SPIN_CAP; it++) { asm volatile("" ::: "memory"); if ((__builtin_amdgcn_ballot_w64(ld4(r, off, 0) != tag) & MASK) == 0) break; __builtin_amdgcn_s_sleep(PSL); }
  asm volatile("" ::: "memory");
  return it;
}

template <unsigned long long MASK> __device__ __forceinline__ int pollg(v4u r, int off, unsigned tag) {
  int it;
#pragma unroll 1
  for (it = 0; it < SPIN_CAP; it++) {
    unsigned f; look_async(f, r, off); waitv<0>(); asm volatile("" : "+v"(f));
    if ((__builtin_amdgcn_ballot_w64(f != tag) & MASK) == 0) break;
    __builtin_amdgcn_s_sleep(PSL);
  }
  return it;
}
template <int V> struct IC { static constexpr int v = V; };
template <int A, int B, class F> __device__ __forceinline__ void sfor(F f) { if constexpr (A < B) { f(IC<A>{}); sfor<A + 1, B>(f); } }

constexpr int K1 = 1024, L1S = 26 * K1, L1R = 90 * K1, L3X = 150 * K1;
constexpr int RS = 1312;
constexpr int L0R = 52 * K1, L0W = 130 * K1, L0C = 150 * K1, L0D = 152 * K1;

template <int MT>
__device__ __forceinline__ void consume1(ldsp smem, int w, int lane, v4f (&acc)[MT][5]) {
  const int g = lane >> 4, r = lane & 15;
  const int kh = MT == 4 ? w >> 1 : 0, rb = MT == 4 ? (w & 1) * 4 : w * 2;
  const int a0 = (rb * 16 + r) * 128 + (((kh * 4 + g) ^ (r >> 1)) * 16), a1 = MT == 4 ? a0 : a0 ^ 64;
  const int b0 = 16 * K1 + kh * K1 + lane * 16, b1 = MT == 4 ? b0 : b0 + K1;
  v8s pa[MT], pb[5], qa[MT], qb[5];
#pragma unroll
  for (int i = 0; i < MT; i++) { qa[i] = v8s{0, 0, 0, 0, 0, 0, 0, 0};
#pragma unroll
    for (int j = 0; j < 5; j++) acc[i][j] = v4f{0, 0, 0, 0}; }
#pragma unroll
  for (int j = 0; j < 5; j++) qb[j] = v8s{0, 0, 0, 0, 0, 0, 0, 0};
#define RD(A, B, ao, bo) { B[0] = *(LDS v8s*)(smem + so + bo); _Pragma("unroll") for (int i = 0; i < MT; i++) A[i] = *(LDS v8s*)(smem + so + ao + i * 2048); \
                           _Pragma("unroll") for (int j = 1; j < 5; j++) B[j] = *(LDS v8s*)(smem + so + bo + j * 2048); }
#define MM(A, B) { _Pragma("unroll") for (int j = 0; j < 5; j++) _Pragma("unroll") for (int i = 0; i < MT; i++) acc[i][j] = mfma(B[j], A[i], acc[i][j]); }
#define NEXT { bar(); so += L1S; if (so == 5 * L1S) so = 0; }
  int so = 3 * L1S;
#pragma unroll 1
  for (int s = 0; s < 31; s++) {
    RD(pa, pb, a0, b0); MM(qa, qb); ilv<5 * MT, 5 + MT>(); sbar();
    RD(qa, qb, a1, b1); MM(pa, pb); ilv<5 * MT, 5 + MT>(); sbar();
    if (TD > 10 && s == 30) waitl0();
    NEXT
  }
  RD(pa, pb, a0, b0); MM(qa, qb); ilv<5 * MT, 5 + MT>(); sbar();
  if (MT == 4) NEXT
  RD(qa, qb, a1, b1); MM(pa, pb); ilv<5 * MT, 5 + MT>();
  if (MT == 4) { waitl0(); bar(); } else sbar();
  MM(qa, qb);
#undef RD
#undef MM
#undef NEXT
}

template <int NJ, int NU, bool PAIR, int BL, int NR, class FB, class FS, class FX, class FH, class FT>
__device__ __forceinline__ void consume(ldsp bl0, rsrc_t rw, int va0, int va1, ldsp mine, v4f (&acc)[2][NJ], const v8s (&rb)[NR + 1][NJ], const int (&so)[BL], FB boff, FS sync, FX extra, FH hook, FT stamp) {
  v8s a[UD][2], bq[2][NJ];
  int va[2] = {va0, va1};
  ldsp bl[3] = {bl0, bl0 + 65536, bl0 + 131072};
  asm volatile("" : "+v"(va[0]), "+v"(va[1]), "+v"(bl[0]), "+v"(bl[1]), "+v"(bl[2]));
  bar();
  stamp(-1);
#define LDA(u) _Pragma("unroll") for (int i = 0; i < 2; i++) a[(u) % UD][i] = __builtin_bit_cast(v8s, \
    __builtin_amdgcn_raw_buffer_load_b128(rw, va[i] + (PAIR ? (u) / BL * BL * 2048 : ((u) & 1) * 1024), so[(PAIR ? (u) : (u) >> 1) % BL], SC1));
#define LDB(u) _Pragma("unroll") for (int j = 0; j < NJ; j++) bq[(u) & 1][j] = *(LDS v8s*)(bl[(boff(u) + j * 2048) >> 16] + ((boff(u) + j * 2048) & 0xffff));
  LDA(0) sbar(); if (!NR) { LDB(0) } sbar();
#pragma unroll
  for (int u = 1; u < UD - 1; u++) { LDA(u) sbar(); }
  extra();
  sbar();
  sfor<1, NU + 1>([&](auto uc) {
    constexpr int u = uc.v;
    if (u < NU) { if (sync(u)) bar(); if (u >= NR) { LDB(u) } }
    const bool ld = u - 2 + UD < NU;
    if (ld) { LDA(u - 2 + UD) }
    hook(u);
    if constexpr (PAIR && u == NU) {
#pragma unroll
      for (int j = 0; j < NJ; j++) acc[1][j] = mfma(bq[(u - 1) & 1][j], a[(u - 1) % UD][1], acc[1][j]);
      sbar();
#pragma unroll
      for (int j = 0; j < NJ; j++) {
        acc[0][j] = mfma(bq[(u - 1) & 1][j], a[(u - 1) % UD][0], acc[0][j]);
        *(LDS v4f*)(mine + j * 1024) = acc[1][j];
        sbar();
      }
      stamp(u);
      return;
    }
#pragma unroll
    for (int j = 0; j < NJ; j++) {
      v8s bf;
      if (u <= NR) bf = rb[u <= NR ? u - 1 : 0][j]; else bf = bq[(u - 1) & 1][j];
#pragma unroll
      for (int i = 0; i < 2; i++) acc[i][j] = mfma(bf, a[(u - 1) % UD][i], u == 1 && (i || !PAIR) ? v4f{0, 0, 0, 0} : acc[i][j]);
    }
#pragma unroll
    for (int k = 0; k < 2 * NJ; k++) {
      __builtin_amdgcn_sched_group_barrier(0x8, 1, 0);
      if (u < NU && u >= NR && (SP == 1 ? (k & 1) : SP == 2 ? !(k & 1) : k < NJ)) __builtin_amdgcn_sched_group_barrier(0x100, 1, 0);
      if (ld && (SP == 1 ? k == 2 || k == 6 : SP == 2 ? k == 3 || k == 7 : k >= NJ && k < NJ + 2)) __builtin_amdgcn_sched_group_barrier(0x20, 1, 0);
    }
    sbar();
    stamp(u);
  });
#undef LDA
#undef LDB
}

__device__ __forceinline__ void pair_add(ldsp other, v4f (&acc)[2][5], v4f (&o)[5]) {
#pragma unroll
  for (int j = 0; j < 5; j++) o[j] = *(LDS v4f*)(other + j * 1024);
  sbar();
#pragma unroll
  for (int j = 0; j < 5; j++) o[j] += acc[0][j];
}
#endif

__global__ __launch_bounds__(512) void k_fnet(Params p) {
#if defined(__HIP_DEVICE_COMPILE__)
  extern __shared__ __attribute__((aligned(16))) char smem_[];
  ldsp smem = (ldsp)smem_;
  const int lane = threadIdx.x & 63, w = __builtin_amdgcn_readfirstlane(threadIdx.x >> 6), b = blockIdx.x;
  const int tg = (b & 7) * 2 + (b >> 7), c = (b >> 3) & 15, hf = c >> 3, nt = c & 7, odd = nt & 1;
  const int tg3 = REMAP3 ? (b & 6) * 2 + (b >> 6) : tg, c3 = REMAP3 ? (b & 1) * 8 + nt : c;
  const bool seven = c3 < 12;
  const int odd3 = seven ? c3 & 1 : 0, T0 = seven ? 7 * c3 : 84 + 6 * (c3 - 12), Ts = odd3 ? T0 : T0 + 6;
  const int rot = ROT >= 2 ? nt + 2 * (b >> 6) : ROT ? nt : 0, rot10 = ROT == 3 ? 0 : rot % 10, rot9 = rot % 9;
  const int g = lane >> 4;
  const rsrc_t rw = mkrsrc(p.ws, WS_BYTES);
  if (__builtin_expect(w < 4, 1)) {
    TS(0);
    unsigned ep = ld4(rw, OFF_E, 0);
    const v2u* xr = (const v2u*)(p.x + (size_t)(b * 8 + w * 2 + (lane >> 5)) * 1280) + (lane & 31);
    v2u xv[5];
#pragma unroll
    for (int i = 0; i < 5; i++) xv[i] = xr[32 * i];
    if (w == 0 && lane < 4) *(LDS unsigned*)(smem + L0C + lane * 4) = 0;
    waitl0(); bar();
    asm volatile("" : "+v"(ep), "+v"(xv[0]), "+v"(xv[1]), "+v"(xv[2]), "+v"(xv[3]), "+v"(xv[4]));
    const unsigned tag = __builtin_amdgcn_readfirstlane(ep) + 1;
    {
      float v[20], s = 0;
#pragma unroll
      for (int i = 0; i < 5; i++) { v[4 * i] = lo(xv[i][0]); v[4 * i + 1] = hi(xv[i][0]); v[4 * i + 2] = lo(xv[i][1]); v[4 * i + 3] = hi(xv[i][1]); }
#pragma unroll
      for (int i = 0; i < 20; i++) s += v[i];
      const float mean = xsum16(rowsum(s)) * (1.f / 640.f);
      float q = 0;
#pragma unroll
      for (int i = 0; i < 20; i++) { v[i] -= mean; q += v[i] * v[i]; }
      const float rs = 1.0f / sqrtf(xsum16(rowsum(q)) * (1.f / 640.f) + 1e-5f);
#pragma unroll
      for (int i = 0; i < 5; i++)
        *(LDS v2u*)(smem + L0R + (w * 2 + (lane >> 5)) * RS + ((lane & 31) + 32 * i) * 8) = v2u{pk(v[4 * i] * rs, v[4 * i + 1] * rs), pk(v[4 * i + 2] * rs, v[4 * i + 3] * rs)};
      if (w == 0 && lane == 0) *(LDS unsigned*)(smem + L0C + 16) = tag;
      waitl0();
      if (lane == 0) lds_add(smem + L0C);
      lds_wait(smem + L0C, 4);
      TS(1);
      const int qq = (lane & 15) >> 2, pp = lane & 3;
#pragma unroll
      for (int i = 0; i < 3; i++) {
        const int T = 10 * w + 4 * i + (i < 2 ? g : g & 1);
        ldsp a = smem + L0R + qq * RS + piece_col(T, pp, split640(T)) * 2;
        const v4s v0 = __builtin_amdgcn_ds_read_tr16_b64_v4i16((LDS v4s*)a), v1 = __builtin_amdgcn_ds_read_tr16_b64_v4i16((LDS v4s*)(a + 4 * RS));
        st16(rw, i == 2 && g >= 2 ? OOB : OFF_LNF + T * 65536 + (b >> 2) * 1024 + (b & 3) * 256 + (lane & 15) * 16,
             __builtin_bit_cast(v4u, __builtin_shufflevector(v0, v1, 0, 1, 2, 3, 4, 5, 6, 7)));
      }
      if (HW && !HV && lane == 0) lds_add(smem + L0C + 12);
      waitv<0>();
      unsigned old = 0;
      if (lane == 0) old = lds_add(smem + L0C + 4);
      if (__builtin_amdgcn_readfirstlane(old) == 3 && lane < 2) st4(rw, lane ? OFF_F0P + b * 4 : OFF_F0 + b * FSTR, tag);
      TS(2);
    }
#define PUB(i) st4(rw, lane == 0 ? OFF_FG + (i) * FM + w * FWV : OOB, tag)
    const int kb0 = 5 * (nt >> 1), sp = ((odd * 2 + (g >> 1)) * 16 + (lane & 15)) * 16 + (g & 1) * 8;
    const int kh = w >> 1, tb0 = tg * 8 + hf * 4 + (w & 1) * 2, tb = tb0 + kh, t = tb * 16 + (lane & 15);
    const int cp = (kb0 + odd * 3) * 32 + g * 8, cs = (kb0 + 2) * 32 + odd * 16 + g * 4;
    sbar();
    const v4u x0 = *(const v4u*)(p.x + (size_t)t * 1280 + cp * 2), x1 = *(const v4u*)(p.x + (size_t)t * 1280 + cp * 2 + 64);
    const v2u x2 = *(const v2u*)(p.x + (size_t)t * 1280 + cs * 2);
    sbar();
    {
      v4f acc[MT1][5];
      bar(); TS(3);
      consume1<MT1>(smem, w, lane, acc);
      TS(4);
      v4f (&o)[2][5] = acc;
      const int tb1 = tg * 8 + w * 2;
      const float sg = hf ? -1.f : 1.f;
#pragma unroll
      for (int mi = 0; mi < 2; mi++) {
        const int dst = OFF_PQF + (tb1 + mi) * 40960 + (hf * 20 + kb0) * 1024;
#pragma unroll
        for (int u = 0; u < 2; u++) {
          const v4f a = o[mi][2 * u] * sg, c2 = o[mi][2 * u + 1] * sg;
          st16(rw, dst + (odd * 3 + u) * 1024 + lane * 16, v4u{pk(a[0], a[1]), pk(a[2], a[3]), pk(c2[0], c2[1]), pk(c2[2], c2[3])});
        }
        const v4f a = o[mi][4] * sg;
        st8(rw, dst + 2048 + sp, v2u{pk(a[0], a[1]), pk(a[2], a[3])});
      }
      TS(50); bar(); TS(51); waitv<0>(); TS(52);
      PUB(tg * 16 + c);
      TS(5);
    }
    v4u h0, h1, c0, c1; v2u h2, c2;
    int so10[10];
#pragma unroll
    for (int i = 0; i < 10; i++) { so10[i] = (i + rot10 < 10 ? i + rot10 : i + rot10 - 10) * 2048; asm volatile("" : "+s"(so10[i])); }
    const v8s none5[1][5] = {}, none7[1][7] = {};
#define UNP(a, b) v4f{lo(a), hi(a), lo(b), hi(b)}
    {
      v4f acc[2][5], z[5];
      const int r = lane & 15;
      acc[0][0] = UNP(x0[0], x0[1]); acc[0][1] = UNP(x0[2], x0[3]); acc[0][2] = UNP(x1[0], x1[1]); acc[0][3] = UNP(x1[2], x1[3]); acc[0][4] = UNP(x2[0], x2[1]);
      asm volatile("" : "+v"(acc[0][0]), "+v"(acc[0][1]), "+v"(acc[0][2]), "+v"(acc[0][3]), "+v"(acc[0][4]));
      consume<5, 20, true, 10, 0>(smem + r * 128 + ((kh * 4 + g) ^ (r >> 1)) * 16, rw, OFF_PQF + tb * 40960 + kh * 1024 + lane * 16,
        OFF_PQF + (tb ^ 1) * 40960 + kh * 1024 + lane * 16, smem + (100 + 5 * w) * K1 + lane * 16, acc, none5, so10,
        [](int u) { return (130 + 10 * u) % 160 * K1; }, [](int u) { return u == 5 || u == US2; }, [] {}, [](int) {}, [&](int u) { if (u < 0) TS(6); if (u == 1) TS(32); if (u == 4) TS(33); if (u == 9) TS(34); if (u == 12) TS(35); if (u == 15) TS(36); if (u == 18) TS(37); });
      TS(7);
      waitl0(); bar(); TS(63);
      pair_add(smem + (100 + 5 * (w ^ 2)) * K1 + lane * 16, acc, z);
      h0 = v4u{pk(z[0][0], z[0][1]), pk(z[0][2], z[0][3]), pk(z[1][0], z[1][1]), pk(z[1][2], z[1][3])};
      h1 = v4u{pk(z[2][0], z[2][1]), pk(z[2][2], z[2][3]), pk(z[3][0], z[3][1]), pk(z[3][2], z[3][3])};
      h2 = v2u{pk(z[4][0], z[4][1]), pk(z[4][2], z[4][3])};
      const int dst = OFF_HF + tb * 20480 + kb0 * 1024;
      st16(rw, dst + odd * 3072 + lane * 16, h0);
      st16(rw, dst + odd * 3072 + 1024 + lane * 16, h1);
      st8(rw, dst + 2048 + sp, h2);
      const v4f s4 = (z[0] + z[1]) + (z[2] + z[3]) + z[4];
      const float m = xsum32(xsum16((s4[0] + s4[1]) + (s4[2] + s4[3]))) * (1.f / 80.f);
      v4f q4 = {0, 0, 0, 0};
#pragma unroll
      for (int j = 0; j < 5; j++) { const v4f d = z[j] - m; q4 += d * d; }
      const float q = xsum32(xsum16((q4[0] + q4[1]) + (q4[2] + q4[3])));
      st8(rw, lane < 16 ? OFF_ST + nt * 16384 + t * 8 : OOB, v2u{asu(m), asu(q)});
      TS(53); bar(); TS(54); waitv<0>(); TS(55);
      PUB(256 + tg * 16 + c);
      TS(8);
    }
      v8s rb[RB + 1][5];
      int so9[9];
#pragma unroll
      for (int i = 0; i < 9; i++) { so9[i] = (i + rot9 < 9 ? i + rot9 : i + rot9 - 9) * 2048; asm volatile("" : "+s"(so9[i])); }
      auto ldrb = [&] {
      sbar();
#pragma unroll
      for (int u = 0; u < RB; u++)
#pragma unroll
        for (int j = 0; j < 5; j++)
          rb[u][j] = __builtin_bit_cast(v8s, __builtin_amdgcn_raw_buffer_load_b128(rw, OFF_W2F + kh * 1024 + lane * 16, (5 * nt + (odd ? (j + 1) % 5 : j)) * 55296 + so9[u], SC1));
      sbar();
      };
    {
      const int k3 = (T0 + odd3) >> 1;
      v4f acc[2][7];
      v8s ea[2], eb[7];
      float st[2];
      consume<7, 20, false, 10, 0>(smem + lane * 16, rw, OFF_HF + (tg3 * 8 + w * 2) * 20480 + lane * 16, OFF_HF + (tg3 * 8 + w * 2 + 1) * 20480 + lane * 16, smem, acc, none7, so10,
        [](int u) { return (10 + 14 * (u >> 1) + (u & 1)) * K1; }, [](int u) { return u == 7 || u == US3 || (TX3 > TX2 && u == US4); }, [&] {
          c0 = *(const v4u*)(p.b2 + cp * 2); c1 = *(const v4u*)(p.b2 + cp * 2 + 64); c2 = *(const v2u*)(p.b2 + cs * 2); },
        [&](int u) {
          if (u == HK3) {
#pragma unroll
            for (int i = 0; i < 2; i++) ea[i] = *(LDS v8s*)(smem + L3X + (g ? 4608 + lane * 16 : 2048 + (w * 32 + lane) * 16) + i * 256);
#pragma unroll
            for (int j = 0; j < 7; j++) eb[j] = *(LDS v8s*)(smem + L3X + (lane & 15) * 16 + j * 256);
          }
          if (u == HK3 + 1) {
#pragma unroll
            for (int j = 0; j < 7; j++)
#pragma unroll
              for (int i = 0; i < 2; i++) acc[i][j] = mfma(eb[j], ea[i], acc[i][j]);
            sbar();
          }
          if (u == 19) { st[0] = *(LDS float*)(smem + L3X + 4096 + (w * 32 + (lane & 15)) * 4); st[1] = *(LDS float*)(smem + L3X + 4160 + (w * 32 + (lane & 15)) * 4); } },
        [&](int u) { if (u < 0) TS(9); if (u == 1) TS(38); if (u == 4) TS(39); if (u == 8) TS(40); if (u == 12) TS(41); if (u == 15) TS(42); if (u == 18) TS(43); });
      TS(10);
#pragma unroll
      for (int mi = 0; mi < 2; mi++) {
        const int dst = OFF_HH + (tg3 * 8 + w * 2 + mi) * 55296;
        unsigned r[14];
#pragma unroll
        for (int j = 0; j < 7; j++) {
          v4f z = acc[mi][j] * st[mi];
          z = __builtin_elementwise_max(z, v4f{0, 0, 0, 0}); z *= z;
          r[2 * j] = pk(z[0], z[1]); r[2 * j + 1] = pk(z[2], z[3]);
          if (j & 1) st16(rw, dst + (k3 + (j >> 1)) * 1024 + lane * 16, v4u{r[2 * j - 2], r[2 * j - 1], r[2 * j], r[2 * j + 1]});
        }
        st8(rw, seven ? dst + (Ts >> 1) * 1024 + ((odd3 * 2 + (g >> 1)) * 16 + (lane & 15)) * 16 + (g & 1) * 8 : OOB, v2u{r[12], r[13]});
      }
      TS(56); bar(); TS(57);
      if (RBE) { ldrb(); waitv<5 * RB>(); } else waitv<0>();
      TS(58);
      PUB(512 + tg3 * 16 + c3);
      TS(11);
    }
    {
      v4f acc[2][5], z[5];
      if (!RBE) ldrb();
      acc[0][0] = UNP(h0[0], h0[1]) + UNP(c0[0], c0[1]); acc[0][1] = UNP(h0[2], h0[3]) + UNP(c0[2], c0[3]); acc[0][2] = UNP(h1[0], h1[1]) + UNP(c1[0], c1[1]);
      acc[0][3] = UNP(h1[2], h1[3]) + UNP(c1[2], c1[3]); acc[0][4] = UNP(h2[0], h2[1]) + UNP(c2[0], c2[1]);
      asm volatile("" : "+v"(acc[0][0]), "+v"(acc[0][1]), "+v"(acc[0][2]), "+v"(acc[0][3]), "+v"(acc[0][4]));
      consume<5, 27, true, 9, RB>(smem + kh * 1024 + lane * 16, rw, OFF_HH + tb * 55296 + kh * 1024 + lane * 16, OFF_HH + (tb ^ 1) * 55296 + kh * 1024 + lane * 16,
        smem + (120 + 5 * w) * K1 + lane * 16, acc, rb, so9,
        [](int u) { return (u + 15 - RB) % 15 * 10 * K1; }, [](int u) { return u == 5 + RB || u == 9 + RB || u == 15 + RB || (NS4 < 8 && u == 19 + RB) || u == 23 + RB; }, [] {}, [](int) {}, [&](int u) { if (u < 0) TS(12); if (u == 1) TS(44); if (u == 6) TS(45); if (u == 11) TS(46); if (u == 16) TS(47); if (u == 21) TS(48); if (u == 25) TS(49); });
      TS(13);
      waitl0(); bar();
      pair_add(smem + (120 + 5 * (w ^ 2)) * K1 + lane * 16, acc, z);
      char* dst = p.out + (size_t)t * 1280;
      *(v4u*)(dst + cp * 2) = v4u{pk(z[0][0], z[0][1]), pk(z[0][2], z[0][3]), pk(z[1][0], z[1][1]), pk(z[1][2], z[1][3])};
      *(v4u*)(dst + cp * 2 + 64) = v4u{pk(z[2][0], z[2][1]), pk(z[2][2], z[2][3]), pk(z[3][0], z[3][1]), pk(z[3][2], z[3][3])};
      *(v2u*)(dst + cs * 2) = v2u{pk(z[4][0], z[4][1]), pk(z[4][2], z[4][3])};
      TS(14);
    }
  } else if (w < 6) {
    const int lw = w - 4;
    int vt[5];
#pragma unroll
    for (int i = 0; i < 5; i++) { const int j = (lw * 5 + i) >> 1; vt[i] = 5 * nt + (odd ? (j + 1) % 5 : j); }
    unsigned tag, t0;
    v4u sg[10][5];
    int vd[5];
#pragma unroll
    for (int i = 0; i < 5; i++) {
      const int row = (lw * 5 + i) * 8 + (lane >> 3), j = row >> 4, T = 5 * nt + (odd ? (j + 1) % 5 : j), n = row & 15;
      vd[i] = (piece_col(T, n >> 2, j == 4) + (n & 3)) * 1280 + ((lane & 7) ^ ((row >> 1) & 7)) * 16;
    }
    auto e2 = [&](int s0, int s1) {
#pragma unroll 1
      for (int s = s0; s < s1; s++) {
        const rsrc_t r = mkrsrc(s < 10 ? p.dr : p.di - 1280, D_ * D_ * 2 + (s < 10 ? 0 : 1280));
        ldsp d = smem + ((s + 13) & 15) * 10240 + lw * 5120;
        const int q = s + rot10;
#pragma unroll
        for (int i = 0; i < 5; i++) dma<0>(r, d + i * 1024, vd[i], (q < (s < 10 ? 10 : 20) ? q : q - 10) * 128);
      }
    };
    auto stage2 = [&](int k0, int k1) {
      sbar();
#pragma unroll
      for (int k = k0; k < k1; k++)
#pragma unroll
        for (int i = 0; i < 5; i++) sg[k][i] = __builtin_amdgcn_raw_buffer_load_b128(mkrsrc(p.di, D_ * D_ * 2), vd[i], (6 + k + rot10) % 10 * 128, 0);
      sbar();
    };
    {
      const rsrc_t ra = mkrsrc((hf ? p.ci : p.cr) + (size_t)tg * 128 * 4096, 128 * 4096);
      int va[8], vb[5];
#pragma unroll
      for (int i = 0; i < 8; i++) { const int row = (lw * 8 + i) * 8 + (lane >> 3); va[i] = row * 4096 + ((lane & 7) ^ ((row >> 1) & 7)) * 16; }
#pragma unroll
      for (int i = 0; i < 5; i++) vb[i] = OFF_LNF + vt[i] * 65536 + ((lw + i) & 1) * 1024 + lane * 16;
      auto ks = [&](int s) { return ((s & ~3) + ((s + (R1 ? b >> 6 : 0)) & 3) + 4 * nt) & 31; };
      auto l1 = [&](int s, int so) {
#pragma unroll
        for (int i = 0; i < 5; i++) dma<SC1>(rw, smem + so + (16 + lw * 5 + i) * 1024, vb[i],
          ks(s) * 2048);
      };
      auto e1 = [&](int s, int so, bool wa) {
        if (wa) {
#pragma unroll
          for (int i = 0; i < 8; i++) dma<0>(ra, smem + so + (lw * 8 + i) * 1024, va[i], ks(s) * 128);
        }
        l1(s, so);
      };
      const int vf0 = lane < 32 ? OFF_F0 + (32 * nt + lane) * FSTR : OOB;
      unsigned f0;
      bar();
#pragma unroll
      for (int s = 0; s < NPRE; s++)
#pragma unroll
        for (int i = 0; i < 8; i++) {
          if (s * 8 + i == PI) look_async(f0, srd(p.ws, WS_BYTES), vf0);
          dma<0>(ra, smem + (s + 3) % 5 * L1S + (lw * 8 + i) * 1024, va[i], ks(s) * 128);
          if (s * 8 + i >= CTH - 1) waitv<CTH - 1>();
        }
      if (HV && lw == 0) *(volatile LDS unsigned*)(smem + L0C + 12) = 1;
      lds_wait(smem + L0C, 4);
      tag = __builtin_amdgcn_readfirstlane(*(volatile LDS unsigned*)(smem + L0C + 16));
      TS(16);
      if (PI < NPRE * 8) {
        waitv<NPRE * 8 - PI>();
        asm volatile("" : "+v"(f0));
      }
      if (PI >= NPRE * 8 || (unsigned)__builtin_amdgcn_ballot_w64(f0 != tag)) { if (AP0) pollg<0xffffffffull>(srd(p.ws, WS_BYTES), vf0, tag); else polln<0xffffffffull>(rw, vf0, tag); }
      TS(17);
      constexpr int N2 = NPRE > 2 ? 5 : 13, N3 = NPRE > 3 ? 5 : 13;
      l1(0, 3 * L1S);
      v4u fp = ld16_async(p.ws, OFF_F0P + lane * 16);
      l1(1, 4 * L1S); e1(2, 0, NPRE < 3);
      waitv<6 + N2>(); bar();
      e1(3, L1S, NPRE < 4);
      waitv<N2 + N3>();
      asm volatile("" : "+v"(fp));
      if (lw == 0) {
        if (!eq4(fp, tag)) poll256(rw, OFF_F0P + lane * 16, tag);
        if (b == 0 && lane == 0) st4(rw, OFF_E, tag);
      }
      bar();
      e1(4, 2 * L1S, true); waitv<N3 + 13>(); bar();
      int so = 3 * L1S;
#pragma unroll 1
      for (int s = 5; s < 32; s++) {
        e1(s, so, true); so += L1S; if (so == 5 * L1S) so = 0;
        waitv<26>(); bar();
      }
      e2(0, 5); waitv<38>(); bar();
      e2(5, TD < 8 ? TD : 8); waitv<5 * (TD < 8 ? TD : 8)>(); bar();
      e2(8, TD);
      if (MT1 == 4) { bar(); bar(); }
      bar();
      t0 = now();
      e2(TD, 16);
      stage2(0, NS2 - KB1);
      TS(59);
    }
    auto put = [&](int k0, int k1, int slot) {
#pragma unroll
      for (int k = k0; k < k1; k++)
#pragma unroll
        for (int i = 0; i < 5; i++) *(LDS v4u*)(smem + (slot + k - k0) % 16 * 10240 + lw * 5120 + i * 1024 + lane * 16) = sg[k][i];
      waitl0();
    };
    int vb3[7];
#pragma unroll
    for (int i = 0; i < 7; i++) { const int f = lw * 7 + i, j = f >> 1, T = j < 6 ? T0 + odd3 + j : Ts;
      vb3[i] = (j < 6 || seven) ? OFF_W1F + T * 20480 + (f & 1) * 1024 + lane * 16 : OOB; }
    auto e3 = [&](int s0, int s1) {
#pragma unroll 1
      for (int s = s0; s < s1; s++) {
        ldsp d = smem + (10 + 14 * s + lw * 7) * K1;
#pragma unroll
        for (int i = 0; i < 7; i++) dma<SC1>(rw, d + i * 1024, vb3[i], (s + rot10 < 10 ? s + rot10 : s + rot10 - 10) * 2048);
      }
    };
    const int fl = OFF_FG + (lane >> 2) * FM + (lane & 3) * FWV, i2 = lw * 64 + lane;
#define LOOK(D, N, grp, more, k) { unsigned f; until<D>(t0); look_async(f, srd(p.ws, WS_BYTES), lw ? OOB : fl + (grp) * 16 * FM); more; waitv<N>(); \
      asm volatile("" : "+v"(f)); if (lw == 0 && __builtin_amdgcn_ballot_w64(f != tag)) { PC(k) (1 + pollg<~0ull>(srd(p.ws, WS_BYTES), fl + (grp) * 16 * FM, tag)); } }
    {
      LOOK(PD1, 5 * KB1, tg, stage2(NS2 - KB1, NS2), 0)
      TS(18);
      bar();
      v4u fp = ld16_async(p.ws, OFF_FWP + lane * 16);
      bar();
      if (NS2) put(0, 4, 13); else e2(16, 20);
      waitv<0>();
      asm volatile("" : "+v"(fp));
      if (lw == 0 && !eq4(fp, tag)) poll256(rw, OFF_FWP + lane * 16, tag);
      bar();
      e3(0, TW);
      const int j = i2 >> 4, T = j < 6 ? T0 + odd3 + j : Ts;
      const bool on = i2 < (seven ? 112 : 96);
      const int vc = on ? OFF_CS + (T * 16 + (i2 & 15)) * 4 : OOB, vbias = on ? (piece_col(T, (i2 & 15) >> 2, split1728(T)) + (i2 & 3)) * 2 : OOB;
      unsigned cpart[20], bb;
      bar();
      ld2_async(bb, srd(p.b1, F_ * 2), vbias);
      sfor<0, 20>([&](auto k) { ld4_async<k.v * F_ * 4>(cpart[k.v], srd(p.ws, WS_BYTES), vc); });
      bar();
      t0 = now();
      e3(TW, K2);
      waitv<7 * (K2 - TW)>();
      float cq[4] = {0, 0, 0, 0};
#pragma unroll
      for (int k = 0; k < 20; k++) { asm volatile("" : "+v"(cpart[k])); cq[k & 3] += asf(cpart[k]); }
      asm volatile("" : "+v"(bb));
      const float csum = (cq[0] + cq[1]) + (cq[2] + cq[3]);
      const unsigned c1 = pk(csum, csum), c2 = pk(csum - lo(c1), csum - lo(c1)), c3 = pk(0, csum - lo(c1) - lo(c2));
      const v4u fb = {(c1 & 0xffff) | (c2 & 0xffff0000u), (c1 & 0xffff) | c3, (c2 & 0xffff) | (c1 & 0xffff0000u), bb * 0x10001u};
      asm volatile("ds_write_b128 %0, %1\n ds_write_b128 %0, %2 offset:4608" ::"v"((unsigned)(size_t)(smem + L3X + i2 * 16)), "v"(fb), "v"(v4u{0, 0, 0, 0}) : "memory");
      TS(60);
    }
    int vb4[5];
#pragma unroll
    for (int i = 0; i < 5; i++) vb4[i] = OFF_W2F + vt[i] * 55296 + ((lw + i) & 1) * 1024 + lane * 16;
    auto e4 = [&](int s0, int s1) {
#pragma unroll 1
      for (int s = s0; s < s1; s++) {
        ldsp d = smem + (s >= 15 ? s - 15 : s) * 10240 + lw * 5120;
        const int u = s + RB, q = u + rot9, e = u < 9 ? 9 : u < 18 ? 18 : 27;
#pragma unroll
        for (int i = 0; i < 5; i++) dma<SC1>(rw, d + i * 1024, vb4[i], (q < e ? q : q - 9) * 2048);
      }
    };
    auto stage4 = [&](int k0, int k1) {
      sbar();
#pragma unroll
      for (int k = k0; k < k1; k++) {
        const int u = 15 + k + RB, q = u + rot9, so = (q < (u < 18 ? 18 : 27) ? q : q - 9) * 2048;
#pragma unroll
        for (int i = 0; i < 5; i++) sg[k][i] = __builtin_amdgcn_raw_buffer_load_b128(rw, vb4[i], so, SC1);
      }
      sbar();
    };
    {
      LOOK(PD2, 7 * (10 - K2), 16 + tg3, e3(K2, 10), 8)
      TS(19);
      bar();
      v2u sv[8];
#pragma unroll
      for (int k = 0; k < 8; k++) sv[k] = ld8(rw, OFF_ST + (tg3 * 128 + i2) * 8, k * 16384);
      if (LP1) __builtin_amdgcn_s_setprio(LP1);
#pragma unroll
      for (int k = 0; k < 8; k++) asm volatile("" : "+v"(sv[k]));
      const float m = (((asf(sv[0][0]) + asf(sv[1][0])) + (asf(sv[2][0]) + asf(sv[3][0]))) + ((asf(sv[4][0]) + asf(sv[5][0])) + (asf(sv[6][0]) + asf(sv[7][0])))) * 0.125f;
      float q[2] = {0, 0}, dq[2] = {0, 0};
#pragma unroll
      for (int k = 0; k < 8; k++) { const float d = asf(sv[k][0]) - m; dq[k & 1] += d * d; q[k & 1] += asf(sv[k][1]); }
      const float var = (q[0] + q[1] + 80.f * (dq[0] + dq[1])) * (1.f / 640.f) + 1e-5f, rs = __builtin_amdgcn_rsqf(var), sd = var * rs;
      const unsigned a1 = pk(-m, -m), as1 = pk(-m - lo(a1), sd), as2 = pk(-m - lo(a1) - lo(as1), sd - hi(as1));
      const v4u fa = {a1, (as1 & 0xffff) | (a1 & 0xffff0000u), (as1 & 0xffff) | as2 << 16, as1 >> 16 | (as2 & 0xffff0000u)};
      asm volatile("ds_write_b128 %0, %1 offset:2048\n ds_write_b32 %2, %3\n s_waitcnt lgkmcnt(0)"
                   ::"v"((unsigned)(size_t)(smem + L3X + i2 * 16)), "v"(fa), "v"((unsigned)(size_t)(smem + L3X + 4096 + i2 * 4)), "v"(rs) : "memory");
      if (LP1) __builtin_amdgcn_s_setprio(0);
      bar();
      e4(0, TX1);
      bar();
      e4(TX1, TX2);
      bar();
      if (TX3 > TX2) { e4(TX2, TX3); bar(); }
      t0 = now();
      e4(TX3, 15);
      stage4(0, NS4 - KB3);
      TS(61);
    }
    {
      LOOK(PD3, 5 * KB3, 32 + tg, stage4(NS4 - KB3, NS4), 16)
      TS(20);
      bar();
      bar();
      if (NS4) put(0, 4, 0); else e4(15, 19);
      bar();
      if (NS4 >= 8) {
        put(4, 8, 4); bar();
        if (RB < 4) { put(8, NS4, 8); e4(15 + NS4, 27 - RB); waitv<0>(); bar(); }
      } else {
        e4(19, 23); waitv<20>(); bar();
        e4(23, 27 - RB); waitv<5 * (4 - RB)>(); bar();
        if (RB < 4) { waitv<0>(); bar(); }
      }
    }
    if (w == 4) { TSOUT(16, 5); TSOUT(59, 4); }
  } else {
    const int hw = w - 6, nu = hw == HV && b < 56 ? 3 : 2;
    if (hw == 0) {
      unsigned long long pc; unsigned back;
      asm volatile("s_getpc_b64 %0\n.Lfnet_pc:\n s_mov_b32 %1, .Lfnet_pc-_Z6k_fnet6Params" : "=s"(pc), "=s"(back));
      dma<0>(mkrsrc((const void*)(pc - back), CODELEN), smem + L0D, (b >> 3) * 1024 + lane * 16, 0);
    }
    auto touch = [&] {
#pragma unroll
    for (int k = 0; k < 4; k++) {
      const int q = b + 256 * (hw * 3 + k);
      const rsrc_t r = hw || k < 3 ? mkrsrc(q < 800 ? p.dr + q * 1024 : p.di + (q - 800) * 1024, q < 1600 ? 1024 : 0)
                                   : mkrsrc(b & 32 ? p.b2 : p.b1, b & 32 ? D_ * 2 : F_ * 2);
      dma<0>(r, smem + L0D, lane * 16 + (hw || k < 3 ? 0 : (b >> 3 & 3) * 1024), 0);
    }
    };
    bar();
    if (HV) lds_wait(smem + L0C + 12, 1); else if (HW) lds_wait(smem + L0C + 12, 4);
#pragma unroll 1
    for (int n = 0; n < nu; n++) {
      const int i = n < 2 ? hw * 2 + n : 4, u = b + 256 * i;
      const bool isw1 = u < 540;
      const int uu = isw1 ? u : u - 540, kb = isw1 ? uu / 27 : uu / 10, cu = isw1 ? uu % 27 : uu % 10, ld = isw1 ? F_ * 2 : D_ * 2;
      const rsrc_t r = mkrsrc(isw1 ? p.w1 : p.w2, D_ * F_ * 2);
#pragma unroll
      for (int k = 0; k < 4; k++) dma<0>(r, smem + L0W + i * 4096 + k * 1024, (kb * 32 + k * 8 + (lane >> 3)) * ld + cu * 128 + (lane & 7) * 16, 0);
    }
    if (HV) {
      touch();
#pragma unroll 1
      for (int n = 0; n <= HS0; n++) bar();
    }
    waitv<0>();
    TS(22);
    if (!HV) lds_wait(smem + L0C + 4, 4);
    const int qq = (lane & 15) >> 2, pp = lane & 3;
#pragma unroll 1
    for (int n = 0; n < nu; n++) {
      const int i = n < 2 ? hw * 2 + n : 4, u = b + 256 * i;
      const bool isw1 = u < 540;
      const int uu = isw1 ? u : u - 540, kb = isw1 ? uu / 27 : uu / 10, cu = isw1 ? uu % 27 : uu % 10;
      unsigned a[4];
#pragma unroll
      for (int t = 0; t < 4; t++) { const int T = cu * 4 + t;
        a[t] = (unsigned)(size_t)(smem + L0W + i * 4096 + (g * 8 + qq) * 128 + (piece_col(T, pp, isw1 ? split1728(T) : split640(T)) - cu * 64) * 2); }
      v2u v[8];
      asm volatile("ds_read_b64_tr_b16 %0, %8\n ds_read_b64_tr_b16 %1, %8 offset:512\n ds_read_b64_tr_b16 %2, %9\n ds_read_b64_tr_b16 %3, %9 offset:512\n"
                   "ds_read_b64_tr_b16 %4, %10\n ds_read_b64_tr_b16 %5, %10 offset:512\n ds_read_b64_tr_b16 %6, %11\n ds_read_b64_tr_b16 %7, %11 offset:512\n s_waitcnt lgkmcnt(0)"
                   : "=&v"(v[0]), "=&v"(v[1]), "=&v"(v[2]), "=&v"(v[3]), "=&v"(v[4]), "=&v"(v[5]), "=&v"(v[6]), "=&v"(v[7]) : "v"(a[0]), "v"(a[1]), "v"(a[2]), "v"(a[3]) : "memory");
      const int dst = (isw1 ? OFF_W1F + cu * 4 * 20480 : OFF_W2F + cu * 4 * 55296) + kb * 1024 + lane * 16;
#pragma unroll
      for (int t = 0; t < 4; t++) st16(rw, dst + t * (isw1 ? 20480 : 55296), v4u{v[2 * t][0], v[2 * t][1], v[2 * t + 1][0], v[2 * t + 1][1]});
      if (isw1) {
        float cs = 0;
#pragma unroll
        for (int t = 0; t < 4; t++) {
          const v2u v0 = v[2 * t], v1 = v[2 * t + 1];
          const float c1 = xsum32(xsum16((lo(v0[0]) + hi(v0[0])) + (lo(v0[1]) + hi(v0[1])) + (lo(v1[0]) + hi(v1[0])) + (lo(v1[1]) + hi(v1[1]))));
          cs = g == t ? c1 : cs;
        }
        st4(rw, OFF_CS + (kb * F_ + cu * 64 + lane) * 4, asu(cs));
      }
#pragma unroll 1
      for (int k = 0; k < HV * HSD; k++) bar();
    }
    TS(23);
    if (!HV && !ST) touch();
    if (HV) { bar(); bar(); waitv<0>(); }
    else if (HM) {
#pragma unroll 1
      for (int k = 0; k < HM; k++) bar();
      waitv<0>();
    } else waitv<ST ? 0 : 4>();
    const unsigned tag = *(volatile LDS unsigned*)(smem + L0C + 16);
    unsigned old = 0;
    if (lane == 0) old = lds_add(smem + L0C + 8);
    if (__builtin_amdgcn_readfirstlane(old) == 1 && lane == 0) st4(rw, OFF_FWP + b * 4, tag);
    TS(21);
    if (ST == 1) {
      const char* q = (hw ? p.di : p.dr) + b * 3200, * qb = (b & 1 ? p.b2 : p.b1) + (b & 1 ? (b >> 1) % 10 : (b >> 1) % 27) * 128;
      unsigned d, o;
      asm volatile("s_load_dword %0, %3, 0x0\n s_mov_b32 %1, 0\n"
                   ".Lfnet_touch:\n s_load_dword %0, %2, %1\n s_load_dword %0, %2, %1 offset:0x80\n s_load_dword %0, %2, %1 offset:0x100\n"
                   "s_barrier\n s_add_u32 %1, %1, 384\n s_cmp_lg_u32 %1, 3072\n s_cbranch_scc1 .Lfnet_touch\n"
                   "s_load_dword %0, %2, %1\n s_barrier\n s_barrier\n s_barrier\n s_barrier\n s_waitcnt lgkmcnt(0)"
                   : "=&s"(d), "=&s"(o) : "s"(q), "s"(qb) : "memory", "scc");
    } else if (!HV && !HM) { bar(); bar(); bar(); }
    waitv<0>();
    if (w == 6) TSOUT(21, 3);
  }
#endif
}

extern "C" void kernel_launch(void* const* d_in, const int* in_sizes, int n_in, void* d_out, int out_size, void* d_ws, size_t ws_size,
                              hipStream_t stream) {
  Params p;
  p.x = (const char*)d_in[0]; p.cr = (const char*)d_in[1]; p.ci = (const char*)d_in[2]; p.dr = (const char*)d_in[3]; p.di = (const char*)d_in[4];
  p.w1 = (const char*)d_in[6]; p.b1 = (const char*)d_in[7]; p.w2 = (const char*)d_in[8]; p.b2 = (const char*)d_in[9];
  p.out = (char*)d_out; p.ws = (char*)d_ws;
  (void)hipFuncSetAttribute((const void*)k_fnet, hipFuncAttributeMaxDynamicSharedMemorySize, LDS_BYTES);
  k_fnet<<<256, 512, LDS_BYTES, stream>>>(p);
}
```

```cpp
#include <hip/hip_runtime.h>
#include <hip/hip_bf16.h>
#include <stdint.h>

#define OA 16
constexpr int CTH = 8;
constexpr int NPRE = 4;
constexpr int TD = 10;
constexpr int TW = 4;
constexpr int TX1 = 4, TX2 = 9, TX3 = 12;
constexpr int US2 = 16, US3 = 13, US4 = 17;
constexpr int PD1 = 130, PD2 = 75, PD3 = 130;
constexpr int PSL = 4;
constexpr int NS2 = 4, NS4 = 4;
constexpr int KB1 = 4, K2 = 8, KB3 = NS4;
constexpr int UD = 5;
constexpr int HK3 = 14;
constexpr int RB = 3;
constexpr int LP1 = 3;
constexpr int CODELEN = 32768;

#define LDS __attribute__((address_space(3)))
typedef LDS char* ldsp;
typedef short v4s __attribute__((ext_vector_type(4)));
typedef short v8s __attribute__((ext_vector_type(8)));
typedef __bf16 v8b __attribute__((ext_vector_type(8)));
typedef __bf16 v2b __attribute__((ext_vector_type(2)));
typedef float v4f __attribute__((ext_vector_type(4)));
typedef float v2f __attribute__((ext_vector_type(2)));
typedef unsigned v4u __attribute__((ext_vector_type(4)));
typedef unsigned v2u __attribute__((ext_vector_type(2)));

constexpr int S_ = 2048, D_ = 640, F_ = 1728;
constexpr int OFF_E   = 0;
constexpr int FSTR    = 128;
constexpr int OFF_F0  = 4096;
constexpr int OFF_F0P = 1024;
constexpr int OFF_FWP = 2048;
constexpr int OFF_LNF = 256 * 1024;
constexpr int OFF_PQF = OFF_LNF + 40 * 64 * 1024;
constexpr int OFF_HF  = OFF_PQF + 128 * 40 * 1024;
constexpr int OFF_HH  = OFF_HF + 128 * 20 * 1024;
constexpr int OFF_W1F = OFF_HH + 128 * 54 * 1024;
constexpr int OFF_W2F = OFF_W1F + 108 * 20 * 1024;
constexpr int OFF_CS  = OFF_W2F + 40 * 54 * 1024;
constexpr int OFF_ST  = OFF_CS + 20 * 1728 * 4;
constexpr int OFF_FG  = OFF_ST + 8 * 2048 * 8;
constexpr int FM = FSTR, FWV = 4;
constexpr int WS_BYTES = OFF_FG + 768 * FM;
constexpr int OOB = 0x40000000;
constexpr int LDS_BYTES = 160 * 1024;

struct Params {
  const char* x; const char* cr; const char* ci; const char* dr; const char* di;
  const char* w1; const char* b1; const char* w2; const char* b2;
  char* out; char* ws;
};

#if defined(__HIP_DEVICE_COMPILE__)
typedef __amdgpu_buffer_rsrc_t rsrc_t;
constexpr int SC1 = 16;
__device__ __forceinline__ rsrc_t mkrsrc(const void* p, unsigned bytes) { return __builtin_amdgcn_make_buffer_rsrc((void*)p, 0, bytes, 0x00020000); }
template <int AUX> __device__ __forceinline__ void dma(rsrc_t r, ldsp dst, int voff, int soff) {
  __builtin_amdgcn_raw_ptr_buffer_load_lds(r, (LDS void*)dst, 16, voff, soff, 0, AUX);
}
__device__ __forceinline__ void st16(rsrc_t r, int off, v4u v) { __builtin_amdgcn_raw_buffer_store_b128(v, r, off, 0, SC1); }
__device__ __forceinline__ void st8(rsrc_t r, int off, v2u v) { __builtin_amdgcn_raw_buffer_store_b64(v, r, off, 0, SC1); }
__device__ __forceinline__ void st4(rsrc_t r, int off, unsigned v) { __builtin_amdgcn_raw_buffer_store_b32(v, r, off, 0, SC1); }
__device__ __forceinline__ v4u ld16(rsrc_t r, int off) { return __builtin_amdgcn_raw_buffer_load_b128(r, off, 0, SC1); }
__device__ __forceinline__ v2u ld8(rsrc_t r, int off, int soff) { return __builtin_amdgcn_raw_buffer_load_b64(r, off, soff, SC1); }
__device__ __forceinline__ unsigned ld4(rsrc_t r, int off, int soff) { return __builtin_amdgcn_raw_buffer_load_b32(r, off, soff, SC1); }
template <int N> __device__ __forceinline__ void waitv() { asm volatile("s_waitcnt vmcnt(%0)" ::"n"(N) : "memory"); }
__device__ __forceinline__ void bar() { asm volatile("" ::: "memory"); __builtin_amdgcn_s_barrier(); asm volatile("" ::: "memory"); }
__device__ __forceinline__ void waitl0() { __builtin_amdgcn_sched_barrier(0); __builtin_amdgcn_s_waitcnt(0xC07F); __builtin_amdgcn_sched_barrier(0); }
__device__ __forceinline__ v4f mfma(v8s a, v8s b, v4f c) {
  return __builtin_amdgcn_mfma_f32_16x16x32_bf16(__builtin_bit_cast(v8b, a), __builtin_bit_cast(v8b, b), c, 0, 0, 0);
}
__device__ __forceinline__ unsigned pk(float a, float b) { v2b r = {(__bf16)a, (__bf16)b}; return __builtin_bit_cast(unsigned, r); }
__device__ __forceinline__ float asf(unsigned u) { return __builtin_bit_cast(float, u); }
__device__ __forceinline__ unsigned asu(float f) { return __builtin_bit_cast(unsigned, f); }
__device__ __forceinline__ float lo(unsigned u) { return asf(u << 16); }
__device__ __forceinline__ float hi(unsigned u) { return asf(u & 0xffff0000u); }
template <int CTRL> __device__ __forceinline__ float dppadd(float v) { return v + asf(__builtin_amdgcn_update_dpp(0, asu(v), CTRL, 0xf, 0xf, true)); }
__device__ __forceinline__ float rowsum(float v) { return dppadd<0x121>(dppadd<0x122>(dppadd<0x124>(dppadd<0x128>(v)))); }
__device__ __forceinline__ float xsum16(float v) { const auto r = __builtin_amdgcn_permlane16_swap(asu(v), asu(v), false, false); return asf(r[0]) + asf(r[1]); }
__device__ __forceinline__ float xsum32(float v) { const auto r = __builtin_amdgcn_permlane32_swap(asu(v), asu(v), false, false); return asf(r[0]) + asf(r[1]); }
template <int NM, int ND> __device__ __forceinline__ void ilv() {
#pragma unroll
  for (int i = 0; i < NM; i++) { __builtin_amdgcn_sched_group_barrier(0x8, 1, 0); if (i < ND) __builtin_amdgcn_sched_group_barrier(0x100, 1, 0); }
}
__device__ __forceinline__ int piece_col(int T, int p, bool split) { return 16 * T + 4 * p + ((4 * p - 12 * (T & 1)) & ((int)split - 1)); }
__device__ __forceinline__ bool split640(int T) { return (T >> 1) % 5 == 2; }
__device__ __forceinline__ bool split1728(int T) { return (T >> 1) % 7 == 3 && T < 84; }
__device__ __forceinline__ unsigned lds_add(ldsp a) { return __hip_atomic_fetch_add((LDS unsigned*)a, 1u, __ATOMIC_RELAXED, __HIP_MEMORY_SCOPE_WORKGROUP); }
__device__ __forceinline__ void lds_wait(ldsp a, unsigned v) { while (*(volatile LDS unsigned*)a != v) __builtin_amdgcn_s_sleep(1); asm volatile("" ::: "memory"); }
constexpr int SPIN_CAP = 1 << 17;
__device__ __forceinline__ v4u ld16_async(const char* base, unsigned off) { v4u v; asm volatile("global_load_dwordx4 %0, %1, %2 sc1" : "=v"(v) : "v"(off), "s"(base) : "memory"); return v; }
template <int SO> __device__ __forceinline__ void ld4_async(unsigned& d, v4u r, int voff) {
  int t; asm volatile("s_mov_b32 %1, %4\n buffer_load_dword %0, %2, %3, %1 offen sc1" : "=v"(d), "=&s"(t) : "v"(voff), "s"(r), "n"(SO));
}
__device__ __forceinline__ void look_async(unsigned& d, v4u r, int voff) {
  d = 0; asm volatile("buffer_atomic_or %0, %1, %2, 0 offen sc0 sc1" : "+v"(d) : "v"(voff), "s"(r));
}
__device__ __forceinline__ void ld2_async(unsigned& d, v4u r, int voff) { asm volatile("buffer_load_ushort %0, %1, %2, 0 offen" : "=v"(d) : "v"(voff), "s"(r)); }
__device__ __forceinline__ v4u srd(const void* p, unsigned bytes) { return v4u{(unsigned)(size_t)p, (unsigned)((size_t)p >> 32) & 0xffffu, bytes, 0x00020000u}; }
__device__ __forceinline__ void sbar() { __builtin_amdgcn_sched_barrier(0); }
__device__ __forceinline__ unsigned now() { return (unsigned)__builtin_amdgcn_s_memrealtime(); }
template <int D> __device__ __forceinline__ void until(unsigned t0) {
#pragma unroll 1
  for (int it = 0; it < 64 && now() - t0 < D; it++) __builtin_amdgcn_s_sleep(1);
}
__device__ __forceinline__ bool eq4(v4u v, unsigned tag) { return __builtin_amdgcn_ballot_w64(v[0] != tag || v[1] != tag || v[2] != tag || v[3] != tag) == 0; }
__device__ __forceinline__ void poll256(rsrc_t r, int off, unsigned tag) {
#pragma unroll 1
  for (int it = 0; it < SPIN_CAP; it++) { asm volatile("" ::: "memory"); if (eq4(ld16(r, off), tag)) break; __builtin_amdgcn_s_sleep(2); }
  asm volatile("" ::: "memory");
}
template <unsigned long long MASK> __device__ __forceinline__ int pollg(v4u r, int off, unsigned tag) {
  int it;
#pragma unroll 1
  for (it = 0; it < SPIN_CAP; it++) {
    unsigned f; look_async(f, r, off); waitv<0>(); asm volatile("" : "+v"(f));
    if ((__builtin_amdgcn_ballot_w64(f != tag) & MASK) == 0) break;
    __builtin_amdgcn_s_sleep(PSL);
  }
  return it;
}
template <int V> struct IC { static constexpr int v = V; };
template <int A, int B, class F> __device__ __forceinline__ void sfor(F f) { if constexpr (A < B) { f(IC<A>{}); sfor<A + 1, B>(f); } }

constexpr int K1 = 1024, L1S = 26 * K1, L1R = 90 * K1, L3X = 150 * K1;
constexpr int RS = 1312;
constexpr int L0R = 52 * K1, L0W = 130 * K1, L0C = 150 * K1, L0D = 152 * K1;

__device__ __forceinline__ void consume1(ldsp smem, int w, int lane, v4f (&acc)[2][5]) {
  constexpr int MT = 2;
  const int g = lane >> 4, r = lane & 15;
  const int a0 = (w * 32 + r) * 128 + ((g ^ (r >> 1)) * 16), a1 = a0 ^ 64;
  const int b0 = 16 * K1 + lane * 16, b1 = b0 + K1;
  v8s pa[MT], pb[5], qa[MT], qb[5];
#pragma unroll
  for (int i = 0; i < MT; i++) { qa[i] = v8s{0, 0, 0, 0, 0, 0, 0, 0};
#pragma unroll
    for (int j = 0; j < 5; j++) acc[i][j] = v4f{0, 0, 0, 0}; }
#pragma unroll
  for (int j = 0; j < 5; j++) qb[j] = v8s{0, 0, 0, 0, 0, 0, 0, 0};
#define RD(A, B, ao, bo) { B[0] = *(LDS v8s*)(smem + so + bo); _Pragma("unroll") for (int i = 0; i < MT; i++) A[i] = *(LDS v8s*)(smem + so + ao + i * 2048); \
                           _Pragma("unroll") for (int j = 1; j < 5; j++) B[j] = *(LDS v8s*)(smem + so + bo + j * 2048); }
#define MM(A, B) { _Pragma("unroll") for (int j = 0; j < 5; j++) _Pragma("unroll") for (int i = 0; i < MT; i++) acc[i][j] = mfma(B[j], A[i], acc[i][j]); }
#define NEXT { bar(); so += L1S; if (so == 5 * L1S) so = 0; }
  int so = 3 * L1S;
#pragma unroll 1
  for (int s = 0; s < 31; s++) {
    RD(pa, pb, a0, b0); MM(qa, qb); ilv<5 * MT, 5 + MT>(); sbar();
    RD(qa, qb, a1, b1); MM(pa, pb); ilv<5 * MT, 5 + MT>(); sbar();
    NEXT
  }
  RD(pa, pb, a0, b0); MM(qa, qb); ilv<5 * MT, 5 + MT>(); sbar();
  RD(qa, qb, a1, b1); MM(pa, pb); ilv<5 * MT, 5 + MT>();
  sbar();
  MM(qa, qb);
#undef RD
#undef MM
#undef NEXT
}

template <int NJ, int NU, bool PAIR, int BL, int NR, class FB, class FS, class FX, class FH>
__device__ __forceinline__ void consume(ldsp bl0, rsrc_t rw, int va0, int va1, ldsp mine, v4f (&acc)[2][NJ], const v8s (&rb)[NR + 1][NJ], const int (&so)[BL], FB boff, FS sync, FX extra, FH hook) {
  v8s a[UD][2], bq[2][NJ];
  int va[2] = {va0, va1};
  ldsp bl[3] = {bl0, bl0 + 65536, bl0 + 131072};
  asm volatile("" : "+v"(va[0]), "+v"(va[1]), "+v"(bl[0]), "+v"(bl[1]), "+v"(bl[2]));
  bar();
#define LDA(u) _Pragma("unroll") for (int i = 0; i < 2; i++) a[(u) % UD][i] = __builtin_bit_cast(v8s, \
    __builtin_amdgcn_raw_buffer_load_b128(rw, va[i] + (PAIR ? (u) / BL * BL * 2048 : ((u) & 1) * 1024), so[(PAIR ? (u) : (u) >> 1) % BL], SC1));
#define LDB(u) _Pragma("unroll") for (int j = 0; j < NJ; j++) bq[(u) & 1][j] = *(LDS v8s*)(bl[(boff(u) + j * 2048) >> 16] + ((boff(u) + j * 2048) & 0xffff));
  LDA(0) sbar(); if (!NR) { LDB(0) } sbar();
#pragma unroll
  for (int u = 1; u < UD - 1; u++) { LDA(u) sbar(); }
  extra();
  sbar();
  sfor<1, NU + 1>([&](auto uc) {
    constexpr int u = uc.v;
    if (u < NU) { if (sync(u)) bar(); if (u >= NR) { LDB(u) } }
    const bool ld = u - 2 + UD < NU;
    if (ld) { LDA(u - 2 + UD) }
    hook(u);
    if constexpr (PAIR && u == NU) {
#pragma unroll
      for (int j = 0; j < NJ; j++) acc[1][j] = mfma(bq[(u - 1) & 1][j], a[(u - 1) % UD][1], acc[1][j]);
      sbar();
#pragma unroll
      for (int j = 0; j < NJ; j++) {
        acc[0][j] = mfma(bq[(u - 1) & 1][j], a[(u - 1) % UD][0], acc[0][j]);
        *(LDS v4f*)(mine + j * 1024) = acc[1][j];
        sbar();
      }
      return;
    }
#pragma unroll
    for (int j = 0; j < NJ; j++) {
      v8s bf;
      if (u <= NR) bf = rb[u <= NR ? u - 1 : 0][j]; else bf = bq[(u - 1) & 1][j];
#pragma unroll
      for (int i = 0; i < 2; i++) acc[i][j] = mfma(bf, a[(u - 1) % UD][i], u == 1 && (i || !PAIR) ? v4f{0, 0, 0, 0} : acc[i][j]);
    }
#pragma unroll
    for (int k = 0; k < 2 * NJ; k++) {
      __builtin_amdgcn_sched_group_barrier(0x8, 1, 0);
      if (u < NU && u >= NR && (k & 1)) __builtin_amdgcn_sched_group_barrier(0x100, 1, 0);
      if (ld && (k == 2 || k == 6)) __builtin_amdgcn_sched_group_barrier(0x20, 1, 0);
    }
    sbar();
  });
#undef LDA
#undef LDB
}

__device__ __forceinline__ void pair_add(ldsp other, v4f (&acc)[2][5], v4f (&o)[5]) {
#pragma unroll
  for (int j = 0; j < 5; j++) o[j] = *(LDS v4f*)(other + j * 1024);
  sbar();
#pragma unroll
  for (int j = 0; j < 5; j++) o[j] += acc[0][j];
}
#endif

__global__ __launch_bounds__(512, 2) void k_fnet(Params p) {
#if defined(__HIP_DEVICE_COMPILE__)
  extern __shared__ __attribute__((aligned(16))) char smem_[];
  ldsp smem = (ldsp)smem_;
  const int lane = threadIdx.x & 63, w = __builtin_amdgcn_readfirstlane(threadIdx.x >> 6), b = blockIdx.x;
  const int tg = (b & 7) * 2 + (b >> 7), c = (b >> 3) & 15, hf = c >> 3, nt = c & 7, odd = nt & 1;
  const int tg3 = (b & 6) * 2 + (b >> 6), c3 = (b & 1) * 8 + nt;
  const bool seven = c3 < 12;
  const int odd3 = seven ? c3 & 1 : 0, T0 = seven ? 7 * c3 : 84 + 6 * (c3 - 12), Ts = odd3 ? T0 : T0 + 6;
  const int g = lane >> 4;
  const rsrc_t rw = mkrsrc(p.ws, WS_BYTES);
  if (__builtin_expect(w < 4, 1)) {
    unsigned ep = ld4(rw, OFF_E, 0);
    const v2u* xr = (const v2u*)(p.x + (size_t)(b * 8 + w * 2 + (lane >> 5)) * 1280) + (lane & 31);
    v2u xv[5];
#pragma unroll
    for (int i = 0; i < 5; i++) xv[i] = xr[32 * i];
    if (w == 0 && lane < 4) *(LDS unsigned*)(smem + L0C + lane * 4) = 0;
    waitl0(); bar();
    asm volatile("" : "+v"(ep), "+v"(xv[0]), "+v"(xv[1]), "+v"(xv[2]), "+v"(xv[3]), "+v"(xv[4]));
    const unsigned tag = __builtin_amdgcn_readfirstlane(ep) + 1;
    {
      float v[20], s = 0;
#pragma unroll
      for (int i = 0; i < 5; i++) { v[4 * i] = lo(xv[i][0]); v[4 * i + 1] = hi(xv[i][0]); v[4 * i + 2] = lo(xv[i][1]); v[4 * i + 3] = hi(xv[i][1]); }
#pragma unroll
      for (int i = 0; i < 20; i++) s += v[i];
      const float mean = xsum16(rowsum(s)) * (1.f / 640.f);
      float q = 0;
#pragma unroll
      for (int i = 0; i < 20; i++) { v[i] -= mean; q += v[i] * v[i]; }
      const float rs = 1.0f / sqrtf(xsum16(rowsum(q)) * (1.f / 640.f) + 1e-5f);
#pragma unroll
      for (int i = 0; i < 5; i++)
        *(LDS v2u*)(smem + L0R + (w * 2 + (lane >> 5)) * RS + ((lane & 31) + 32 * i) * 8) = v2u{pk(v[4 * i] * rs, v[4 * i + 1] * rs), pk(v[4 * i + 2] * rs, v[4 * i + 3] * rs)};
      if (w == 0 && lane == 0) *(LDS unsigned*)(smem + L0C + 16) = tag;
      waitl0();
      if (lane == 0) lds_add(smem + L0C);
      lds_wait(smem + L0C, 4);
      const int qq = (lane & 15) >> 2, pp = lane & 3;
#pragma unroll
      for (int i = 0; i < 3; i++) {
        const int T = 10 * w + 4 * i + (i < 2 ? g : g & 1);
        ldsp a = smem + L0R + qq * RS + piece_col(T, pp, split640(T)) * 2;
        const v4s v0 = __builtin_amdgcn_ds_read_tr16_b64_v4i16((LDS v4s*)a), v1 = __builtin_amdgcn_ds_read_tr16_b64_v4i16((LDS v4s*)(a + 4 * RS));
        st16(rw, i == 2 && g >= 2 ? OOB : OFF_LNF + T * 65536 + (b >> 2) * 1024 + (b & 3) * 256 + (lane & 15) * 16,
             __builtin_bit_cast(v4u, __builtin_shufflevector(v0, v1, 0, 1, 2, 3, 4, 5, 6, 7)));
      }
      waitv<0>();
      unsigned old = 0;
      if (lane == 0) old = lds_add(smem + L0C + 4);
      if (__builtin_amdgcn_readfirstlane(old) == 3 && lane < 2) st4(rw, lane ? OFF_F0P + b * 4 : OFF_F0 + b * FSTR, tag);
    }
#define PUB(i) st4(rw, lane == 0 ? OFF_FG + (i) * FM + w * FWV : OOB, tag)
    const int kb0 = 5 * (nt >> 1), sp = ((odd * 2 + (g >> 1)) * 16 + (lane & 15)) * 16 + (g & 1) * 8;
    const int kh = w >> 1, tb0 = tg * 8 + hf * 4 + (w & 1) * 2, tb = tb0 + kh, t = tb * 16 + (lane & 15);
    const int cp = (kb0 + odd * 3) * 32 + g * 8, cs = (kb0 + 2) * 32 + odd * 16 + g * 4;
    sbar();
    const v4u x0 = *(const v4u*)(p.x + (size_t)t * 1280 + cp * 2), x1 = *(const v4u*)(p.x + (size_t)t * 1280 + cp * 2 + 64);
    const v2u x2 = *(const v2u*)(p.x + (size_t)t * 1280 + cs * 2);
    sbar();
    {
      v4f acc[2][5];
      bar();
      consume1(smem, w, lane, acc);
      v4f (&o)[2][5] = acc;
      const int tb1 = tg * 8 + w * 2;
      const float sg = hf ? -1.f : 1.f;
#pragma unroll
      for (int mi = 0; mi < 2; mi++) {
        const int dst = OFF_PQF + (tb1 + mi) * 40960 + (hf * 20 + kb0) * 1024;
#pragma unroll
        for (int u = 0; u < 2; u++) {
          const v4f a = o[mi][2 * u] * sg, c2 = o[mi][2 * u + 1] * sg;
          st16(rw, dst + (odd * 3 + u) * 1024 + lane * 16, v4u{pk(a[0], a[1]), pk(a[2], a[3]), pk(c2[0], c2[1]), pk(c2[2], c2[3])});
        }
        const v4f a = o[mi][4] * sg;
        st8(rw, dst + 2048 + sp, v2u{pk(a[0], a[1]), pk(a[2], a[3])});
      }
      bar(); waitv<0>();
      PUB(tg * 16 + c);
    }
    v4u h0, h1, c0, c1; v2u h2, c2;
    int so10[10];
#pragma unroll
    for (int i = 0; i < 10; i++) { so10[i] = i * 2048; asm volatile("" : "+s"(so10[i])); }
    const v8s none5[1][5] = {}, none7[1][7] = {};
#define UNP(a, b) v4f{lo(a), hi(a), lo(b), hi(b)}
    {
      v4f acc[2][5], z[5];
      const int r = lane & 15;
      acc[0][0] = UNP(x0[0], x0[1]); acc[0][1] = UNP(x0[2], x0[3]); acc[0][2] = UNP(x1[0], x1[1]); acc[0][3] = UNP(x1[2], x1[3]); acc[0][4] = UNP(x2[0], x2[1]);
      asm volatile("" : "+v"(acc[0][0]), "+v"(acc[0][1]), "+v"(acc[0][2]), "+v"(acc[0][3]), "+v"(acc[0][4]));
      consume<5, 20, true, 10, 0>(smem + r * 128 + ((kh * 4 + g) ^ (r >> 1)) * 16, rw, OFF_PQF + tb * 40960 + kh * 1024 + lane * 16,
        OFF_PQF + (tb ^ 1) * 40960 + kh * 1024 + lane * 16, smem + (100 + 5 * w) * K1 + lane * 16, acc, none5, so10,
        [](int u) { return (130 + 10 * u) % 160 * K1; }, [](int u) { return u == 5 || u == US2; }, [] {}, [](int) {});
      waitl0(); bar();
      pair_add(smem + (100 + 5 * (w ^ 2)) * K1 + lane * 16, acc, z);
      h0 = v4u{pk(z[0][0], z[0][1]), pk(z[0][2], z[0][3]), pk(z[1][0], z[1][1]), pk(z[1][2], z[1][3])};
      h1 = v4u{pk(z[2][0], z[2][1]), pk(z[2][2], z[2][3]), pk(z[3][0], z[3][1]), pk(z[3][2], z[3][3])};
      h2 = v2u{pk(z[4][0], z[4][1]), pk(z[4][2], z[4][3])};
      const int dst = OFF_HF + tb * 20480 + kb0 * 1024;
      st16(rw, dst + odd * 3072 + lane * 16, h0);
      st16(rw, dst + odd * 3072 + 1024 + lane * 16, h1);
      st8(rw, dst + 2048 + sp, h2);
      const v4f s4 = (z[0] + z[1]) + (z[2] + z[3]) + z[4];
      const float m = xsum32(xsum16((s4[0] + s4[1]) + (s4[2] + s4[3]))) * (1.f / 80.f);
      v4f q4 = {0, 0, 0, 0};
#pragma unroll
      for (int j = 0; j < 5; j++) { const v4f d = z[j] - m; q4 += d * d; }
      const float q = xsum32(xsum16((q4[0] + q4[1]) + (q4[2] + q4[3])));
      st8(rw, lane < 16 ? OFF_ST + nt * 16384 + t * 8 : OOB, v2u{asu(m), asu(q)});
      bar(); waitv<0>();
      PUB(256 + tg * 16 + c);
    }
      v8s rb[RB + 1][5];
      int so9[9];
#pragma unroll
      for (int i = 0; i < 9; i++) { so9[i] = i * 2048; asm volatile("" : "+s"(so9[i])); }
      auto ldrb = [&] {
      sbar();
#pragma unroll
      for (int u = 0; u < RB; u++)
#pragma unroll
        for (int j = 0; j < 5; j++)
          rb[u][j] = __builtin_bit_cast(v8s, __builtin_amdgcn_raw_buffer_load_b128(rw, OFF_W2F + kh * 1024 + lane * 16, (5 * nt + (odd ? (j + 1) % 5 : j)) * 55296 + so9[u], SC1));
      sbar();
      };
    {
      const int k3 = (T0 + odd3) >> 1;
      v4f acc[2][7];
      v8s ea[2], eb[7];
      float st[2];
      consume<7, 20, false, 10, 0>(smem + lane * 16, rw, OFF_HF + (tg3 * 8 + w * 2) * 20480 + lane * 16, OFF_HF + (tg3 * 8 + w * 2 + 1) * 20480 + lane * 16, smem, acc, none7, so10,
        [](int u) { return (10 + 14 * (u >> 1) + (u & 1)) * K1; }, [](int u) { return u == 7 || u == US3 || (TX3 > TX2 && u == US4); }, [&] {
          c0 = *(const v4u*)(p.b2 + cp * 2); c1 = *(const v4u*)(p.b2 + cp * 2 + 64); c2 = *(const v2u*)(p.b2 + cs * 2); },
        [&](int u) {
          if (u == HK3) {
#pragma unroll
            for (int i = 0; i < 2; i++) ea[i] = *(LDS v8s*)(smem + L3X + (g ? 4608 + lane * 16 : 2048 + (w * 32 + lane) * 16) + i * 256);
#pragma unroll
            for (int j = 0; j < 7; j++) eb[j] = *(LDS v8s*)(smem + L3X + (lane & 15) * 16 + j * 256);
          }
          if (u == HK3 + 1) {
#pragma unroll
            for (int j = 0; j < 7; j++)
#pragma unroll
              for (int i = 0; i < 2; i++) acc[i][j] = mfma(eb[j], ea[i], acc[i][j]);
            sbar();
          }
          if (u == 19) { st[0] = *(LDS float*)(smem + L3X + 4096 + (w * 32 + (lane & 15)) * 4); st[1] = *(LDS float*)(smem + L3X + 4160 + (w * 32 + (lane & 15)) * 4); } });
#pragma unroll
      for (int mi = 0; mi < 2; mi++) {
        const int dst = OFF_HH + (tg3 * 8 + w * 2 + mi) * 55296;
        unsigned r[14];
#pragma unroll
        for (int j = 0; j < 7; j++) {
          v4f z = acc[mi][j] * st[mi];
          z = __builtin_elementwise_max(z, v4f{0, 0, 0, 0}); z *= z;
          r[2 * j] = pk(z[0], z[1]); r[2 * j + 1] = pk(z[2], z[3]);
          if (j & 1) st16(rw, dst + (k3 + (j >> 1)) * 1024 + lane * 16, v4u{r[2 * j - 2], r[2 * j - 1], r[2 * j], r[2 * j + 1]});
        }
        st8(rw, seven ? dst + (Ts >> 1) * 1024 + ((odd3 * 2 + (g >> 1)) * 16 + (lane & 15)) * 16 + (g & 1) * 8 : OOB, v2u{r[12], r[13]});
      }
      bar();
      waitv<0>();
      PUB(512 + tg3 * 16 + c3);
    }
    {
      v4f acc[2][5], z[5];
      ldrb();
      acc[0][0] = UNP(h0[0], h0[1]) + UNP(c0[0], c0[1]); acc[0][1] = UNP(h0[2], h0[3]) + UNP(c0[2], c0[3]); acc[0][2] = UNP(h1[0], h1[1]) + UNP(c1[0], c1[1]);
      acc[0][3] = UNP(h1[2], h1[3]) + UNP(c1[2], c1[3]); acc[0][4] = UNP(h2[0], h2[1]) + UNP(c2[0], c2[1]);
      asm volatile("" : "+v"(acc[0][0]), "+v"(acc[0][1]), "+v"(acc[0][2]), "+v"(acc[0][3]), "+v"(acc[0][4]));
      consume<5, 27, true, 9, RB>(smem + kh * 1024 + lane * 16, rw, OFF_HH + tb * 55296 + kh * 1024 + lane * 16, OFF_HH + (tb ^ 1) * 55296 + kh * 1024 + lane * 16,
        smem + (120 + 5 * w) * K1 + lane * 16, acc, rb, so9,
        [](int u) { return (u + 15 - RB) % 15 * 10 * K1; }, [](int u) { return u == 5 + RB || u == 9 + RB || u == 15 + RB || (NS4 < 8 && u == 19 + RB) || u == 23 + RB; }, [] {}, [](int) {});
      waitl0(); bar();
      pair_add(smem + (120 + 5 * (w ^ 2)) * K1 + lane * 16, acc, z);
      const rsrc_t ro = mkrsrc(p.out, S_ * D_ * 2);
      __builtin_amdgcn_raw_buffer_store_b128(v4u{pk(z[0][0], z[0][1]), pk(z[0][2], z[0][3]), pk(z[1][0], z[1][1]), pk(z[1][2], z[1][3])}, ro, t * 1280 + cp * 2, 0, OA);
      __builtin_amdgcn_raw_buffer_store_b128(v4u{pk(z[2][0], z[2][1]), pk(z[2][2], z[2][3]), pk(z[3][0], z[3][1]), pk(z[3][2], z[3][3])}, ro, t * 1280 + cp * 2 + 64, 0, OA);
      __builtin_amdgcn_raw_buffer_store_b64(v2u{pk(z[4][0], z[4][1]), pk(z[4][2], z[4][3])}, ro, t * 1280 + cs * 2, 0, OA);
    }
  } else if (w < 6) {
    const int lw = w - 4;
    int vt[5];
#pragma unroll
    for (int i = 0; i < 5; i++) { const int j = (lw * 5 + i) >> 1; vt[i] = 5 * nt + (odd ? (j + 1) % 5 : j); }
    unsigned tag, t0;
    v4u sg[10][5];
    int vd[5];
#pragma unroll
    for (int i = 0; i < 5; i++) {
      const int row = (lw * 5 + i) * 8 + (lane >> 3), j = row >> 4, T = 5 * nt + (odd ? (j + 1) % 5 : j), n = row & 15;
      vd[i] = (piece_col(T, n >> 2, j == 4) + (n & 3)) * 1280 + ((lane & 7) ^ ((row >> 1) & 7)) * 16;
    }
    auto e2 = [&](int s0, int s1) {
#pragma unroll 1
      for (int s = s0; s < s1; s++) {
        const rsrc_t r = mkrsrc(s < 10 ? p.dr : p.di - 1280, D_ * D_ * 2 + (s < 10 ? 0 : 1280));
        ldsp d = smem + ((s + 13) & 15) * 10240 + lw * 5120;
        const int q = s;
#pragma unroll
        for (int i = 0; i < 5; i++) dma<0>(r, d + i * 1024, vd[i], (q < (s < 10 ? 10 : 20) ? q : q - 10) * 128);
      }
    };
    auto stage2 = [&](int k0, int k1) {
      sbar();
#pragma unroll
      for (int k = k0; k < k1; k++)
#pragma unroll
        for (int i = 0; i < 5; i++) sg[k][i] = __builtin_amdgcn_raw_buffer_load_b128(mkrsrc(p.di, D_ * D_ * 2), vd[i], (6 + k) % 10 * 128, 0);
      sbar();
    };
    {
      const rsrc_t ra = mkrsrc((hf ? p.ci : p.cr) + (size_t)tg * 128 * 4096, 128 * 4096);
      int va[8], vb[5];
#pragma unroll
      for (int i = 0; i < 8; i++) { const int row = (lw * 8 + i) * 8 + (lane >> 3); va[i] = row * 4096 + ((lane & 7) ^ ((row >> 1) & 7)) * 16; }
#pragma unroll
      for (int i = 0; i < 5; i++) vb[i] = OFF_LNF + vt[i] * 65536 + ((lw + i) & 1) * 1024 + lane * 16;
      auto ks = [&](int s) { return ((s & ~3) + ((s + (b >> 6)) & 3) + 4 * nt) & 31; };
      auto l1 = [&](int s, int so) {
#pragma unroll
        for (int i = 0; i < 5; i++) dma<SC1>(rw, smem + so + (16 + lw * 5 + i) * 1024, vb[i], ks(s) * 2048);
      };
      auto e1 = [&](int s, int so, bool wa) {
        if (wa) {
#pragma unroll
          for (int i = 0; i < 8; i++) dma<0>(ra, smem + so + (lw * 8 + i) * 1024, va[i], ks(s) * 128);
        }
        l1(s, so);
      };
      const int vf0 = lane < 32 ? OFF_F0 + (32 * nt + lane) * FSTR : OOB;
      bar();
#pragma unroll
      for (int s = 0; s < NPRE; s++)
#pragma unroll
        for (int i = 0; i < 8; i++) {
          dma<0>(ra, smem + (s + 3) % 5 * L1S + (lw * 8 + i) * 1024, va[i], ks(s) * 128);
          if (s * 8 + i >= CTH - 1) waitv<CTH - 1>();
        }
      lds_wait(smem + L0C, 4);
      tag = __builtin_amdgcn_readfirstlane(*(volatile LDS unsigned*)(smem + L0C + 16));
      pollg<0xffffffffull>(srd(p.ws, WS_BYTES), vf0, tag);
      constexpr int N2 = NPRE > 2 ? 5 : 13, N3 = NPRE > 3 ? 5 : 13;
      l1(0, 3 * L1S);
      v4u fp = ld16_async(p.ws, OFF_F0P + lane * 16);
      l1(1, 4 * L1S); e1(2, 0, NPRE < 3);
      waitv<6 + N2>(); bar();
      e1(3, L1S, NPRE < 4);
      waitv<N2 + N3>();
      asm volatile("" : "+v"(fp));
      if (lw == 0) {
        if (!eq4(fp, tag)) poll256(rw, OFF_F0P + lane * 16, tag);
        if (b == 0 && lane == 0) st4(rw, OFF_E, tag);
      }
      bar();
      e1(4, 2 * L1S, true); waitv<N3 + 13>(); bar();
      int so = 3 * L1S;
#pragma unroll 1
      for (int s = 5; s < 32; s++) {
        e1(s, so, true); so += L1S; if (so == 5 * L1S) so = 0;
        waitv<26>(); bar();
      }
      e2(0, 5); waitv<38>(); bar();
      e2(5, TD < 8 ? TD : 8); waitv<5 * (TD < 8 ? TD : 8)>(); bar();
      e2(8, TD);
      bar();
      t0 = now();
      e2(TD, 16);
      stage2(0, NS2 - KB1);
    }
    auto put = [&](int k0, int k1, int slot) {
#pragma unroll
      for (int k = k0; k < k1; k++)
#pragma unroll
        for (int i = 0; i < 5; i++) *(LDS v4u*)(smem + (slot + k - k0) % 16 * 10240 + lw * 5120 + i * 1024 + lane * 16) = sg[k][i];
      waitl0();
    };
    int vb3[7];
#pragma unroll
    for (int i = 0; i < 7; i++) { const int f = lw * 7 + i, j = f >> 1, T = j < 6 ? T0 + odd3 + j : Ts;
      vb3[i] = (j < 6 || seven) ? OFF_W1F + T * 20480 + (f & 1) * 1024 + lane * 16 : OOB; }
    auto e3 = [&](int s0, int s1) {
#pragma unroll 1
      for (int s = s0; s < s1; s++) {
        ldsp d = smem + (10 + 14 * s + lw * 7) * K1;
#pragma unroll
        for (int i = 0; i < 7; i++) dma<SC1>(rw, d + i * 1024, vb3[i], s * 2048);
      }
    };
    const int fl = OFF_FG + (lane >> 2) * FM + (lane & 3) * FWV, i2 = lw * 64 + lane;
#define LOOK(D, N, grp, more) { unsigned f; until<D>(t0); look_async(f, srd(p.ws, WS_BYTES), lw ? OOB : fl + (grp) * 16 * FM); more; waitv<N>(); \
      asm volatile("" : "+v"(f)); if (lw == 0 && __builtin_amdgcn_ballot_w64(f != tag)) { pollg<~0ull>(srd(p.ws, WS_BYTES), fl + (grp) * 16 * FM, tag); } }
    {
      LOOK(PD1, 5 * KB1, tg, stage2(NS2 - KB1, NS2))
      bar();
      v4u fp = ld16_async(p.ws, OFF_FWP + lane * 16);
      bar();
      if (NS2) put(0, 4, 13); else e2(16, 20);
      waitv<0>();
      asm volatile("" : "+v"(fp));
      if (lw == 0 && !eq4(fp, tag)) poll256(rw, OFF_FWP + lane * 16, tag);
      bar();
      e3(0, TW);
      const int j = i2 >> 4, T = j < 6 ? T0 + odd3 + j : Ts;
      const bool on = i2 < (seven ? 112 : 96);
      const int vc = on ? OFF_CS + (T * 16 + (i2 & 15)) * 4 : OOB, vbias = on ? (piece_col(T, (i2 & 15) >> 2, split1728(T)) + (i2 & 3)) * 2 : OOB;
      unsigned cpart[20], bb;
      bar();
      ld2_async(bb, srd(p.b1, F_ * 2), vbias);
      sfor<0, 20>([&](auto k) { ld4_async<k.v * F_ * 4>(cpart[k.v], srd(p.ws, WS_BYTES), vc); });
      bar();
      t0 = now();
      e3(TW, K2);
      waitv<7 * (K2 - TW)>();
      float cq[4] = {0, 0, 0, 0};
#pragma unroll
      for (int k = 0; k < 20; k++) { asm volatile("" : "+v"(cpart[k])); cq[k & 3] += asf(cpart[k]); }
      asm volatile("" : "+v"(bb));
      const float csum = (cq[0] + cq[1]) + (cq[2] + cq[3]);
      const unsigned c1 = pk(csum, csum), c2 = pk(csum - lo(c1), csum - lo(c1)), c3 = pk(0, csum - lo(c1) - lo(c2));
      const v4u fb = {(c1 & 0xffff) | (c2 & 0xffff0000u), (c1 & 0xffff) | c3, (c2 & 0xffff) | (c1 & 0xffff0000u), bb * 0x10001u};
      asm volatile("ds_write_b128 %0, %1\n ds_write_b128 %0, %2 offset:4608" ::"v"((unsigned)(size_t)(smem + L3X + i2 * 16)), "v"(fb), "v"(v4u{0, 0, 0, 0}) : "memory");
    }
    int vb4[5];
#pragma unroll
    for (int i = 0; i < 5; i++) vb4[i] = OFF_W2F + vt[i] * 55296 + ((lw + i) & 1) * 1024 + lane * 16;
    auto e4 = [&](int s0, int s1) {
#pragma unroll 1
      for (int s = s0; s < s1; s++) {
        ldsp d = smem + (s >= 15 ? s - 15 : s) * 10240 + lw * 5120;
        const int u = s + RB, q = u, e = u < 9 ? 9 : u < 18 ? 18 : 27;
#pragma unroll
        for (int i = 0; i < 5; i++) dma<SC1>(rw, d + i * 1024, vb4[i], (q < e ? q : q - 9) * 2048);
      }
    };
    auto stage4 = [&](int k0, int k1) {
      sbar();
#pragma unroll
      for (int k = k0; k < k1; k++) {
        const int u = 15 + k + RB, q = u, so = (q < (u < 18 ? 18 : 27) ? q : q - 9) * 2048;
#pragma unroll
        for (int i = 0; i < 5; i++) sg[k][i] = __builtin_amdgcn_raw_buffer_load_b128(rw, vb4[i], so, SC1);
      }
      sbar();
    };
    {
      LOOK(PD2, 7 * (10 - K2), 16 + tg3, e3(K2, 10))
      bar();
      v2u sv[8];
#pragma unroll
      for (int k = 0; k < 8; k++) sv[k] = ld8(rw, OFF_ST + (tg3 * 128 + i2) * 8, k * 16384);
      __builtin_amdgcn_s_setprio(LP1);
#pragma unroll
      for (int k = 0; k < 8; k++) asm volatile("" : "+v"(sv[k]));
      const float m = (((asf(sv[0][0]) + asf(sv[1][0])) + (asf(sv[2][0]) + asf(sv[3][0]))) + ((asf(sv[4][0]) + asf(sv[5][0])) + (asf(sv[6][0]) + asf(sv[7][0])))) * 0.125f;
      float q[2] = {0, 0}, dq[2] = {0, 0};
#pragma unroll
      for (int k = 0; k < 8; k++) { const float d = asf(sv[k][0]) - m; dq[k & 1] += d * d; q[k & 1] += asf(sv[k][1]); }
      const float var = (q[0] + q[1] + 80.f * (dq[0] + dq[1])) * (1.f / 640.f) + 1e-5f, rs = __builtin_amdgcn_rsqf(var), sd = var * rs;
      const unsigned a1 = pk(-m, -m), as1 = pk(-m - lo(a1), sd), as2 = pk(-m - lo(a1) - lo(as1), sd - hi(as1));
      const v4u fa = {a1, (as1 & 0xffff) | (a1 & 0xffff0000u), (as1 & 0xffff) | as2 << 16, as1 >> 16 | (as2 & 0xffff0000u)};
      asm volatile("ds_write_b128 %0, %1 offset:2048\n ds_write_b32 %2, %3\n s_waitcnt lgkmcnt(0)"
                   ::"v"((unsigned)(size_t)(smem + L3X + i2 * 16)), "v"(fa), "v"((unsigned)(size_t)(smem + L3X + 4096 + i2 * 4)), "v"(rs) : "memory");
      __builtin_amdgcn_s_setprio(0);
      bar();
      e4(0, TX1);
      bar();
      e4(TX1, TX2);
      bar();
      if (TX3 > TX2) { e4(TX2, TX3); bar(); }
      t0 = now();
      e4(TX3, 15);
      stage4(0, NS4 - KB3);
    }
    {
      LOOK(PD3, 5 * KB3, 32 + tg, stage4(NS4 - KB3, NS4))
      bar();
      bar();
      if (NS4) put(0, 4, 0); else e4(15, 19);
      bar();
      if (NS4 >= 8) {
        put(4, 8, 4); bar();
        if (RB < 4) { put(8, NS4, 8); e4(15 + NS4, 27 - RB); waitv<0>(); bar(); }
      } else {
        e4(19, 23); waitv<20>(); bar();
        e4(23, 27 - RB); waitv<5 * (4 - RB)>(); bar();
        if (RB < 4) { waitv<0>(); bar(); }
      }
    }
  } else {
    const int hw = w - 6, nu = hw == 0 && b < 56 ? 3 : 2;
    if (hw == 0) {
      unsigned long long pc; unsigned back;
      asm volatile("s_getpc_b64 %0\n.Lfnet_pc:\n s_mov_b32 %1, .Lfnet_pc-_Z6k_fnet6Params" : "=s"(pc), "=s"(back));
      dma<0>(mkrsrc((const void*)(pc - back), CODELEN), smem + L0D, (b >> 3) * 1024 + lane * 16, 0);
    }
    auto touch = [&] {
#pragma unroll
    for (int k = 0; k < 4; k++) {
      const int q = b + 256 * (hw * 3 + k);
      const rsrc_t r = hw || k < 3 ? mkrsrc(q < 800 ? p.dr + q * 1024 : p.di + (q - 800) * 1024, q < 1600 ? 1024 : 0)
                                   : mkrsrc(b & 32 ? p.b2 : p.b1, b & 32 ? D_ * 2 : F_ * 2);
      dma<0>(r, smem + L0D, lane * 16 + (hw || k < 3 ? 0 : (b >> 3 & 3) * 1024), 0);
    }
    };
    bar();
#pragma unroll 1
    for (int n = 0; n < nu; n++) {
      const int i = n < 2 ? hw * 2 + n : 4, u = b + 256 * i;
      const bool isw1 = u < 540;
      const int uu = isw1 ? u : u - 540, kb = isw1 ? uu / 27 : uu / 10, cu = isw1 ? uu % 27 : uu % 10, ld = isw1 ? F_ * 2 : D_ * 2;
      const rsrc_t r = mkrsrc(isw1 ? p.w1 : p.w2, D_ * F_ * 2);
#pragma unroll
      for (int k = 0; k < 4; k++) dma<0>(r, smem + L0W + i * 4096 + k * 1024, (kb * 32 + k * 8 + (lane >> 3)) * ld + cu * 128 + (lane & 7) * 16, 0);
    }
    waitv<0>();
    lds_wait(smem + L0C + 4, 4);
    const int qq = (lane & 15) >> 2, pp = lane & 3;
#pragma unroll 1
    for (int n = 0; n < nu; n++) {
      const int i = n < 2 ? hw * 2 + n : 4, u = b + 256 * i;
      const bool isw1 = u < 540;
      const int uu = isw1 ? u : u - 540, kb = isw1 ? uu / 27 : uu / 10, cu = isw1 ? uu % 27 : uu % 10;
      unsigned a[4];
#pragma unroll
      for (int t = 0; t < 4; t++) { const int T = cu * 4 + t;
        a[t] = (unsigned)(size_t)(smem + L0W + i * 4096 + (g * 8 + qq) * 128 + (piece_col(T, pp, isw1 ? split1728(T) : split640(T)) - cu * 64) * 2); }
      v2u v[8];
      asm volatile("ds_read_b64_tr_b16 %0, %8\n ds_read_b64_tr_b16 %1, %8 offset:512\n ds_read_b64_tr_b16 %2, %9\n ds_read_b64_tr_b16 %3, %9 offset:512\n"
                   "ds_read_b64_tr_b16 %4, %10\n ds_read_b64_tr_b16 %5, %10 offset:512\n ds_read_b64_tr_b16 %6, %11\n ds_read_b64_tr_b16 %7, %11 offset:512\n s_waitcnt lgkmcnt(0)"
                   : "=&v"(v[0]), "=&v"(v[1]), "=&v"(v[2]), "=&v"(v[3]), "=&v"(v[4]), "=&v"(v[5]), "=&v"(v[6]), "=&v"(v[7]) : "v"(a[0]), "v"(a[1]), "v"(a[2]), "v"(a[3]) : "memory");
      const int dst = (isw1 ? OFF_W1F + cu * 4 * 20480 : OFF_W2F + cu * 4 * 55296) + kb * 1024 + lane * 16;
#pragma unroll
      for (int t = 0; t < 4; t++) st16(rw, dst + t * (isw1 ? 20480 : 55296), v4u{v[2 * t][0], v[2 * t][1], v[2 * t + 1][0], v[2 * t + 1][1]});
      if (isw1) {
        float cs = 0;
#pragma unroll
        for (int t = 0; t < 4; t++) {
          const v2u v0 = v[2 * t], v1 = v[2 * t + 1];
          const float c1 = xsum32(xsum16((lo(v0[0]) + hi(v0[0])) + (lo(v0[1]) + hi(v0[1])) + (lo(v1[0]) + hi(v1[0])) + (lo(v1[1]) + hi(v1[1]))));
          cs = g == t ? c1 : cs;
        }
        st4(rw, OFF_CS + (kb * F_ + cu * 64 + lane) * 4, asu(cs));
      }
    }
    touch();
    waitv<4>();
    const unsigned tag = *(volatile LDS unsigned*)(smem + L0C + 16);
    unsigned old = 0;
    if (lane == 0) old = lds_add(smem + L0C + 8);
    if (__builtin_amdgcn_readfirstlane(old) == 1 && lane == 0) st4(rw, OFF_FWP + b * 4, tag);
    bar(); bar(); bar();
    waitv<0>();
  }
#endif
}

extern "C" void kernel_launch(void* const* d_in, const int* in_sizes, int n_in, void* d_out, int out_size, void* d_ws, size_t ws_size,
                              hipStream_t stream) {
  Params p;
  p.x = (const char*)d_in[0]; p.cr = (const char*)d_in[1]; p.ci = (const char*)d_in[2]; p.dr = (const char*)d_in[3]; p.di = (const char*)d_in[4];
  p.w1 = (const char*)d_in[6]; p.b1 = (const char*)d_in[7]; p.w2 = (const char*)d_in[8]; p.b2 = (const char*)d_in[9];
  p.out = (char*)d_out; p.ws = (char*)d_ws;
  (void)hipFuncSetAttribute((const void*)k_fnet, hipFuncAttributeMaxDynamicSharedMemorySize, LDS_BYTES);
  k_fnet<<<256, 512, LDS_BYTES, stream>>>(p);
}
```

```cpp
#include <hip/hip_runtime.h>
#include <hip/hip_bf16.h>
#include <stdint.h>

constexpr int CTH = 8;
constexpr int NPRE = 4;
constexpr int TD = 10;
constexpr int TW = 4;
constexpr int TX1 = 4, TX2 = 9, TX3 = 12;
constexpr int US2 = 16, US3 = 13, US4 = 17;
constexpr int PD1 = 130, PD2 = 75, PD3 = 130;
constexpr int PSL = 4;
constexpr int NS2 = 4, NS4 = 4;
constexpr int KB1 = 4, K2 = 8, KB3 = NS4;
constexpr int UD = 5;
constexpr int HK3 = 14;
constexpr int RB = 3;
constexpr int LP1 = 3;
constexpr int CODELEN = 32768;

#define LDS __attribute__((address_space(3)))
typedef LDS char* ldsp;
typedef short v4s __attribute__((ext_vector_type(4)));
typedef short v8s __attribute__((ext_vector_type(8)));
typedef __bf16 v8b __attribute__((ext_vector_type(8)));
typedef __bf16 v2b __attribute__((ext_vector_type(2)));
typedef float v4f __attribute__((ext_vector_type(4)));
typedef float v2f __attribute__((ext_vector_type(2)));
typedef unsigned v4u __attribute__((ext_vector_type(4)));
typedef unsigned v2u __attribute__((ext_vector_type(2)));

constexpr int S_ = 2048, D_ = 640, F_ = 1728;
constexpr int OFF_E   = 0;
constexpr int FSTR    = 128;
constexpr int OFF_F0  = 4096;
constexpr int OFF_F0P = 1024;
constexpr int OFF_FWP = 2048;
constexpr int OFF_LNF = 256 * 1024;
constexpr int OFF_PQF = OFF_LNF + 40 * 64 * 1024;
constexpr int OFF_HF  = OFF_PQF + 128 * 40 * 1024;
constexpr int OFF_HH  = OFF_HF + 128 * 20 * 1024;
constexpr int OFF_W1F = OFF_HH + 128 * 54 * 1024;
constexpr int OFF_W2F = OFF_W1F + 108 * 20 * 1024;
constexpr int OFF_CS  = OFF_W2F + 40 * 54 * 1024;
constexpr int OFF_ST  = OFF_CS + 20 * 1728 * 4;
constexpr int OFF_FG  = OFF_ST + 8 * 2048 * 8;
constexpr int FM = FSTR, FWV = 4;
constexpr int WS_BYTES = OFF_FG + 768 * FM;
constexpr int OOB = 0x40000000;
constexpr int LDS_BYTES = 160 * 1024;

struct Params {
  const char* x; const char* cr; const char* ci; const char* dr; const char* di;
  const char* w1; const char* b1; const char* w2; const char* b2;
  char* out; char* ws;
};

#if defined(__HIP_DEVICE_COMPILE__)
typedef __amdgpu_buffer_rsrc_t rsrc_t;
constexpr int SC1 = 16;
__device__ __forceinline__ rsrc_t mkrsrc(const void* p, unsigned bytes) { return __builtin_amdgcn_make_buffer_rsrc((void*)p, 0, bytes, 0x00020000); }
template <int AUX> __device__ __forceinline__ void dma(rsrc_t r, ldsp dst, int voff, int soff) {
  __builtin_amdgcn_raw_ptr_buffer_load_lds(r, (LDS void*)dst, 16, voff, soff, 0, AUX);
}
__device__ __forceinline__ void st16(rsrc_t r, int off, v4u v) { __builtin_amdgcn_raw_buffer_store_b128(v, r, off, 0, SC1); }
__device__ __forceinline__ void st8(rsrc_t r, int off, v2u v) { __builtin_amdgcn_raw_buffer_store_b64(v, r, off, 0, SC1); }
__device__ __forceinline__ void st4(rsrc_t r, int off, unsigned v) { __builtin_amdgcn_raw_buffer_store_b32(v, r, off, 0, SC1); }
__device__ __forceinline__ v4u ld16(rsrc_t r, int off) { return __builtin_amdgcn_raw_buffer_load_b128(r, off, 0, SC1); }
__device__ __forceinline__ v2u ld8(rsrc_t r, int off, int soff) { return __builtin_amdgcn_raw_buffer_load_b64(r, off, soff, SC1); }
__device__ __forceinline__ unsigned ld4(rsrc_t r, int off, int soff) { return __builtin_amdgcn_raw_buffer_load_b32(r, off, soff, SC1); }
template <int N> __device__ __forceinline__ void waitv() { asm volatile("s_waitcnt vmcnt(%0)" ::"n"(N) : "memory"); }
__device__ __forceinline__ void bar() { asm volatile("" ::: "memory"); __builtin_amdgcn_s_barrier(); asm volatile("" ::: "memory"); }
__device__ __forceinline__ void waitl0() { __builtin_amdgcn_sched_barrier(0); __builtin_amdgcn_s_waitcnt(0xC07F); __builtin_amdgcn_sched_barrier(0); }
__device__ __forceinline__ v4f mfma(v8s a, v8s b, v4f c) {
  return __builtin_amdgcn_mfma_f32_16x16x32_bf16(__builtin_bit_cast(v8b, a), __builtin_bit_cast(v8b, b), c, 0, 0, 0);
}
__device__ __forceinline__ unsigned pk(float a, float b) { v2b r = {(__bf16)a, (__bf16)b}; return __builtin_bit_cast(unsigned, r); }
__device__ __forceinline__ float asf(unsigned u) { return __builtin_bit_cast(float, u); }
__device__ __forceinline__ unsigned asu(float f) { return __builtin_bit_cast(unsigned, f); }
__device__ __forceinline__ float lo(unsigned u) { return asf(u << 16); }
__device__ __forceinline__ float hi(unsigned u) { return asf(u & 0xffff0000u); }
template <int CTRL> __device__ __forceinline__ float dppadd(float v) { return v + asf(__builtin_amdgcn_update_dpp(0, asu(v), CTRL, 0xf, 0xf, true)); }
__device__ __forceinline__ float rowsum(float v) { return dppadd<0x121>(dppadd<0x122>(dppadd<0x124>(dppadd<0x128>(v)))); }
__device__ __forceinline__ float xsum16(float v) { const auto r = __builtin_amdgcn_permlane16_swap(asu(v), asu(v), false, false); return asf(r[0]) + asf(r[1]); }
__device__ __forceinline__ float xsum32(float v) { const auto r = __builtin_amdgcn_permlane32_swap(asu(v), asu(v), false, false); return asf(r[0]) + asf(r[1]); }
template <int NM, int ND> __device__ __forceinline__ void ilv() {
#pragma unroll
  for (int i = 0; i < NM; i++) { __builtin_amdgcn_sched_group_barrier(0x8, 1, 0); if (i < ND) __builtin_amdgcn_sched_group_barrier(0x100, 1, 0); }
}
__device__ __forceinline__ int piece_col(int T, int p, bool split) { return 16 * T + 4 * p + ((4 * p - 12 * (T & 1)) & ((int)split - 1)); }
__device__ __forceinline__ bool split640(int T) { return (T >> 1) % 5 == 2; }
__device__ __forceinline__ bool split1728(int T) { return (T >> 1) % 7 == 3 && T < 84; }
__device__ __forceinline__ unsigned lds_add(ldsp a) { return __hip_atomic_fetch_add((LDS unsigned*)a, 1u, __ATOMIC_RELAXED, __HIP_MEMORY_SCOPE_WORKGROUP); }
__device__ __forceinline__ void lds_wait(ldsp a, unsigned v) { while (*(volatile LDS unsigned*)a != v) __builtin_amdgcn_s_sleep(1); asm volatile("" ::: "memory"); }
constexpr int SPIN_CAP = 1 << 17;
__device__ __forceinline__ v4u ld16_async(const char* base, unsigned off) { v4u v; asm volatile("global_load_dwordx4 %0, %1, %2 sc1" : "=v"(v) : "v"(off), "s"(base) : "memory"); return v; }
template <int SO> __device__ __forceinline__ void ld4_async(unsigned& d, v4u r, int voff) {
  int t; asm volatile("s_mov_b32 %1, %4\n buffer_load_dword %0, %2, %3, %1 offen sc1" : "=v"(d), "=&s"(t) : "v"(voff), "s"(r), "n"(SO));
}
__device__ __forceinline__ void look_async(unsigned& d, v4u r, int voff) {
  d = 0; asm volatile("buffer_atomic_or %0, %1, %2, 0 offen sc0 sc1" : "+v"(d) : "v"(voff), "s"(r));
}
__device__ __forceinline__ void ld2_async(unsigned& d, v4u r, int voff) { asm volatile("buffer_load_ushort %0, %1, %2, 0 offen" : "=v"(d) : "v"(voff), "s"(r)); }
__device__ __forceinline__ v4u srd(const void* p, unsigned bytes) { return v4u{(unsigned)(size_t)p, (unsigned)((size_t)p >> 32) & 0xffffu, bytes, 0x00020000u}; }
__device__ __forceinline__ void sbar() { __builtin_amdgcn_sched_barrier(0); }
__device__ __forceinline__ unsigned now() { return (unsigned)__builtin_amdgcn_s_memrealtime(); }
template <int D> __device__ __forceinline__ void until(unsigned t0) {
#pragma unroll 1
  for (int it = 0; it < 64 && now() - t0 < D; it++) __builtin_amdgcn_s_sleep(1);
}
__device__ __forceinline__ bool eq4(v4u v, unsigned tag) { return __builtin_amdgcn_ballot_w64(v[0] != tag || v[1] != tag || v[2] != tag || v[3] != tag) == 0; }
__device__ __forceinline__ void poll256(rsrc_t r, int off, unsigned tag) {
#pragma unroll 1
  for (int it = 0; it < SPIN_CAP; it++) { asm volatile("" ::: "memory"); if (eq4(ld16(r, off), tag)) break; __builtin_amdgcn_s_sleep(2); }
  asm volatile("" ::: "memory");
}
template <unsigned long long MASK> __device__ __forceinline__ int pollg(v4u r, int off, unsigned tag) {
  int it;
#pragma unroll 1
  for (it = 0; it < SPIN_CAP; it++) {
    unsigned f; look_async(f, r, off); waitv<0>(); asm volatile("" : "+v"(f));
    if ((__builtin_amdgcn_ballot_w64(f != tag) & MASK) == 0) break;
    __builtin_amdgcn_s_sleep(PSL);
  }
  return it;
}
template <int V> struct IC { static constexpr int v = V; };
template <int A, int B, class F> __device__ __forceinline__ void sfor(F f) { if constexpr (A < B) { f(IC<A>{}); sfor<A + 1, B>(f); } }

constexpr int K1 = 1024, L1S = 26 * K1, L1R = 90 * K1, L3X = 150 * K1;
constexpr int RS = 1312;
constexpr int L0R = 52 * K1, L0W = 130 * K1, L0C = 150 * K1, L0D = 152 * K1;

__device__ __forceinline__ void consume1(ldsp smem, int w, int lane, v4f (&acc)[2][5]) {
  constexpr int MT = 2;
  const int g = lane >> 4, r = lane & 15;
  const int a0 = (w * 32 + r) * 128 + ((g ^ (r >> 1)) * 16), a1 = a0 ^ 64;
  const int b0 = 16 * K1 + lane * 16, b1 = b0 + K1;
  v8s pa[MT], pb[5], qa[MT], qb[5];
#pragma unroll
  for (int i = 0; i < MT; i++) { qa[i] = v8s{0, 0, 0, 0, 0, 0, 0, 0};
#pragma unroll
    for (int j = 0; j < 5; j++) acc[i][j] = v4f{0, 0, 0, 0}; }
#pragma unroll
  for (int j = 0; j < 5; j++) qb[j] = v8s{0, 0, 0, 0, 0, 0, 0, 0};
#define RD(A, B, ao, bo) { B[0] = *(LDS v8s*)(smem + so + bo); _Pragma("unroll") for (int i = 0; i < MT; i++) A[i] = *(LDS v8s*)(smem + so + ao + i * 2048); \
                           _Pragma("unroll") for (int j = 1; j < 5; j++) B[j] = *(LDS v8s*)(smem + so + bo + j * 2048); }
#define MM(A, B) { _Pragma("unroll") for (int j = 0; j < 5; j++) _Pragma("unroll") for (int i = 0; i < MT; i++) acc[i][j] = mfma(B[j], A[i], acc[i][j]); }
#define NEXT { bar(); so += L1S; if (so == 5 * L1S) so = 0; }
  int so = 3 * L1S;
#pragma unroll 1
  for (int s = 0; s < 31; s++) {
    RD(pa, pb, a0, b0); MM(qa, qb); ilv<5 * MT, 5 + MT>(); sbar();
    RD(qa, qb, a1, b1); MM(pa, pb); ilv<5 * MT, 5 + MT>(); sbar();
    NEXT
  }
  RD(pa, pb, a0, b0); MM(qa, qb); ilv<5 * MT, 5 + MT>(); sbar();
  RD(qa, qb, a1, b1); MM(pa, pb); ilv<5 * MT, 5 + MT>();
  sbar();
  MM(qa, qb);
#undef RD
#undef MM
#undef NEXT
}

template <int NJ, int NU, bool PAIR, int BL, int NR, class FB, class FS, class FX, class FH>
__device__ __forceinline__ void consume(ldsp bl0, rsrc_t rw, int va0, int va1, ldsp mine, v4f (&acc)[2][NJ], const v8s (&rb)[NR + 1][NJ], const int (&so)[BL], FB boff, FS sync, FX extra, FH hook) {
  v8s a[UD][2], bq[2][NJ];
  int va[2] = {va0, va1};
  ldsp bl[3] = {bl0, bl0 + 65536, bl0 + 131072};
  asm volatile("" : "+v"(va[0]), "+v"(va[1]), "+v"(bl[0]), "+v"(bl[1]), "+v"(bl[2]));
  bar();
#define LDA(u) _Pragma("unroll") for (int i = 0; i < 2; i++) a[(u) % UD][i] = __builtin_bit_cast(v8s, \
    __builtin_amdgcn_raw_buffer_load_b128(rw, va[i] + (PAIR ? (u) / BL * BL * 2048 : ((u) & 1) * 1024), so[(PAIR ? (u) : (u) >> 1) % BL], SC1));
#define LDB(u) _Pragma("unroll") for (int j = 0; j < NJ; j++) bq[(u) & 1][j] = *(LDS v8s*)(bl[(boff(u) + j * 2048) >> 16] + ((boff(u) + j * 2048) & 0xffff));
  LDA(0) sbar(); if (!NR) { LDB(0) } sbar();
#pragma unroll
  for (int u = 1; u < UD - 1; u++) { LDA(u) sbar(); }
  extra();
  sbar();
  sfor<1, NU + 1>([&](auto uc) {
    constexpr int u = uc.v;
    if (u < NU) { if (sync(u)) bar(); if (u >= NR) { LDB(u) } }
    const bool ld = u - 2 + UD < NU;
    if (ld) { LDA(u - 2 + UD) }
    hook(u);
    if constexpr (PAIR && u == NU) {
#pragma unroll
      for (int j = 0; j < NJ; j++) acc[1][j] = mfma(bq[(u - 1) & 1][j], a[(u - 1) % UD][1], acc[1][j]);
      sbar();
#pragma unroll
      for (int j = 0; j < NJ; j++) {
        acc[0][j] = mfma(bq[(u - 1) & 1][j], a[(u - 1) % UD][0], acc[0][j]);
        *(LDS v4f*)(mine + j * 1024) = acc[1][j];
        sbar();
      }
      return;
    }
#pragma unroll
    for (int j = 0; j < NJ; j++) {
      v8s bf;
      if (u <= NR) bf = rb[u <= NR ? u - 1 : 0][j]; else bf = bq[(u - 1) & 1][j];
#pragma unroll
      for (int i = 0; i < 2; i++) acc[i][j] = mfma(bf, a[(u - 1) % UD][i], u == 1 && (i || !PAIR) ? v4f{0, 0, 0, 0} : acc[i][j]);
    }
#pragma unroll
    for (int k = 0; k < 2 * NJ; k++) {
      __builtin_amdgcn_sched_group_barrier(0x8, 1, 0);
      if (u < NU && u >= NR && (k & 1)) __builtin_amdgcn_sched_group_barrier(0x100, 1, 0);
      if (ld && (k == 2 || k == 6)) __builtin_amdgcn_sched_group_barrier(0x20, 1, 0);
    }
    sbar();
  });
#undef LDA
#undef LDB
}

__device__ __forceinline__ void pair_add(ldsp other, v4f (&acc)[2][5], v4f (&o)[5]) {
#pragma unroll
  for (int j = 0; j < 5; j++) o[j] = *(LDS v4f*)(other + j * 1024);
  sbar();
#pragma unroll
  for (int j = 0; j < 5; j++) o[j] += acc[0][j];
}
#endif

__global__ __launch_bounds__(512, 2) void k_fnet(Params p) {
#if defined(__HIP_DEVICE_COMPILE__)
  extern __shared__ __attribute__((aligned(16))) char smem_[];
  ldsp smem = (ldsp)smem_;
  const int lane = threadIdx.x & 63, w = __builtin_amdgcn_readfirstlane(threadIdx.x >> 6), b = blockIdx.x;
  const int tg = (b & 7) * 2 + (b >> 7), c = (b >> 3) & 15, hf = c >> 3, nt = c & 7, odd = nt & 1;
  const int tg3 = (b & 6) * 2 + (b >> 6), c3 = (b & 1) * 8 + nt;
  const bool seven = c3 < 12;
  const int odd3 = seven ? c3 & 1 : 0, T0 = seven ? 7 * c3 : 84 + 6 * (c3 - 12), Ts = odd3 ? T0 : T0 + 6;
  const int g = lane >> 4;
  const rsrc_t rw = mkrsrc(p.ws, WS_BYTES);
  if (__builtin_expect(w < 4, 1)) {
    unsigned ep = ld4(rw, OFF_E, 0);
    const v2u* xr = (const v2u*)(p.x + (size_t)(b * 8 + w * 2 + (lane >> 5)) * 1280) + (lane & 31);
    v2u xv[5];
#pragma unroll
    for (int i = 0; i < 5; i++) xv[i] = xr[32 * i];
    if (w == 0 && lane < 4) *(LDS unsigned*)(smem + L0C + lane * 4) = 0;
    waitl0(); bar();
    asm volatile("" : "+v"(ep), "+v"(xv[0]), "+v"(xv[1]), "+v"(xv[2]), "+v"(xv[3]), "+v"(xv[4]));
    const unsigned tag = __builtin_amdgcn_readfirstlane(ep) + 1;
    {
      float v[20], s = 0;
#pragma unroll
      for (int i = 0; i < 5; i++) { v[4 * i] = lo(xv[i][0]); v[4 * i + 1] = hi(xv[i][0]); v[4 * i + 2] = lo(xv[i][1]); v[4 * i + 3] = hi(xv[i][1]); }
#pragma unroll
      for (int i = 0; i < 20; i++) s += v[i];
      const float mean = xsum16(rowsum(s)) * (1.f / 640.f);
      float q = 0;
#pragma unroll
      for (int i = 0; i < 20; i++) { v[i] -= mean; q += v[i] * v[i]; }
      const float rs = 1.0f / sqrtf(xsum16(rowsum(q)) * (1.f / 640.f) + 1e-5f);
#pragma unroll
      for (int i = 0; i < 5; i++)
        *(LDS v2u*)(smem + L0R + (w * 2 + (lane >> 5)) * RS + ((lane & 31) + 32 * i) * 8) = v2u{pk(v[4 * i] * rs, v[4 * i + 1] * rs), pk(v[4 * i + 2] * rs, v[4 * i + 3] * rs)};
      if (w == 0 && lane == 0) *(LDS unsigned*)(smem + L0C + 16) = tag;
      waitl0();
      if (lane == 0) lds_add(smem + L0C);
      lds_wait(smem + L0C, 4);
      const int qq = (lane & 15) >> 2, pp = lane & 3;
#pragma unroll
      for (int i = 0; i < 3; i++) {
        const int T = 10 * w + 4 * i + (i < 2 ? g : g & 1);
        ldsp a = smem + L0R + qq * RS + piece_col(T, pp, split640(T)) * 2;
        const v4s v0 = __builtin_amdgcn_ds_read_tr16_b64_v4i16((LDS v4s*)a), v1 = __builtin_amdgcn_ds_read_tr16_b64_v4i16((LDS v4s*)(a + 4 * RS));
        st16(rw, i == 2 && g >= 2 ? OOB : OFF_LNF + T * 65536 + (b >> 2) * 1024 + (b & 3) * 256 + (lane & 15) * 16,
             __builtin_bit_cast(v4u, __builtin_shufflevector(v0, v1, 0, 1, 2, 3, 4, 5, 6, 7)));
      }
      waitv<0>();
      unsigned old = 0;
      if (lane == 0) old = lds_add(smem + L0C + 4);
      if (__builtin_amdgcn_readfirstlane(old) == 3 && lane < 2) st4(rw, lane ? OFF_F0P + b * 4 : OFF_F0 + b * FSTR, tag);
    }
#define PUB(i) st4(rw, lane == 0 ? OFF_FG + (i) * FM + w * FWV : OOB, tag)
    const int kb0 = 5 * (nt >> 1), sp = ((odd * 2 + (g >> 1)) * 16 + (lane & 15)) * 16 + (g & 1) * 8;
    const int kh = w >> 1, tb0 = tg * 8 + hf * 4 + (w & 1) * 2, tb = tb0 + kh, t = tb * 16 + (lane & 15);
    const int cp = (kb0 + odd * 3) * 32 + g * 8, cs = (kb0 + 2) * 32 + odd * 16 + g * 4;
    sbar();
    const v4u x0 = *(const v4u*)(p.x + (size_t)t * 1280 + cp * 2), x1 = *(const v4u*)(p.x + (size_t)t * 1280 + cp * 2 + 64);
    const v2u x2 = *(const v2u*)(p.x + (size_t)t * 1280 + cs * 2);
    sbar();
    {
      v4f acc[2][5];
      bar();
      consume1(smem, w, lane, acc);
      v4f (&o)[2][5] = acc;
      const int tb1 = tg * 8 + w * 2;
      const float sg = hf ? -1.f : 1.f;
#pragma unroll
      for (int mi = 0; mi < 2; mi++) {
        const int dst = OFF_PQF + (tb1 + mi) * 40960 + (hf * 20 + kb0) * 1024;
#pragma unroll
        for (int u = 0; u < 2; u++) {
          const v4f a = o[mi][2 * u] * sg, c2 = o[mi][2 * u + 1] * sg;
          st16(rw, dst + (odd * 3 + u) * 1024 + lane * 16, v4u{pk(a[0], a[1]), pk(a[2], a[3]), pk(c2[0], c2[1]), pk(c2[2], c2[3])});
        }
        const v4f a = o[mi][4] * sg;
        st8(rw, dst + 2048 + sp, v2u{pk(a[0], a[1]), pk(a[2], a[3])});
      }
      bar(); waitv<0>();
      PUB(tg * 16 + c);
    }
    v4u h0, h1, c0, c1; v2u h2, c2;
    int so10[10];
#pragma unroll
    for (int i = 0; i < 10; i++) { so10[i] = i * 2048; asm volatile("" : "+s"(so10[i])); }
    const v8s none5[1][5] = {}, none7[1][7] = {};
#define UNP(a, b) v4f{lo(a), hi(a), lo(b), hi(b)}
    {
      v4f acc[2][5], z[5];
      const int r = lane & 15;
      acc[0][0] = UNP(x0[0], x0[1]); acc[0][1] = UNP(x0[2], x0[3]); acc[0][2] = UNP(x1[0], x1[1]); acc[0][3] = UNP(x1[2], x1[3]); acc[0][4] = UNP(x2[0], x2[1]);
      asm volatile("" : "+v"(acc[0][0]), "+v"(acc[0][1]), "+v"(acc[0][2]), "+v"(acc[0][3]), "+v"(acc[0][4]));
      consume<5, 20, true, 10, 0>(smem + r * 128 + ((kh * 4 + g) ^ (r >> 1)) * 16, rw, OFF_PQF + tb * 40960 + kh * 1024 + lane * 16,
        OFF_PQF + (tb ^ 1) * 40960 + kh * 1024 + lane * 16, smem + (100 + 5 * w) * K1 + lane * 16, acc, none5, so10,
        [](int u) { return (130 + 10 * u) % 160 * K1; }, [](int u) { return u == 5 || u == US2; }, [] {}, [](int) {});
      waitl0(); bar();
      pair_add(smem + (100 + 5 * (w ^ 2)) * K1 + lane * 16, acc, z);
      h0 = v4u{pk(z[0][0], z[0][1]), pk(z[0][2], z[0][3]), pk(z[1][0], z[1][1]), pk(z[1][2], z[1][3])};
      h1 = v4u{pk(z[2][0], z[2][1]), pk(z[2][2], z[2][3]), pk(z[3][0], z[3][1]), pk(z[3][2], z[3][3])};
      h2 = v2u{pk(z[4][0], z[4][1]), pk(z[4][2], z[4][3])};
      const int dst = OFF_HF + tb * 20480 + kb0 * 1024;
      st16(rw, dst + odd * 3072 + lane * 16, h0);
      st16(rw, dst + odd * 3072 + 1024 + lane * 16, h1);
      st8(rw, dst + 2048 + sp, h2);
      const v4f s4 = (z[0] + z[1]) + (z[2] + z[3]) + z[4];
      const float m = xsum32(xsum16((s4[0] + s4[1]) + (s4[2] + s4[3]))) * (1.f / 80.f);
      v4f q4 = {0, 0, 0, 0};
#pragma unroll
      for (int j = 0; j < 5; j++) { const v4f d = z[j] - m; q4 += d * d; }
      const float q = xsum32(xsum16((q4[0] + q4[1]) + (q4[2] + q4[3])));
      st8(rw, lane < 16 ? OFF_ST + nt * 16384 + t * 8 : OOB, v2u{asu(m), asu(q)});
      bar(); waitv<0>();
      PUB(256 + tg * 16 + c);
    }
      v8s rb[RB + 1][5];
      int so9[9];
#pragma unroll
      for (int i = 0; i < 9; i++) { so9[i] = i * 2048; asm volatile("" : "+s"(so9[i])); }
      auto ldrb = [&] {
      sbar();
#pragma unroll
      for (int u = 0; u < RB; u++)
#pragma unroll
        for (int j = 0; j < 5; j++)
          rb[u][j] = __builtin_bit_cast(v8s, __builtin_amdgcn_raw_buffer_load_b128(rw, OFF_W2F + kh * 1024 + lane * 16, (5 * nt + (odd ? (j + 1) % 5 : j)) * 55296 + so9[u], SC1));
      sbar();
      };
    {
      const int k3 = (T0 + odd3) >> 1;
      v4f acc[2][7];
      v8s ea[2], eb[7];
      float st[2];
      consume<7, 20, false, 10, 0>(smem + lane * 16, rw, OFF_HF + (tg3 * 8 + w * 2) * 20480 + lane * 16, OFF_HF + (tg3 * 8 + w * 2 + 1) * 20480 + lane * 16, smem, acc, none7, so10,
        [](int u) { return (10 + 14 * (u >> 1) + (u & 1)) * K1; }, [](int u) { return u == 7 || u == US3 || (TX3 > TX2 && u == US4); }, [&] {
          c0 = *(const v4u*)(p.b2 + cp * 2); c1 = *(const v4u*)(p.b2 + cp * 2 + 64); c2 = *(const v2u*)(p.b2 + cs * 2); },
        [&](int u) {
          if (u == HK3) {
#pragma unroll
            for (int i = 0; i < 2; i++) ea[i] = *(LDS v8s*)(smem + L3X + (g ? 4608 + lane * 16 : 2048 + (w * 32 + lane) * 16) + i * 256);
#pragma unroll
            for (int j = 0; j < 7; j++) eb[j] = *(LDS v8s*)(smem + L3X + (lane & 15) * 16 + j * 256);
          }
          if (u == HK3 + 1) {
#pragma unroll
            for (int j = 0; j < 7; j++)
#pragma unroll
              for (int i = 0; i < 2; i++) acc[i][j] = mfma(eb[j], ea[i], acc[i][j]);
            sbar();
          }
          if (u == 19) { st[0] = *(LDS float*)(smem + L3X + 4096 + (w * 32 + (lane & 15)) * 4); st[1] = *(LDS float*)(smem + L3X + 4160 + (w * 32 + (lane & 15)) * 4); } });
#pragma unroll
      for (int mi = 0; mi < 2; mi++) {
        const int dst = OFF_HH + (tg3 * 8 + w * 2 + mi) * 55296;
        unsigned r[14];
#pragma unroll
        for (int j = 0; j < 7; j++) {
          v4f z = acc[mi][j] * st[mi];
          z = __builtin_elementwise_max(z, v4f{0, 0, 0, 0}); z *= z;
          r[2 * j] = pk(z[0], z[1]); r[2 * j + 1] = pk(z[2], z[3]);
          if (j & 1) st16(rw, dst + (k3 + (j >> 1)) * 1024 + lane * 16, v4u{r[2 * j - 2], r[2 * j - 1], r[2 * j], r[2 * j + 1]});
        }
        st8(rw, seven ? dst + (Ts >> 1) * 1024 + ((odd3 * 2 + (g >> 1)) * 16 + (lane & 15)) * 16 + (g & 1) * 8 : OOB, v2u{r[12], r[13]});
      }
      bar();
      waitv<0>();
      PUB(512 + tg3 * 16 + c3);
    }
    {
      v4f acc[2][5], z[5];
      ldrb();
      acc[0][0] = UNP(h0[0], h0[1]) + UNP(c0[0], c0[1]); acc[0][1] = UNP(h0[2], h0[3]) + UNP(c0[2], c0[3]); acc[0][2] = UNP(h1[0], h1[1]) + UNP(c1[0], c1[1]);
      acc[0][3] = UNP(h1[2], h1[3]) + UNP(c1[2], c1[3]); acc[0][4] = UNP(h2[0], h2[1]) + UNP(c2[0], c2[1]);
      asm volatile("" : "+v"(acc[0][0]), "+v"(acc[0][1]), "+v"(acc[0][2]), "+v"(acc[0][3]), "+v"(acc[0][4]));
      consume<5, 27, true, 9, RB>(smem + kh * 1024 + lane * 16, rw, OFF_HH + tb * 55296 + kh * 1024 + lane * 16, OFF_HH + (tb ^ 1) * 55296 + kh * 1024 + lane * 16,
        smem + (120 + 5 * w) * K1 + lane * 16, acc, rb, so9,
        [](int u) { return (u + 15 - RB) % 15 * 10 * K1; }, [](int u) { return u == 5 + RB || u == 9 + RB || u == 15 + RB || (NS4 < 8 && u == 19 + RB) || u == 23 + RB; }, [] {}, [](int) {});
      waitl0(); bar();
      pair_add(smem + (120 + 5 * (w ^ 2)) * K1 + lane * 16, acc, z);
      char* dst = p.out + (size_t)t * 1280;
      *(v4u*)(dst + cp * 2) = v4u{pk(z[0][0], z[0][1]), pk(z[0][2], z[0][3]), pk(z[1][0], z[1][1]), pk(z[1][2], z[1][3])};
      *(v4u*)(dst + cp * 2 + 64) = v4u{pk(z[2][0], z[2][1]), pk(z[2][2], z[2][3]), pk(z[3][0], z[3][1]), pk(z[3][2], z[3][3])};
      *(v2u*)(dst + cs * 2) = v2u{pk(z[4][0], z[4][1]), pk(z[4][2], z[4][3])};
    }
  } else if (w < 6) {
    const int lw = w - 4;
    int vt[5];
#pragma unroll
    for (int i = 0; i < 5; i++) { const int j = (lw * 5 + i) >> 1; vt[i] = 5 * nt + (odd ? (j + 1) % 5 : j); }
    unsigned tag, t0;
    v4u sg[10][5];
    int vd[5];
#pragma unroll
    for (int i = 0; i < 5; i++) {
      const int row = (lw * 5 + i) * 8 + (lane >> 3), j = row >> 4, T = 5 * nt + (odd ? (j + 1) % 5 : j), n = row & 15;
      vd[i] = (piece_col(T, n >> 2, j == 4) + (n & 3)) * 1280 + ((lane & 7) ^ ((row >> 1) & 7)) * 16;
    }
    auto e2 = [&](int s0, int s1) {
#pragma unroll 1
      for (int s = s0; s < s1; s++) {
        const rsrc_t r = mkrsrc(s < 10 ? p.dr : p.di - 1280, D_ * D_ * 2 + (s < 10 ? 0 : 1280));
        ldsp d = smem + ((s + 13) & 15) * 10240 + lw * 5120;
        const int q = s;
#pragma unroll
        for (int i = 0; i < 5; i++) dma<0>(r, d + i * 1024, vd[i], (q < (s < 10 ? 10 : 20) ? q : q - 10) * 128);
      }
    };
    auto stage2 = [&](int k0, int k1) {
      sbar();
#pragma unroll
      for (int k = k0; k < k1; k++)
#pragma unroll
        for (int i = 0; i < 5; i++) sg[k][i] = __builtin_amdgcn_raw_buffer_load_b128(mkrsrc(p.di, D_ * D_ * 2), vd[i], (6 + k) % 10 * 128, 0);
      sbar();
    };
    {
      const rsrc_t ra = mkrsrc((hf ? p.ci : p.cr) + (size_t)tg * 128 * 4096, 128 * 4096);
      int va[8], vb[5];
#pragma unroll
      for (int i = 0; i < 8; i++) { const int row = (lw * 8 + i) * 8 + (lane >> 3); va[i] = row * 4096 + ((lane & 7) ^ ((row >> 1) & 7)) * 16; }
#pragma unroll
      for (int i = 0; i < 5; i++) vb[i] = OFF_LNF + vt[i] * 65536 + ((lw + i) & 1) * 1024 + lane * 16;
      auto ks = [&](int s) { return ((s & ~3) + ((s + (b >> 6)) & 3) + 4 * nt) & 31; };
      auto l1 = [&](int s, int so) {
#pragma unroll
        for (int i = 0; i < 5; i++) dma<SC1>(rw, smem + so + (16 + lw * 5 + i) * 1024, vb[i], ks(s) * 2048);
      };
      auto e1 = [&](int s, int so, bool wa) {
        if (wa) {
#pragma unroll
          for (int i = 0; i < 8; i++) dma<0>(ra, smem + so + (lw * 8 + i) * 1024, va[i], ks(s) * 128);
        }
        l1(s, so);
      };
      const int vf0 = lane < 32 ? OFF_F0 + (32 * nt + lane) * FSTR : OOB;
      bar();
#pragma unroll
      for (int s = 0; s < NPRE; s++)
#pragma unroll
        for (int i = 0; i < 8; i++) {
          dma<0>(ra, smem + (s + 3) % 5 * L1S + (lw * 8 + i) * 1024, va[i], ks(s) * 128);
          if (s * 8 + i >= CTH - 1) waitv<CTH - 1>();
        }
      lds_wait(smem + L0C, 4);
      tag = __builtin_amdgcn_readfirstlane(*(volatile LDS unsigned*)(smem + L0C + 16));
      pollg<0xffffffffull>(srd(p.ws, WS_BYTES), vf0, tag);
      constexpr int N2 = NPRE > 2 ? 5 : 13, N3 = NPRE > 3 ? 5 : 13;
      l1(0, 3 * L1S);
      v4u fp = ld16_async(p.ws, OFF_F0P + lane * 16);
      l1(1, 4 * L1S); e1(2, 0, NPRE < 3);
      waitv<6 + N2>(); bar();
      e1(3, L1S, NPRE < 4);
      waitv<N2 + N3>();
      asm volatile("" : "+v"(fp));
      if (lw == 0) {
        if (!eq4(fp, tag)) poll256(rw, OFF_F0P + lane * 16, tag);
        if (b == 0 && lane == 0) st4(rw, OFF_E, tag);
      }
      bar();
      e1(4, 2 * L1S, true); waitv<N3 + 13>(); bar();
      int so = 3 * L1S;
#pragma unroll 1
      for (int s = 5; s < 32; s++) {
        e1(s, so, true); so += L1S; if (so == 5 * L1S) so = 0;
        waitv<26>(); bar();
      }
      e2(0, 5); waitv<38>(); bar();
      e2(5, TD < 8 ? TD : 8); waitv<5 * (TD < 8 ? TD : 8)>(); bar();
      e2(8, TD);
      bar();
      t0 = now();
      e2(TD, 16);
      stage2(0, NS2 - KB1);
    }
    auto put = [&](int k0, int k1, int slot) {
#pragma unroll
      for (int k = k0; k < k1; k++)
#pragma unroll
        for (int i = 0; i < 5; i++) *(LDS v4u*)(smem + (slot + k - k0) % 16 * 10240 + lw * 5120 + i * 1024 + lane * 16) = sg[k][i];
      waitl0();
    };
    int vb3[7];
#pragma unroll
    for (int i = 0; i < 7; i++) { const int f = lw * 7 + i, j = f >> 1, T = j < 6 ? T0 + odd3 + j : Ts;
      vb3[i] = (j < 6 || seven) ? OFF_W1F + T * 20480 + (f & 1) * 1024 + lane * 16 : OOB; }
    auto e3 = [&](int s0, int s1) {
#pragma unroll 1
      for (int s = s0; s < s1; s++) {
        ldsp d = smem + (10 + 14 * s + lw * 7) * K1;
#pragma unroll
        for (int i = 0; i < 7; i++) dma<SC1>(rw, d + i * 1024, vb3[i], s * 2048);
      }
    };
    const int fl = OFF_FG + (lane >> 2) * FM + (lane & 3) * FWV, i2 = lw * 64 + lane;
#define LOOK(D, N, grp, more) { unsigned f; until<D>(t0); look_async(f, srd(p.ws, WS_BYTES), lw ? OOB : fl + (grp) * 16 * FM); more; waitv<N>(); \
      asm volatile("" : "+v"(f)); if (lw == 0 && __builtin_amdgcn_ballot_w64(f != tag)) { pollg<~0ull>(srd(p.ws, WS_BYTES), fl + (grp) * 16 * FM, tag); } }
    {
      LOOK(PD1, 5 * KB1, tg, stage2(NS2 - KB1, NS2))
      bar();
      v4u fp = ld16_async(p.ws, OFF_FWP + lane * 16);
      bar();
      if (NS2) put(0, 4, 13); else e2(16, 20);
      waitv<0>();
      asm volatile("" : "+v"(fp));
      if (lw == 0 && !eq4(fp, tag)) poll256(rw, OFF_FWP + lane * 16, tag);
      bar();
      e3(0, TW);
      const int j = i2 >> 4, T = j < 6 ? T0 + odd3 + j : Ts;
      const bool on = i2 < (seven ? 112 : 96);
      const int vc = on ? OFF_CS + (T * 16 + (i2 & 15)) * 4 : OOB, vbias = on ? (piece_col(T, (i2 & 15) >> 2, split1728(T)) + (i2 & 3)) * 2 : OOB;
      unsigned cpart[20], bb;
      bar();
      ld2_async(bb, srd(p.b1, F_ * 2), vbias);
      sfor<0, 20>([&](auto k) { ld4_async<k.v * F_ * 4>(cpart[k.v], srd(p.ws, WS_BYTES), vc); });
      bar();
      t0 = now();
      e3(TW, K2);
      waitv<7 * (K2 - TW)>();
      float cq[4] = {0, 0, 0, 0};
#pragma unroll
      for (int k = 0; k < 20; k++) { asm volatile("" : "+v"(cpart[k])); cq[k & 3] += asf(cpart[k]); }
      asm volatile("" : "+v"(bb));
      const float csum = (cq[0] + cq[1]) + (cq[2] + cq[3]);
      const unsigned c1 = pk(csum, csum), c2 = pk(csum - lo(c1), csum - lo(c1)), c3 = pk(0, csum - lo(c1) - lo(c2));
      const v4u fb = {(c1 & 0xffff) | (c2 & 0xffff0000u), (c1 & 0xffff) | c3, (c2 & 0xffff) | (c1 & 0xffff0000u), bb * 0x10001u};
      asm volatile("ds_write_b128 %0, %1\n ds_write_b128 %0, %2 offset:4608" ::"v"((unsigned)(size_t)(smem + L3X + i2 * 16)), "v"(fb), "v"(v4u{0, 0, 0, 0}) : "memory");
    }
    int vb4[5];
#pragma unroll
    for (int i = 0; i < 5; i++) vb4[i] = OFF_W2F + vt[i] * 55296 + ((lw + i) & 1) * 1024 + lane * 16;
    auto e4 = [&](int s0, int s1) {
#pragma unroll 1
      for (int s = s0; s < s1; s++) {
        ldsp d = smem + (s >= 15 ? s - 15 : s) * 10240 + lw * 5120;
        const int u = s + RB, q = u, e = u < 9 ? 9 : u < 18 ? 18 : 27;
#pragma unroll
        for (int i = 0; i < 5; i++) dma<SC1>(rw, d + i * 1024, vb4[i], (q < e ? q : q - 9) * 2048);
      }
    };
    auto stage4 = [&](int k0, int k1) {
      sbar();
#pragma unroll
      for (int k = k0; k < k1; k++) {
        const int u = 15 + k + RB, q = u, so = (q < (u < 18 ? 18 : 27) ? q : q - 9) * 2048;
#pragma unroll
        for (int i = 0; i < 5; i++) sg[k][i] = __builtin_amdgcn_raw_buffer_load_b128(rw, vb4[i], so, SC1);
      }
      sbar();
    };
    {
      LOOK(PD2, 7 * (10 - K2), 16 + tg3, e3(K2, 10))
      bar();
      v2u sv[8];
#pragma unroll
      for (int k = 0; k < 8; k++) sv[k] = ld8(rw, OFF_ST + (tg3 * 128 + i2) * 8, k * 16384);
      __builtin_amdgcn_s_setprio(LP1);
#pragma unroll
      for (int k = 0; k < 8; k++) asm volatile("" : "+v"(sv[k]));
      const float m = (((asf(sv[0][0]) + asf(sv[1][0])) + (asf(sv[2][0]) + asf(sv[3][0]))) + ((asf(sv[4][0]) + asf(sv[5][0])) + (asf(sv[6][0]) + asf(sv[7][0])))) * 0.125f;
      float q[2] = {0, 0}, dq[2] = {0, 0};
#pragma unroll
      for (int k = 0; k < 8; k++) { const float d = asf(sv[k][0]) - m; dq[k & 1] += d * d; q[k & 1] += asf(sv[k][1]); }
      const float var = (q[0] + q[1] + 80.f * (dq[0] + dq[1])) * (1.f / 640.f) + 1e-5f, rs = __builtin_amdgcn_rsqf(var), sd = var * rs;
      const unsigned a1 = pk(-m, -m), as1 = pk(-m - lo(a1), sd), as2 = pk(-m - lo(a1) - lo(as1), sd - hi(as1));
      const v4u fa = {a1, (as1 & 0xffff) | (a1 & 0xffff0000u), (as1 & 0xffff) | as2 << 16, as1 >> 16 | (as2 & 0xffff0000u)};
      asm volatile("ds_write_b128 %0, %1 offset:2048\n ds_write_b32 %2, %3\n s_waitcnt lgkmcnt(0)"
                   ::"v"((unsigned)(size_t)(smem + L3X + i2 * 16)), "v"(fa), "v"((unsigned)(size_t)(smem + L3X + 4096 + i2 * 4)), "v"(rs) : "memory");
      __builtin_amdgcn_s_setprio(0);
      bar();
      e4(0, TX1);
      bar();
      e4(TX1, TX2);
      bar();
      if (TX3 > TX2) { e4(TX2, TX3); bar(); }
      t0 = now();
      e4(TX3, 15);
      stage4(0, NS4 - KB3);
    }
    {
      LOOK(PD3, 5 * KB3, 32 + tg, stage4(NS4 - KB3, NS4))
      bar();
      bar();
      if (NS4) put(0, 4, 0); else e4(15, 19);
      bar();
      if (NS4 >= 8) {
        put(4, 8, 4); bar();
        if (RB < 4) { put(8, NS4, 8); e4(15 + NS4, 27 - RB); waitv<0>(); bar(); }
      } else {
        e4(19, 23); waitv<20>(); bar();
        e4(23, 27 - RB); waitv<5 * (4 - RB)>(); bar();
        if (RB < 4) { waitv<0>(); bar(); }
      }
    }
  } else {
    const int hw = w - 6, nu = hw == 0 && b < 56 ? 3 : 2;
    if (hw == 0) {
      unsigned long long pc; unsigned back;
      asm volatile("s_getpc_b64 %0\n.Lfnet_pc:\n s_mov_b32 %1, .Lfnet_pc-_Z6k_fnet6Params" : "=s"(pc), "=s"(back));
      dma<0>(mkrsrc((const void*)(pc - back), CODELEN), smem + L0D, (b >> 3) * 1024 + lane * 16, 0);
    }
    auto touch = [&] {
#pragma unroll
    for (int k = 0; k < 4; k++) {
      const int q = b + 256 * (hw * 3 + k);
      const rsrc_t r = hw || k < 3 ? mkrsrc(q < 800 ? p.dr + q * 1024 : p.di + (q - 800) * 1024, q < 1600 ? 1024 : 0)
                                   : mkrsrc(b & 32 ? p.b2 : p.b1, b & 32 ? D_ * 2 : F_ * 2);
      dma<0>(r, smem + L0D, lane * 16 + (hw || k < 3 ? 0 : (b >> 3 & 3) * 1024), 0);
    }
    };
    bar();
#pragma unroll 1
    for (int n = 0; n < nu; n++) {
      const int i = n < 2 ? hw * 2 + n : 4, u = b + 256 * i;
      const bool isw1 = u < 540;
      const int uu = isw1 ? u : u - 540, kb = isw1 ? uu / 27 : uu / 10, cu = isw1 ? uu % 27 : uu % 10, ld = isw1 ? F_ * 2 : D_ * 2;
      const rsrc_t r = mkrsrc(isw1 ? p.w1 : p.w2, D_ * F_ * 2);
#pragma unroll
      for (int k = 0; k < 4; k++) dma<0>(r, smem + L0W + i * 4096 + k * 1024, (kb * 32 + k * 8 + (lane >> 3)) * ld + cu * 128 + (lane & 7) * 16, 0);
    }
    waitv<0>();
    lds_wait(smem + L0C + 4, 4);
    const int qq = (lane & 15) >> 2, pp = lane & 3;
#pragma unroll 1
    for (int n = 0; n < nu; n++) {
      const int i = n < 2 ? hw * 2 + n : 4, u = b + 256 * i;
      const bool isw1 = u < 540;
      const int uu = isw1 ? u : u - 540, kb = isw1 ? uu / 27 : uu / 10, cu = isw1 ? uu % 27 : uu % 10;
      unsigned a[4];
#pragma unroll
      for (int t = 0; t < 4; t++) { const int T = cu * 4 + t;
        a[t] = (unsigned)(size_t)(smem + L0W + i * 4096 + (g * 8 + qq) * 128 + (piece_col(T, pp, isw1 ? split1728(T) : split640(T)) - cu * 64) * 2); }
      v2u v[8];
      asm volatile("ds_read_b64_tr_b16 %0, %8\n ds_read_b64_tr_b16 %1, %8 offset:512\n ds_read_b64_tr_b16 %2, %9\n ds_read_b64_tr_b16 %3, %9 offset:512\n"
                   "ds_read_b64_tr_b16 %4, %10\n ds_read_b64_tr_b16 %5, %10 offset:512\n ds_read_b64_tr_b16 %6, %11\n ds_read_b64_tr_b16 %7, %11 offset:512\n s_waitcnt lgkmcnt(0)"
                   : "=&v"(v[0]), "=&v"(v[1]), "=&v"(v[2]), "=&v"(v[3]), "=&v"(v[4]), "=&v"(v[5]), "=&v"(v[6]), "=&v"(v[7]) : "v"(a[0]), "v"(a[1]), "v"(a[2]), "v"(a[3]) : "memory");
      const int dst = (isw1 ? OFF_W1F + cu * 4 * 20480 : OFF_W2F + cu * 4 * 55296) + kb * 1024 + lane * 16;
#pragma unroll
      for (int t = 0; t < 4; t++) st16(rw, dst + t * (isw1 ? 20480 : 55296), v4u{v[2 * t][0], v[2 * t][1], v[2 * t + 1][0], v[2 * t + 1][1]});
      if (isw1) {
        float cs = 0;
#pragma unroll
        for (int t = 0; t < 4; t++) {
          const v2u v0 = v[2 * t], v1 = v[2 * t + 1];
          const float c1 = xsum32(xsum16((lo(v0[0]) + hi(v0[0])) + (lo(v0[1]) + hi(v0[1])) + (lo(v1[0]) + hi(v1[0])) + (lo(v1[1]) + hi(v1[1]))));
          cs = g == t ? c1 : cs;
        }
        st4(rw, OFF_CS + (kb * F_ + cu * 64 + lane) * 4, asu(cs));
      }
    }
    touch();
    waitv<4>();
    const unsigned tag = *(volatile LDS unsigned*)(smem + L0C + 16);
    unsigned old = 0;
    if (lane == 0) old = lds_add(smem + L0C + 8);
    if (__builtin_amdgcn_readfirstlane(old) == 1 && lane == 0) st4(rw, OFF_FWP + b * 4, tag);
    bar(); bar(); bar();
    waitv<0>();
  }
#endif
}

extern "C" void kernel_launch(void* const* d_in, const int* in_sizes, int n_in, void* d_out, int out_size, void* d_ws, size_t ws_size,
                              hipStream_t stream) {
  Params p;
  p.x = (const char*)d_in[0]; p.cr = (const char*)d_in[1]; p.ci = (const char*)d_in[2]; p.dr = (const char*)d_in[3]; p.di = (const char*)d_in[4];
  p.w1 = (const char*)d_in[6]; p.b1 = (const char*)d_in[7]; p.w2 = (const char*)d_in[8]; p.b2 = (const char*)d_in[9];
  p.out = (char*)d_out; p.ws = (char*)d_ws;
  (void)hipFuncSetAttribute((const void*)k_fnet, hipFuncAttributeMaxDynamicSharedMemorySize, LDS_BYTES);
  k_fnet<<<256, 512, LDS_BYTES, stream>>>(p);
}
```
